```python
import torch

def _softmax(x, axis=-1):
    return torch.softmax(x, dim=axis)
def _gelu(x):
    return torch.nn.functional.gelu(x, approximate="tanh")
def _silu(x):
    return torch.nn.functional.silu(x)
def _sigmoid(x):
    return torch.sigmoid(x)
def _layernorm(x, axis=-1, eps=1e-5):
    m = x.mean(dim=axis, keepdim=True)
    v = x.var(dim=axis, keepdim=True, unbiased=False)
    return (x - m) / torch.sqrt(v + eps)
def _scatter_add(tgt, idx, src, axis=0):
    return tgt.clone().index_add_(axis, idx, src)
def _segment_sum(src, seg, n):
    out = torch.zeros((n,) + src.shape[1:], dtype=src.dtype)
    return out.index_add_(0, seg, src)
def _pad(x, pw):
    flat = tuple(v for pair in reversed(pw) for v in pair)
    return torch.nn.functional.pad(x, flat)

kernel_name = 'sequential_preLN__sliding_block_cached__glu_gated_swiglu__decode'

def setup_inputs():

    _w = (lambda a: (lambda t: t.to(torch.bfloat16) if t.is_floating_point() else t)(torch.as_tensor(a)))
    def _A_setup():
        B, Sq, D = 4, 1, 640
        Hq, Hkv, Sk, Dh = 32, 32, 8192, 128
        assert Hq % Hkv == 0, (Hq, Hkv)
        return {
            "x": _w((lambda *s: torch.randn(*s, dtype=torch.bfloat16) * 0.05)(B, Sq, D)),
            "k_cache": _w((lambda *s: torch.randn(*s, dtype=torch.bfloat16) * 1.6)(B, Hkv, Sk, Dh)),
            "v_cache": _w((lambda *s: torch.randn(*s, dtype=torch.bfloat16) * 0.8)(B, Hkv, Sk, Dh)),
            "wq": _w((lambda *s: torch.randn(*s, dtype=torch.bfloat16) * 1.6)(D, Hq * Dh) * (D ** -0.5)),
            "wo": _w((lambda *s: torch.randn(*s, dtype=torch.bfloat16) * 0.8)(Hq * Dh, D) * ((Hq * Dh) ** -0.5)),
        }
    _A_in = _A_setup()

    def _B_setup():
        B, S, D, F = 4, 1, 640, 1728
        return {
            "x": _w((lambda *s: torch.randn(*s, dtype=torch.bfloat16) * 0.05)(B, S, D)),
            "w1": _w((lambda *s: torch.randn(*s, dtype=torch.bfloat16) * 0.4)(D, F) * (2.0 / D) ** 0.5),
            "w3": _w((lambda *s: torch.randn(*s, dtype=torch.bfloat16) * 0.4)(D, F) * (2.0 / D) ** 0.5),
            "w2": _w((lambda *s: torch.randn(*s, dtype=torch.bfloat16) * 0.4)(F, D) * (2.0 / F) ** 0.5),
        }
    _B_in = _B_setup()

    return {
        "A_x": _A_in["x"],
        "A_k_cache": _A_in["k_cache"],
        "A_v_cache": _A_in["v_cache"],
        "A_wq": _A_in["wq"],
        "A_wo": _A_in["wo"],
        "B_x": _B_in["x"],
        "B_w1": _B_in["w1"],
        "B_w3": _B_in["w3"],
        "B_w2": _B_in["w2"],
    }

def reference(A_x, A_k_cache, A_v_cache, A_wq, A_wo, B_x, B_w1, B_w3, B_w2):
    def _A_ref(x, k_cache, v_cache, wq, wo):
        B, Sq, D = x.shape
        Hq, Dh = 32, 128
        Hkv, Sk = k_cache.shape[1], k_cache.shape[2]
        W = min(4096, max(256, Sk // 4))
        rep = Hq // Hkv
        q = (lambda x, *d: x.permute(*d))((x @ wq).reshape(B, Sq, Hq, Dh), 0, 2, 1, 3)
        k = (lambda x, n, axis: torch.repeat_interleave(x, n, dim=axis))(k_cache[:, :, Sk - W:, :], rep, axis=1)
        v = (lambda x, n, axis: torch.repeat_interleave(x, n, dim=axis))(v_cache[:, :, Sk - W:, :], rep, axis=1)
        scores = torch.einsum("bhqd,bhkd->bhqk", q, k) * (Dh ** -0.5)
        p = _softmax(scores, axis=-1)
        o = torch.einsum("bhqk,bhkd->bhqd", p, v)
        return (lambda x, *d: x.permute(*d))(o, 0, 2, 1, 3).reshape(B, Sq, Hq * Dh) @ wo

    def _B_ref(x, w1, w3, w2):
        gate = torch.einsum("bsd,df->bsf", x, w1)
        up = torch.einsum("bsd,df->bsf", x, w3)
        h = _silu(gate) * up
        out = torch.einsum("bsf,fd->bsd", h, w2)
        return out

    x = A_x
    h = x + _A_ref(_layernorm(x, axis=-1), A_k_cache, A_v_cache, A_wq, A_wo)
    h2 = h + _B_ref(_layernorm(h, axis=-1), B_w1, B_w3, B_w2)
    return h2
```

```cpp
#include <hip/hip_runtime.h>
#include <hip/hip_bf16.h>
typedef unsigned short u16;
__device__ __forceinline__ float bf2f(u16 b){ return __uint_as_float(((unsigned)b)<<16); }
__device__ __forceinline__ u16 f2bf(float f){ unsigned u=__float_as_uint(f); u += 0x7fffu + ((u>>16)&1u); return (u16)(u>>16); }
constexpr int B=4, D=640, H=32, SK=8192, DH=128, W=2048, F=1728;
__device__ float block_sum(float v, float* red){
  for(int o=32;o>0;o>>=1) v+=__shfl_xor(v,o);
  int w=threadIdx.x>>6; __syncthreads(); if((threadIdx.x&63)==0) red[w]=v; __syncthreads();
  float s=0; for(int i=0;i<(int)(blockDim.x>>6);++i) s+=red[i]; return s; }
__device__ float block_max(float v, float* red){
  for(int o=32;o>0;o>>=1) v=fmaxf(v,__shfl_xor(v,o));
  int w=threadIdx.x>>6; __syncthreads(); if((threadIdx.x&63)==0) red[w]=v; __syncthreads();
  float s=-INFINITY; for(int i=0;i<(int)(blockDim.x>>6);++i) s=fmaxf(s,red[i]); return s; }
__device__ void ln_row(const float* xin  , float* ln, float* red){
  float s=0; for(int i=threadIdx.x;i<D;i+=blockDim.x) s+=xin[i];
  float m=block_sum(s,red)/D; float v=0; for(int i=threadIdx.x;i<D;i+=blockDim.x){ float d=xin[i]-m; v+=d*d; }
  v=block_sum(v,red)/D; float r=1.0f/sqrtf(v+1e-5f);
  for(int i=threadIdx.x;i<D;i+=blockDim.x) ln[i]=bf2f(f2bf((xin[i]-m)*r));
  __syncthreads(); }
__global__ void k_q(const u16* x,const u16* wq,float* q){
  __shared__ float xs[D], ln[D], red[16];
  int h=blockIdx.x,b=blockIdx.y;
  for(int i=threadIdx.x;i<D;i+=blockDim.x) xs[i]=bf2f(x[b*D+i]);
  __syncthreads(); ln_row(xs,ln,red);
  int c=h*DH+threadIdx.x; float a=0; for(int j=0;j<D;++j) a+=ln[j]*bf2f(wq[(size_t)j*H*DH+c]);
  q[b*H*DH+c]=bf2f(f2bf(a)); }
__global__ void k_attn(const float* q,const u16* K,const u16* V,float* o){
  __shared__ float qs[DH], p[W], red[16], oacc[2][DH];
  int h=blockIdx.x,b=blockIdx.y;
  if(threadIdx.x<DH) qs[threadIdx.x]=q[b*H*DH+h*DH+threadIdx.x];
  __syncthreads();
  const u16* Kb=K+((size_t)(b*H+h)*SK+(SK-W))*DH; const u16* Vb=V+((size_t)(b*H+h)*SK+(SK-W))*DH;
  float mx=-INFINITY;
  for(int k=threadIdx.x;k<W;k+=blockDim.x){ float a=0; for(int d=0;d<DH;++d) a+=qs[d]*bf2f(Kb[(size_t)k*DH+d]); a*=0.08838834764831845f; p[k]=a; mx=fmaxf(mx,a); }
  mx=block_max(mx,red); float s=0;
  for(int k=threadIdx.x;k<W;k+=blockDim.x){ float e=__expf(p[k]-mx); p[k]=e; s+=e; }
  s=block_sum(s,red);
  int d=threadIdx.x&127, half=threadIdx.x>>7; float a=0;
  for(int k=half;k<W;k+=2) a+=p[k]*bf2f(Vb[(size_t)k*DH+d]);
  oacc[half][d]=a; __syncthreads();
  if(threadIdx.x<DH) o[b*H*DH+h*DH+threadIdx.x]=bf2f(f2bf((oacc[0][threadIdx.x]+oacc[1][threadIdx.x])/s)); }
__global__ void k_o(const float* o,const u16* wo,const u16* x,float* h){
  int c=blockIdx.x*64+(threadIdx.x&63), b=blockIdx.y, part=threadIdx.x>>6; __shared__ float red[4][64];
  float a=0; for(int k=part;k<H*DH;k+=4) a+=o[b*H*DH+k]*bf2f(wo[(size_t)k*D+c]);
  red[part][threadIdx.x&63]=a; __syncthreads();
  if(part==0){ float t=red[0][threadIdx.x]+red[1][threadIdx.x]+red[2][threadIdx.x]+red[3][threadIdx.x]; h[b*D+c]=bf2f(f2bf(bf2f(x[b*D+c])+bf2f(f2bf(t)))); } }
__global__ void k_f1(const float* h,const u16* w1,const u16* w3,float* hh){
  __shared__ float xs[D], ln[D], red[16];
  int b=blockIdx.y;
  for(int i=threadIdx.x;i<D;i+=blockDim.x) xs[i]=h[b*D+i];
  __syncthreads(); ln_row(xs,ln,red);
  int f=blockIdx.x*blockDim.x+threadIdx.x; float g=0,u=0;
  for(int j=0;j<D;++j){ g+=ln[j]*bf2f(w1[(size_t)j*F+f]); u+=ln[j]*bf2f(w3[(size_t)j*F+f]); }
  g=bf2f(f2bf(g)); u=bf2f(f2bf(u)); float s=bf2f(f2bf(g/(1.0f+__expf(-g))));
  hh[b*F+f]=bf2f(f2bf(s*u)); }
__global__ void k_f2(const float* hh,const u16* w2,const float* h,u16* out){
  int c=blockIdx.x*64+(threadIdx.x&63), b=blockIdx.y, part=threadIdx.x>>6; __shared__ float red[4][64];
  float a=0; for(int k=part;k<F;k+=4) a+=hh[b*F+k]*bf2f(w2[(size_t)k*D+c]);
  red[part][threadIdx.x&63]=a; __syncthreads();
  if(part==0){ float t=red[0][threadIdx.x]+red[1][threadIdx.x]+red[2][threadIdx.x]+red[3][threadIdx.x]; out[b*D+c]=f2bf(h[b*D+c]+bf2f(f2bf(t))); } }
extern "C" void kernel_launch(void* const* d_in, const int* in_sizes, int n_in, void* d_out, int out_size, void* d_ws, size_t ws_size, hipStream_t stream) {
  const u16* x=(const u16*)d_in[0]; const u16* K=(const u16*)d_in[1]; const u16* V=(const u16*)d_in[2];
  const u16* wq=(const u16*)d_in[3]; const u16* wo=(const u16*)d_in[4]; const u16* w1=(const u16*)d_in[6]; const u16* w3=(const u16*)d_in[7]; const u16* w2=(const u16*)d_in[8];
  float* ws=(float*)d_ws; float* q=ws; float* o=ws+16384; float* h=ws+32768; float* hh=ws+40960;
  k_q<<<dim3(H,B),128,0,stream>>>(x,wq,q);
  k_attn<<<dim3(H,B),256,0,stream>>>(q,K,V,o);
  k_o<<<dim3(D/64,B),256,0,stream>>>(o,wo,x,h);
  k_f1<<<dim3(F/64,B),64,0,stream>>>(h,w1,w3,hh);
  k_f2<<<dim3(D/64,B),256,0,stream>>>(hh,w2,h,(u16*)d_out);
}
```

```cpp
#include <hip/hip_runtime.h>
#include <stdint.h>

typedef unsigned short u16;
typedef unsigned u32;
typedef unsigned long long u64;
typedef u32 v4u __attribute__((ext_vector_type(4)));
typedef u32 v2u __attribute__((ext_vector_type(2)));
typedef float f4 __attribute__((ext_vector_type(4)));
typedef float f2 __attribute__((ext_vector_type(2)));
typedef __bf16 bf2 __attribute__((ext_vector_type(2)));
typedef __bf16 bf8 __attribute__((ext_vector_type(8)));
#define GAS __attribute__((address_space(1)))
#define LAS __attribute__((address_space(3)))
#define RLX_AGENT __ATOMIC_RELAXED, __HIP_MEMORY_SCOPE_AGENT

#define RD 24
#define STAMPS 0

namespace {
constexpr int DM = 640, NH = 32, DH = 128, SK = 8192, WIN = 2048, FF = 1728;
constexpr int NTHR = 512;
constexpr int AUX_NT = 2, AUX_SC1 = 16, RSRC_W3 = 0x00020000;
constexpr int OFF_X1 = 4096;
constexpr int X1_STRIDE = 136 * 8;
constexpr int OFF_X2 = 0x80000;
constexpr int OFF_X3 = 0x130000;
constexpr int OFF_X4 = 0x140000;
constexpr int NFU = 54;
constexpr int WS_USED = 0x280000;
constexpr size_t OFF_STAMP = 8u << 20;
constexpr u32 SPIN_CAP = 1u << 16;
constexpr float LOG2E = 1.4426950408889634f;

struct Params {
  const u16 *x, *K, *V, *wq, *wo, *w1, *w3, *w2;
  u16* out;
  unsigned char* ws;
};

__device__ __forceinline__ float rb(float f) { return (float)(__bf16)f; }
__device__ __forceinline__ u32 pack2(float lo, float hi) { bf2 v = {(__bf16)lo, (__bf16)hi}; return __builtin_bit_cast(u32, v); }
__device__ __forceinline__ float blo(u32 v) { return __uint_as_float(v << 16); }
__device__ __forceinline__ float bhi(u32 v) { return __uint_as_float(v & 0xffff0000u); }
__device__ __forceinline__ float dot2(u32 a, u32 b, float c) {
  return __builtin_amdgcn_fdot2_f32_bf16(__builtin_bit_cast(bf2, a), __builtin_bit_cast(bf2, b), c, false);
}
__device__ __forceinline__ f4 mfma(v4u a, v4u b, f4 c) {
  return __builtin_amdgcn_mfma_f32_16x16x32_bf16(__builtin_bit_cast(bf8, a), __builtin_bit_cast(bf8, b), c, 0, 0, 0);
}
template <int CTRL> __device__ __forceinline__ float dppf(float x) {
  return __builtin_bit_cast(float, __builtin_amdgcn_mov_dpp(__builtin_bit_cast(int, x), CTRL, 0xf, 0xf, true));
}
template <int CTRL> __device__ __forceinline__ u32 dppu(u32 x) { return (u32)__builtin_amdgcn_mov_dpp((int)x, CTRL, 0xf, 0xf, true); }
constexpr int XOR1 = 0xB1, XOR2 = 0x4E, XOR7 = 0x141, XOR8 = 0x128, XOR15 = 0x140;
__device__ __forceinline__ float xrow_sum(float x) {
  auto s = __builtin_amdgcn_permlane16_swap(__float_as_uint(x), __float_as_uint(x), false, false);
  x = __uint_as_float(s[0]) + __uint_as_float(s[1]);
  auto t = __builtin_amdgcn_permlane32_swap(__float_as_uint(x), __float_as_uint(x), false, false);
  return __uint_as_float(t[0]) + __uint_as_float(t[1]);
}
__device__ __forceinline__ float xrow_max(float x) {
  auto s = __builtin_amdgcn_permlane16_swap(__float_as_uint(x), __float_as_uint(x), false, false);
  x = fmaxf(__uint_as_float(s[0]), __uint_as_float(s[1]));
  auto t = __builtin_amdgcn_permlane32_swap(__float_as_uint(x), __float_as_uint(x), false, false);
  return fmaxf(__uint_as_float(t[0]), __uint_as_float(t[1]));
}
__device__ __forceinline__ float wave_sum(float x) {
  x += dppf<XOR1>(x); x += dppf<XOR2>(x); x += dppf<XOR7>(x); x += dppf<XOR15>(x);
  return xrow_sum(x);
}
__device__ __forceinline__ float wave_max(float x) {
  x = fmaxf(x, dppf<XOR1>(x)); x = fmaxf(x, dppf<XOR2>(x)); x = fmaxf(x, dppf<XOR7>(x)); x = fmaxf(x, dppf<XOR15>(x));
  return xrow_max(x);
}
__device__ __forceinline__ v4u ld_nt(__amdgpu_buffer_rsrc_t r, int voff, int soff) { return __builtin_amdgcn_raw_buffer_load_b128(r, voff, soff, AUX_NT); }
__device__ __forceinline__ v4u ld_sc1(__amdgpu_buffer_rsrc_t r, int voff) { return __builtin_amdgcn_raw_buffer_load_b128(r, voff, 0, AUX_SC1); }
__device__ __forceinline__ void st_sc1(__amdgpu_buffer_rsrc_t r, int voff, v4u v) { __builtin_amdgcn_raw_buffer_store_b128(v, r, voff, 0, AUX_SC1); }
__device__ __forceinline__ __amdgpu_buffer_rsrc_t mkrsrc(const void* p, int bytes) { return __builtin_amdgcn_make_buffer_rsrc((void*)p, 0, bytes, RSRC_W3); }
__device__ __forceinline__ v4u enc2(float a, float b, u32 tlo, u32 thi) {
  v4u g;
  g.x = __builtin_amdgcn_perm(tlo, __float_as_uint(a), 0x04030201u); g.y = thi;
  g.z = __builtin_amdgcn_perm(tlo, __float_as_uint(b), 0x04030201u); g.w = thi;
  return g;
}
__device__ __forceinline__ bool ok2(v4u g, u32 tlo, u32 thi) { return g.y == thi && g.w == thi && (g.x >> 24) == tlo && (g.z >> 24) == tlo; }
__device__ __forceinline__ float dec(u32 w) { return __uint_as_float(w << 8); }

__device__ __forceinline__ void layernorm5(const u32 (&xr)[5], u32 (&ln)[5]) {
  float v[10], s = 0.f;
#pragma unroll
  for (int k = 0; k < 5; ++k) { v[2 * k] = blo(xr[k]); v[2 * k + 1] = bhi(xr[k]); s += v[2 * k] + v[2 * k + 1]; }
  const float mean = wave_sum(s) * (1.0f / DM);
  float q = 0.f;
#pragma unroll
  for (int k = 0; k < 10; ++k) { const float d = v[k] - mean; q = fmaf(d, d, q); }
  const float var = wave_sum(q) * (1.0f / DM);
  const float mb = rb(mean), t = rb(rb(var) + 1e-5f), r = rb(__builtin_amdgcn_sqrtf(t)), ri = __builtin_amdgcn_rcpf(r);
#pragma unroll
  for (int k = 0; k < 5; ++k) ln[k] = pack2(rb(v[2 * k] - mb) * ri, rb(v[2 * k + 1] - mb) * ri);
}
__device__ __forceinline__ u32 mksel(int j, int k, int half, bool act) {
  const u32 src = half ? 0x0302u : 0x0100u;
  const u32 s = (j & 1) ? ((src << 16) | 0x0c0cu) : (0x0c0c0000u | src);
  return (act && (j >> 1) == k) ? s : 0x0c0c0c0cu;
}
#define STAMP(n) do {} while (0)
#define SB() __builtin_amdgcn_sched_barrier(0)

constexpr int L_LN = 0;
constexpr int L_RED = 8 * 1280;
constexpr int L_MISC = L_RED + 8 * 2048;
constexpr int L_BYTES = L_MISC + 4096;

__global__ void __launch_bounds__(NTHR, 2) fused_block(Params p) {
  __shared__ __attribute__((aligned(16))) unsigned char smem[L_BYTES];
  LAS unsigned char* L = (LAS unsigned char*)smem;
  const int tid = threadIdx.x, lane = tid & 63, w = __builtin_amdgcn_readfirstlane(tid >> 6);
  const int bid = blockIdx.x;
  const int r = lane >> 4, c = lane & 15;
  STAMP(0);
  const __amdgpu_buffer_rsrc_t wsr = mkrsrc(p.ws, WS_USED);
  const int h = (bid & 7) + 8 * (bid >> 6), bs = (bid >> 3) & 7, b = bs >> 1, s = bs & 1;
  const size_t kvoff = ((size_t)(b * NH + h) * SK + (SK - WIN) + s * 1024) * DH;
  const __amdgpu_buffer_rsrc_t kr = mkrsrc(p.K + kvoff, 1024 * DH * 2), vr = mkrsrc(p.V + kvoff, 1024 * DH * 2);
  const __amdgpu_buffer_rsrc_t qr = mkrsrc(p.wq + h * DH, DM * NH * DH * 2 - h * DH * 2);
  const u64 epoch = __hip_atomic_load((GAS u64*)p.ws, RLX_AGENT);
  u32 xr[5];
#pragma unroll
  for (int k = 0; k < 5; ++k) xr[k] = ((const GAS u32*)p.x)[b * 320 + lane + 64 * k];
  SB();
  v4u wqv[20];
  {
    const int vo = (80 * w + 20 * r) * (NH * DH * 2) + c * 16;
#pragma unroll
    for (int i = 0; i < 20; ++i) wqv[i] = ld_nt(qr, vo, i * (NH * DH * 2));
  }
  SB();
  v4u ring[RD];
  const int kvo = w * 1024 + lane * 16;
#pragma unroll
  for (int n = 0; n < RD; ++n) ring[n] = ld_nt(kr, kvo, n * 8192);
  SB();
  const u64 tag = epoch + 1;
  const u32 tlo = __builtin_amdgcn_readfirstlane((u32)tag & 0xffu), thi = __builtin_amdgcn_readfirstlane((u32)(tag >> 8));
  const u32 t32 = __builtin_amdgcn_readfirstlane((u32)tag);
  u32 selA[4], selB[4];
#pragma unroll
  for (int k = 0; k < 4; ++k) { selA[k] = mksel(c & 7, k, 0, c < 8); selB[k] = mksel(c & 7, k, 1, c < 8); }
  {
    u32 ln[5];
    layernorm5(xr, ln);
    LAS u32* lnw = (LAS u32*)(L + L_LN + w * 1280);
#pragma unroll
    for (int k = 0; k < 5; ++k) lnw[lane + 64 * k] = ln[k];
    v2u lv[5];
#pragma unroll
    for (int k = 0; k < 5; ++k) lv[k] = *(LAS v2u*)(lnw + 40 * w + 10 * r + 2 * k);
    f4 acc = {0.f, 0.f, 0.f, 0.f};
#pragma unroll
    for (int i = 0; i < 20; ++i) {
      const u32 x = (i & 2) ? lv[i >> 2].y : lv[i >> 2].x;
      v4u a;
#pragma unroll
      for (int k = 0; k < 4; ++k) a[k] = __builtin_amdgcn_perm(x, x, (i & 1) ? selB[k] : selA[k]);
      acc = mfma(a, wqv[i], acc);
    }
    if (lane < 32) *(LAS f4*)(L + L_RED + w * 2048 + (8 * c + 4 * r) * 4) = acc;
  }
  __syncthreads();
  if (tid < 64) {
    f2 sum = {0.f, 0.f};
#pragma unroll
    for (int ww = 0; ww < 8; ++ww) sum += *(LAS f2*)(L + L_RED + ww * 2048 + lane * 8);
    *(LAS u32*)(L + L_MISC + lane * 4) = pack2(sum.x, sum.y);
  }
  __syncthreads();
  const v4u qv = *(LAS v4u*)(L + L_MISC + c * 16);
  STAMP(1);
  float sc[2];
#pragma unroll
  for (int g = 0; g < 2; ++g) {
    float x[16];
#pragma unroll
    for (int i = 0; i < 16; ++i) {
      const int n = 16 * g + i;
      const v4u kk = ring[n % RD];
      x[i] = dot2(kk.w, qv.w, dot2(kk.z, qv.z, dot2(kk.y, qv.y, dot2(kk.x, qv.x, 0.f))));
      const int nn = n + RD;
      if (nn < 32) ring[n % RD] = ld_nt(kr, kvo, nn * 8192);
      else if (nn < 64) ring[n % RD] = ld_nt(vr, kvo, (nn - 32) * 8192);
      SB();
    }
#define TR_STEP(HALF, CTRL, BIT) _Pragma("unroll") for (int hh = 0; hh < (HALF); ++hh) { \
      const float lo_ = x[hh], hi_ = x[hh + (HALF)]; const float t1_ = lo_ + dppf<CTRL>(lo_), t2_ = hi_ + dppf<CTRL>(hi_); x[hh] = (c & (BIT)) ? t2_ : t1_; }
    TR_STEP(8, XOR8, 8) TR_STEP(4, XOR7, 4) TR_STEP(2, XOR2, 2) TR_STEP(1, XOR1, 1)
#undef TR_STEP
    sc[g] = rb(rb(x[0]) * 0.08838834764831845f);
  }
  STAMP(2);
  LAS float* mbuf = (LAS float*)(L + L_MISC + 256);
  LAS float* lbuf = mbuf + 8;
  {
    const float mw = wave_max(fmaxf(sc[0], sc[1]));
    if (lane == 0) mbuf[w] = mw;
  }
  __syncthreads();
  float M;
  { const f4 a = *(LAS f4*)mbuf, bb = *(LAS f4*)(mbuf + 4);
    M = fmaxf(fmaxf(fmaxf(a.x, a.y), fmaxf(a.z, a.w)), fmaxf(fmaxf(bb.x, bb.y), fmaxf(bb.z, bb.w))); }
  float pe[2];
  pe[0] = __builtin_amdgcn_exp2f((sc[0] - M) * LOG2E); pe[1] = __builtin_amdgcn_exp2f((sc[1] - M) * LOG2E);
  { const float lw = wave_sum(pe[0] + pe[1]); if (lane == 0) lbuf[w] = lw; }
  __syncthreads();
  float Lsum;
  { const f4 a = *(LAS f4*)lbuf, bb = *(LAS f4*)(lbuf + 4);
    Lsum = ((a.x + a.y) + (a.z + a.w)) + ((bb.x + bb.y) + (bb.z + bb.w)); }
  const float linv = __builtin_amdgcn_rcpf(Lsum);
  u32 pb[2];
  pb[0] = pack2(pe[0] * linv, 0.f); pb[1] = pack2(pe[1] * linv, 0.f);
  const int x1p = OFF_X1 + (h * 8 + bs) * X1_STRIDE;
  if (tid == 0) { const float lse = M + __builtin_amdgcn_logf(Lsum) * 0.6931471805599453f; st_sc1(wsr, x1p + 128 * 8, enc2(lse, lse, tlo, thi)); }
  {
    f4 acc = {0.f, 0.f, 0.f, 0.f};
#define PV_STEP(i) { const v4u vv = ring[(32 + (i)) % RD]; \
      const u32 x = (u32)__builtin_amdgcn_update_dpp(0, (int)pb[(i) >> 4], 0x150 + ((i) & 15), 0xf, 0xf, false); \
      v4u a; a[0] = __builtin_amdgcn_perm(x, x, selA[0]); a[1] = __builtin_amdgcn_perm(x, x, selA[1]); a[2] = __builtin_amdgcn_perm(x, x, selA[2]); a[3] = __builtin_amdgcn_perm(x, x, selA[3]); \
      acc = mfma(a, vv, acc); \
      if ((i) + 32 + RD < 64) ring[(32 + (i)) % RD] = ld_nt(vr, kvo, ((i) + RD) * 8192); \
      SB(); }
#define PV4(i) PV_STEP(i) PV_STEP((i) + 1) PV_STEP((i) + 2) PV_STEP((i) + 3)
    PV4(0) PV4(4) PV4(8) PV4(12) PV4(16) PV4(20) PV4(24) PV4(28)
    if (lane < 32) *(LAS f4*)(L + L_RED + w * 2048 + (8 * c + 4 * r) * 4) = acc;
  }
  STAMP(3);
  __syncthreads();
  if (tid < 64) {
    f2 sum = {0.f, 0.f};
#pragma unroll
    for (int ww = 0; ww < 8; ++ww) sum += *(LAS f2*)(L + L_RED + ww * 2048 + lane * 8);
    st_sc1(wsr, x1p + lane * 16, enc2(sum.x, sum.y, tlo, thi));
  }
  STAMP(4);
  if (bid < 160) {
    const int hh = bid / 5, j = bid - 5 * hh;
    const __amdgpu_buffer_rsrc_t orr = mkrsrc(p.wo + (size_t)hh * DH * DM + 128 * j, DH * DM * 2 - 256 * j);
    v4u wv[4];
    { const int vo = (16 * w + r) * (DM * 2) + c * 16;
#pragma unroll
      for (int i = 0; i < 4; ++i) wv[i] = ld_nt(orr, vo, i * 4 * DM * 2); }
    SB();
    u32 sel[4];
#pragma unroll
    for (int k = 0; k < 4; ++k) sel[k] = mksel(lane & 7, k, r & 1, true);
    const int pp = lane >> 3, q = lane & 7;
    const int go = OFF_X1 + (hh * 8 + pp) * X1_STRIDE;
    v4u g0, g1;
    for (u32 spin = 0;; ++spin) {
      asm volatile("" ::: "memory");
      g0 = ld_sc1(wsr, go + (16 * w + 2 * q) * 8);
      g1 = ld_sc1(wsr, go + 128 * 8);
      if (__all(ok2(g0, tlo, thi) && ok2(g1, tlo, thi)) || spin > SPIN_CAP) break;
    }
    STAMP(5);
    const float lse = dec(g1.x), lso = dppf<XOR8>(lse);
    const float wt = __builtin_amdgcn_rcpf(1.0f + __builtin_amdgcn_exp2f((lso - lse) * LOG2E));
    float o0 = wt * dec(g0.x), o1 = wt * dec(g0.z);
    o0 += dppf<XOR8>(o0); o1 += dppf<XOR8>(o1);
    const u32 ob = pack2(o0, o1);
    f4 acc[2] = {{0.f, 0.f, 0.f, 0.f}, {0.f, 0.f, 0.f, 0.f}};
#pragma unroll
    for (int i = 0; i < 4; ++i)
#pragma unroll
      for (int P = 0; P < 2; ++P) {
        const int bb = 2 * P + ((lane >> 3) & 1);
        const u32 x = (u32)__builtin_amdgcn_ds_bpermute((16 * bb + 2 * i + (r >> 1)) * 4, (int)ob);
        v4u a;
#pragma unroll
        for (int k = 0; k < 4; ++k) a[k] = __builtin_amdgcn_perm(x, x, sel[k]);
        acc[P] = mfma(a, wv[i], acc[P]);
      }
    __syncthreads();
#pragma unroll
    for (int P = 0; P < 2; ++P) *(LAS f4*)(L + L_RED + w * 2048 + P * 1024 + lane * 16) = acc[P];
    __syncthreads();
    if (tid < 256) {
      const int bb = w, col = 2 * lane;
      const int P = bb >> 1, g = 2 * (bb & 1) + ((col >> 2) & 1), cc = col >> 3, reg = col & 3;
      f2 sum = {0.f, 0.f};
#pragma unroll
      for (int ww = 0; ww < 8; ++ww) sum += *(LAS f2*)(L + L_RED + ww * 2048 + P * 1024 + (16 * g + cc) * 16 + reg * 4);
      const int cs = 8 * j + (col >> 4);
      st_sc1(wsr, OFF_X2 + ((cs * 4 + bb) * 32 + hh) * 128 + (col & 15) * 8, enc2(sum.x, sum.y, tlo, thi));
    }
    STAMP(6);
  } else if (bid < 216) {
    const int k8 = bid - 160, xc = k8 & 7, m = k8 >> 3;
    const int u = m < 6 ? 2 * (xc + 8 * (m >> 1)) + (m & 1) : 48 + xc;
    if (u < NFU) {
      const int g = lane >> 4, rsub = (lane >> 3) & 1, mat = (lane >> 2) & 1, cc = lane & 3;
      v4u wa[10];
      { const GAS u16* base = (const GAS u16*)(mat ? p.w3 : p.w1) + (size_t)(80 * w + 2 * g + rsub) * FF + 32 * u + 8 * cc;
#pragma unroll
        for (int i = 0; i < 10; ++i) wa[i] = __builtin_nontemporal_load((const GAS v4u*)(base + (size_t)i * 8 * FF)); }
      v4u wb[8];
      const __amdgpu_buffer_rsrc_t w2r = mkrsrc(p.w2 + (size_t)32 * u * DM, 32 * DM * 2);
      if (w < 5) {
        const int vo = r * (DM * 2) + w * 256 + c * 16;
#pragma unroll
        for (int i = 0; i < 8; ++i) wb[i] = ld_nt(w2r, vo, i * 4 * DM * 2);
      }
      SB();
      LAS u32* ln2 = (LAS u32*)(L + L_LN);
      if (w < 4) {
        u32 hr[5];
        const GAS u64* hs = (const GAS u64*)(p.ws + OFF_X3) + w * 320 + lane;
        for (u32 spin = 0;; ++spin) {
      asm volatile("" ::: "memory");
          bool ok = true;
#pragma unroll
          for (int k = 0; k < 5; ++k) { const u64 gq = __hip_atomic_load(hs + 64 * k, RLX_AGENT); hr[k] = (u32)gq; ok &= (u32)(gq >> 32) == t32; }
          if (__all(ok) || spin > SPIN_CAP) break;
        }
        STAMP(5);
        u32 ln[5];
        layernorm5(hr, ln);
#pragma unroll
        for (int k = 0; k < 5; ++k) ln2[(lane + 64 * k) * 4 + w] = ln[k];
      }
      __syncthreads();
      u32 sel[4];
#pragma unroll
      for (int k = 0; k < 4; ++k) sel[k] = mksel(lane & 7, k, rsub, true);
      f4 acc[4];
#pragma unroll
      for (int bb = 0; bb < 4; ++bb) acc[bb] = f4{0.f, 0.f, 0.f, 0.f};
#pragma unroll
      for (int i = 0; i < 10; ++i) {
        const v4u xs = *(LAS v4u*)(ln2 + (40 * w + 4 * i + g) * 4);
#pragma unroll
        for (int bb = 0; bb < 4; ++bb) {
          v4u a;
#pragma unroll
          for (int k = 0; k < 4; ++k) a[k] = __builtin_amdgcn_perm(xs[bb], xs[bb], sel[k]);
          acc[bb] = mfma(a, wa[i], acc[bb]);
        }
      }
      if ((lane >> 5) == rsub) {
#pragma unroll
        for (int bb = 0; bb < 4; ++bb)
          *(LAS f4*)(L + L_RED + w * 2048 + bb * 512 + rsub * 256 + mat * 128 + (8 * cc + 4 * (g & 1)) * 4) = acc[bb];
      }
      __syncthreads();
      LAS u16* hhs = (LAS u16*)(L + L_MISC + 512);
      if (tid < 128) {
        const int bb = tid >> 5, f = tid & 31;
        float gs = 0.f, us = 0.f;
#pragma unroll
        for (int ww = 0; ww < 8; ++ww) {
          const LAS float* base = (const LAS float*)(L + L_RED + ww * 2048 + bb * 512) + f;
          gs += base[0] + base[64]; us += base[32] + base[96];
        }
        gs = rb(gs); us = rb(us);
        const float si = rb(gs * __builtin_amdgcn_rcpf(1.0f + __builtin_amdgcn_exp2f(-gs * LOG2E)));
        hhs[f * 4 + bb] = (u16)(pack2(si * us, 0.f) & 0xffffu);
      }
      __syncthreads();
      if (w < 5) {
        const int bsel = (lane >> 3) & 1;
        u32 s2[4];
#pragma unroll
        for (int k = 0; k < 4; ++k) s2[k] = mksel(lane & 7, k, bsel, true);
        f4 d[2] = {{0.f, 0.f, 0.f, 0.f}, {0.f, 0.f, 0.f, 0.f}};
#pragma unroll
        for (int i = 0; i < 8; ++i) {
          const v2u xs = *(LAS v2u*)(hhs + (4 * i + r) * 4);
#pragma unroll
          for (int P = 0; P < 2; ++P) {
            const u32 x = P ? xs.y : xs.x;
            v4u a;
#pragma unroll
            for (int k = 0; k < 4; ++k) a[k] = __builtin_amdgcn_perm(x, x, s2[k]);
            d[P] = mfma(a, wb[i], d[P]);
          }
        }
#pragma unroll
        for (int P = 0; P < 2; ++P) {
          const int bb = 2 * P + (r >> 1), cs = 8 * w + (c >> 1);
          const int o = OFF_X4 + ((cs * 4 + bb) * NFU + u) * 128 + (8 * (c & 1) + 4 * (r & 1)) * 8;
          st_sc1(wsr, o, enc2(d[P].x, d[P].y, tlo, thi));
          st_sc1(wsr, o + 16, enc2(d[P].z, d[P].w, tlo, thi));
        }
      }
      STAMP(6);
    }
  } else if (w < 4) {
    const int cs = bid - 216, bb = w;
    const int pp = lane >> 3, q = lane & 7;
    const u32 xv = ((const GAS u32*)p.x)[bb * 320 + 8 * cs + q];
    float s0, s1;
    {
      const int go = OFF_X2 + ((cs * 4 + bb) * 32 + pp) * 128 + q * 16;
      v4u g[4];
      for (u32 spin = 0;; ++spin) {
      asm volatile("" ::: "memory");
        bool ok = true;
#pragma unroll
        for (int m = 0; m < 4; ++m) g[m] = ld_sc1(wsr, go + m * 1024);
#pragma unroll
        for (int m = 0; m < 4; ++m) ok &= ok2(g[m], tlo, thi);
        if (__all(ok) || spin > SPIN_CAP) break;
      }
      s0 = (dec(g[0].x) + dec(g[1].x)) + (dec(g[2].x) + dec(g[3].x));
      s1 = (dec(g[0].z) + dec(g[1].z)) + (dec(g[2].z) + dec(g[3].z));
      s0 += dppf<XOR8>(s0); s1 += dppf<XOR8>(s1);
      s0 = xrow_sum(s0); s1 = xrow_sum(s1);
    }
    STAMP(5);
    const float h0 = rb(blo(xv) + rb(s0)), h1 = rb(bhi(xv) + rb(s1));
    if (lane < 8) __hip_atomic_store((GAS u64*)(p.ws + OFF_X3) + bb * 320 + 8 * cs + q, ((u64)t32 << 32) | pack2(h0, h1), RLX_AGENT);
    {
      const int go = OFF_X4 + ((cs * 4 + bb) * NFU + pp) * 128 + q * 16;
      v4u g[7];
      const bool last = pp < NFU - 48;
      for (u32 spin = 0;; ++spin) {
      asm volatile("" ::: "memory");
        bool ok = true;
#pragma unroll
        for (int m = 0; m < 6; ++m) g[m] = ld_sc1(wsr, go + m * 1024);
        g[6] = ld_sc1(wsr, go + (last ? 6 * 1024 : 0));
#pragma unroll
        for (int m = 0; m < 7; ++m) ok &= ok2(g[m], tlo, thi);
        if (__all(ok) || spin > SPIN_CAP) break;
      }
      s0 = ((dec(g[0].x) + dec(g[1].x)) + (dec(g[2].x) + dec(g[3].x))) + ((dec(g[4].x) + dec(g[5].x)) + (last ? dec(g[6].x) : 0.f));
      s1 = ((dec(g[0].z) + dec(g[1].z)) + (dec(g[2].z) + dec(g[3].z))) + ((dec(g[4].z) + dec(g[5].z)) + (last ? dec(g[6].z) : 0.f));
      s0 += dppf<XOR8>(s0); s1 += dppf<XOR8>(s1);
      s0 = xrow_sum(s0); s1 = xrow_sum(s1);
    }
    if (lane < 8) ((GAS u32*)p.out)[bb * 320 + 8 * cs + q] = pack2(h0 + rb(s0), h1 + rb(s1));
    if (bid == 216 && tid == 0) __hip_atomic_store((GAS u64*)p.ws, tag, RLX_AGENT);
    STAMP(6);
  }
  STAMP(7);
}
}

extern "C" void kernel_launch(void* const* d_in, const int* in_sizes, int n_in, void* d_out, int out_size, void* d_ws, size_t ws_size, hipStream_t stream) {
  Params p;
  p.x = (const u16*)d_in[0]; p.K = (const u16*)d_in[1]; p.V = (const u16*)d_in[2]; p.wq = (const u16*)d_in[3]; p.wo = (const u16*)d_in[4];
  p.w1 = (const u16*)d_in[6]; p.w3 = (const u16*)d_in[7]; p.w2 = (const u16*)d_in[8];
  p.out = (u16*)d_out; p.ws = (unsigned char*)d_ws;
  hipLaunchKernelGGL(fused_block, dim3(256), dim3(NTHR), 0, stream, p);
}
```

```cpp
#define PROBE 0
#define OUTSC1 1
#include <hip/hip_runtime.h>
#include <stdint.h>

typedef unsigned short u16;
typedef unsigned u32;
typedef unsigned long long u64;
typedef u32 v4u __attribute__((ext_vector_type(4)));
typedef u32 v2u __attribute__((ext_vector_type(2)));
typedef float f4 __attribute__((ext_vector_type(4)));
typedef float f2 __attribute__((ext_vector_type(2)));
typedef __bf16 bf2 __attribute__((ext_vector_type(2)));
typedef __bf16 bf8 __attribute__((ext_vector_type(8)));
#define GAS __attribute__((address_space(1)))
#define LAS __attribute__((address_space(3)))
#define RLX_AGENT __ATOMIC_RELAXED, __HIP_MEMORY_SCOPE_AGENT

#define RD 16
#define STAMPS 0

namespace {
constexpr int DM = 640, NH = 32, DH = 128, SK = 8192, WIN = 2048, FF = 1728;
constexpr int NTHR = 512;
constexpr int AUX_NT = 2, AUX_SC1 = 16, RSRC_W3 = 0x00020000;
constexpr int OFF_X1 = 4096;
constexpr int X1_STRIDE = 136 * 8;
constexpr int OFF_X2 = 0x80000;
constexpr int OFF_X3 = 0x130000;
constexpr int OFF_X4 = 0x140000;
constexpr int NFU = 54;
constexpr int WS_USED = 0x280000;
constexpr size_t OFF_STAMP = 8u << 20;
constexpr u32 SPIN_CAP = 1u << 16;
constexpr float LOG2E = 1.4426950408889634f;

struct Params {
  const u16 *x, *K, *V, *wq, *wo, *w1, *w3, *w2;
  u16* out;
  unsigned char* ws;
};

__device__ __forceinline__ float rb(float f) { return (float)(__bf16)f; }
__device__ __forceinline__ u32 pack2(float lo, float hi) { bf2 v = {(__bf16)lo, (__bf16)hi}; return __builtin_bit_cast(u32, v); }
__device__ __forceinline__ float blo(u32 v) { return __uint_as_float(v << 16); }
__device__ __forceinline__ float bhi(u32 v) { return __uint_as_float(v & 0xffff0000u); }
__device__ __forceinline__ float dot2(u32 a, u32 b, float c) {
  return __builtin_amdgcn_fdot2_f32_bf16(__builtin_bit_cast(bf2, a), __builtin_bit_cast(bf2, b), c, false);
}
__device__ __forceinline__ f4 mfma(v4u a, v4u b, f4 c) {
  return __builtin_amdgcn_mfma_f32_16x16x32_bf16(__builtin_bit_cast(bf8, a), __builtin_bit_cast(bf8, b), c, 0, 0, 0);
}
template <int CTRL> __device__ __forceinline__ float dppf(float x) {
  return __builtin_bit_cast(float, __builtin_amdgcn_mov_dpp(__builtin_bit_cast(int, x), CTRL, 0xf, 0xf, true));
}
template <int CTRL> __device__ __forceinline__ u32 dppu(u32 x) { return (u32)__builtin_amdgcn_mov_dpp((int)x, CTRL, 0xf, 0xf, true); }
constexpr int XOR1 = 0xB1, XOR2 = 0x4E, XOR7 = 0x141, XOR8 = 0x128, XOR15 = 0x140;
__device__ __forceinline__ float xrow_sum(float x) {
  auto s = __builtin_amdgcn_permlane16_swap(__float_as_uint(x), __float_as_uint(x), false, false);
  x = __uint_as_float(s[0]) + __uint_as_float(s[1]);
  auto t = __builtin_amdgcn_permlane32_swap(__float_as_uint(x), __float_as_uint(x), false, false);
  return __uint_as_float(t[0]) + __uint_as_float(t[1]);
}
__device__ __forceinline__ float xrow_max(float x) {
  auto s = __builtin_amdgcn_permlane16_swap(__float_as_uint(x), __float_as_uint(x), false, false);
  x = fmaxf(__uint_as_float(s[0]), __uint_as_float(s[1]));
  auto t = __builtin_amdgcn_permlane32_swap(__float_as_uint(x), __float_as_uint(x), false, false);
  return fmaxf(__uint_as_float(t[0]), __uint_as_float(t[1]));
}
__device__ __forceinline__ float wave_sum(float x) {
  x += dppf<XOR1>(x); x += dppf<XOR2>(x); x += dppf<XOR7>(x); x += dppf<XOR15>(x);
  return xrow_sum(x);
}
__device__ __forceinline__ float wave_max(float x) {
  x = fmaxf(x, dppf<XOR1>(x)); x = fmaxf(x, dppf<XOR2>(x)); x = fmaxf(x, dppf<XOR7>(x)); x = fmaxf(x, dppf<XOR15>(x));
  return xrow_max(x);
}
__device__ __forceinline__ v4u ld_nt(__amdgpu_buffer_rsrc_t r, int voff, int soff) { return __builtin_amdgcn_raw_buffer_load_b128(r, voff, soff, AUX_NT); }
__device__ __forceinline__ v4u ld_sc1(__amdgpu_buffer_rsrc_t r, int voff) { return __builtin_amdgcn_raw_buffer_load_b128(r, voff, 0, AUX_SC1); }
__device__ __forceinline__ void st_sc1(__amdgpu_buffer_rsrc_t r, int voff, v4u v) { __builtin_amdgcn_raw_buffer_store_b128(v, r, voff, 0, AUX_SC1); }
__device__ __forceinline__ __amdgpu_buffer_rsrc_t mkrsrc(const void* p, int bytes) { return __builtin_amdgcn_make_buffer_rsrc((void*)p, 0, bytes, RSRC_W3); }
__device__ __forceinline__ v4u enc2(float a, float b, u32 tlo, u32 thi) {
  v4u g;
  g.x = __builtin_amdgcn_perm(tlo, __float_as_uint(a), 0x04030201u); g.y = thi;
  g.z = __builtin_amdgcn_perm(tlo, __float_as_uint(b), 0x04030201u); g.w = thi;
  return g;
}
__device__ __forceinline__ bool ok2(v4u g, u32 tlo, u32 thi) { return g.y == thi && g.w == thi && (g.x >> 24) == tlo && (g.z >> 24) == tlo; }
__device__ __forceinline__ float dec(u32 w) { return __uint_as_float(w << 8); }

__device__ __forceinline__ void layernorm5(const u32 (&xr)[5], u32 (&ln)[5]) {
  float v[10], s = 0.f;
#pragma unroll
  for (int k = 0; k < 5; ++k) { v[2 * k] = blo(xr[k]); v[2 * k + 1] = bhi(xr[k]); s += v[2 * k] + v[2 * k + 1]; }
  const float mean = wave_sum(s) * (1.0f / DM);
  float q = 0.f;
#pragma unroll
  for (int k = 0; k < 10; ++k) { const float d = v[k] - mean; q = fmaf(d, d, q); }
  const float var = wave_sum(q) * (1.0f / DM);
  const float mb = rb(mean), t = rb(rb(var) + 1e-5f), r = rb(__builtin_amdgcn_sqrtf(t)), ri = __builtin_amdgcn_rcpf(r);
#pragma unroll
  for (int k = 0; k < 5; ++k) ln[k] = pack2(rb(v[2 * k] - mb) * ri, rb(v[2 * k + 1] - mb) * ri);
}
__device__ __forceinline__ u32 mksel(int j, int k, int half, bool act) {
  const u32 src = half ? 0x0302u : 0x0100u;
  const u32 s = (j & 1) ? ((src << 16) | 0x0c0cu) : (0x0c0c0000u | src);
  return (act && (j >> 1) == k) ? s : 0x0c0c0c0cu;
}
#define STAMP(n) do { pst[n] = __builtin_amdgcn_s_memrealtime(); } while (0)
#define SB() __builtin_amdgcn_sched_barrier(0)

constexpr int L_LN = 0;
constexpr int L_RED = 8 * 1280;
constexpr int L_MISC = L_RED + 8 * 2048;
constexpr int L_BYTES = L_MISC + 4096;

__global__ void __launch_bounds__(NTHR, 2) fused_block(Params p) {
  __shared__ __attribute__((aligned(16))) unsigned char smem[L_BYTES];
  LAS unsigned char* L = (LAS unsigned char*)smem;
  const int tid = threadIdx.x, lane = tid & 63, w = __builtin_amdgcn_readfirstlane(tid >> 6);
  const int bid = blockIdx.x;
  const int r = lane >> 4, c = lane & 15;
  u64 pst[8] = {0, 0, 0, 0, 0, 0, 0, 0};
  STAMP(0);
  const __amdgpu_buffer_rsrc_t wsr = mkrsrc(p.ws, WS_USED);
  const int h = (bid & 7) + 8 * (bid >> 6), bs = (bid >> 3) & 7, b = bs >> 1, s = bs & 1;
  const size_t kvoff = ((size_t)(b * NH + h) * SK + (SK - WIN) + s * 1024) * DH;
  const __amdgpu_buffer_rsrc_t kr = mkrsrc(p.K + kvoff, 1024 * DH * 2), vr = mkrsrc(p.V + kvoff, 1024 * DH * 2);
  const __amdgpu_buffer_rsrc_t qr = mkrsrc(p.wq + h * DH, DM * NH * DH * 2 - h * DH * 2);
  const u64 epoch = __hip_atomic_load((GAS u64*)p.ws, RLX_AGENT);
  u32 xr[5];
#pragma unroll
  for (int k = 0; k < 5; ++k) xr[k] = ((const GAS u32*)p.x)[b * 320 + lane + 64 * k];
  SB();
  v4u wqv[20];
  {
    const int vo = (80 * w + 20 * r) * (NH * DH * 2) + c * 16;
#pragma unroll
    for (int i = 0; i < 20; ++i) wqv[i] = ld_nt(qr, vo, i * (NH * DH * 2));
  }
  SB();
  v4u ring[RD];
  const int kvo = w * 1024 + lane * 16;
#pragma unroll
  for (int n = 0; n < RD; ++n) ring[n] = ld_nt(kr, kvo, n * 8192);
  SB();
  const u64 tag = epoch + 1;
  const u32 tlo = __builtin_amdgcn_readfirstlane((u32)tag & 0xffu), thi = __builtin_amdgcn_readfirstlane((u32)(tag >> 8));
  const u32 t32 = __builtin_amdgcn_readfirstlane((u32)tag);
  u32 selA[4], selB[4];
#pragma unroll
  for (int k = 0; k < 4; ++k) { selA[k] = mksel(c & 7, k, 0, c < 8); selB[k] = mksel(c & 7, k, 1, c < 8); }
  {
    u32 ln[5];
    layernorm5(xr, ln);
    LAS u32* lnw = (LAS u32*)(L + L_LN + w * 1280);
#pragma unroll
    for (int k = 0; k < 5; ++k) lnw[lane + 64 * k] = ln[k];
    v2u lv[5];
#pragma unroll
    for (int k = 0; k < 5; ++k) lv[k] = *(LAS v2u*)(lnw + 40 * w + 10 * r + 2 * k);
    f4 acc = {0.f, 0.f, 0.f, 0.f};
#pragma unroll
    for (int i = 0; i < 20; ++i) {
      const u32 x = (i & 2) ? lv[i >> 2].y : lv[i >> 2].x;
      v4u a;
#pragma unroll
      for (int k = 0; k < 4; ++k) a[k] = __builtin_amdgcn_perm(x, x, (i & 1) ? selB[k] : selA[k]);
      acc = mfma(a, wqv[i], acc);
    }
    if (lane < 32) *(LAS f4*)(L + L_RED + w * 2048 + (8 * c + 4 * r) * 4) = acc;
  }
  __syncthreads();
  if (tid < 64) {
    f2 sum = {0.f, 0.f};
#pragma unroll
    for (int ww = 0; ww < 8; ++ww) sum += *(LAS f2*)(L + L_RED + ww * 2048 + lane * 8);
    *(LAS u32*)(L + L_MISC + lane * 4) = pack2(sum.x, sum.y);
  }
  __syncthreads();
  const v4u qv = *(LAS v4u*)(L + L_MISC + c * 16);
  STAMP(1);
  float sc[2];
#pragma unroll
  for (int g = 0; g < 2; ++g) {
    float x[16];
#pragma unroll
    for (int i = 0; i < 16; ++i) {
      const int n = 16 * g + i;
      const v4u kk = ring[n % RD];
      x[i] = dot2(kk.w, qv.w, dot2(kk.z, qv.z, dot2(kk.y, qv.y, dot2(kk.x, qv.x, 0.f))));
      const int nn = n + RD;
      if (nn < 32) ring[n % RD] = ld_nt(kr, kvo, nn * 8192);
      else if (nn < 64) ring[n % RD] = ld_nt(vr, kvo, (nn - 32) * 8192);
      SB();
    }
#define TR_STEP(HALF, CTRL, BIT) _Pragma("unroll") for (int hh = 0; hh < (HALF); ++hh) { \
      const float lo_ = x[hh], hi_ = x[hh + (HALF)]; const float t1_ = lo_ + dppf<CTRL>(lo_), t2_ = hi_ + dppf<CTRL>(hi_); x[hh] = (c & (BIT)) ? t2_ : t1_; }
    TR_STEP(8, XOR8, 8) TR_STEP(4, XOR7, 4) TR_STEP(2, XOR2, 2) TR_STEP(1, XOR1, 1)
#undef TR_STEP
    sc[g] = rb(rb(x[0]) * 0.08838834764831845f);
  }
  STAMP(2);
  LAS float* mbuf = (LAS float*)(L + L_MISC + 256);
  LAS float* lbuf = mbuf + 8;
  {
    const float mw = wave_max(fmaxf(sc[0], sc[1]));
    if (lane == 0) mbuf[w] = mw;
  }
  __syncthreads();
  float M;
  { const f4 a = *(LAS f4*)mbuf, bb = *(LAS f4*)(mbuf + 4);
    M = fmaxf(fmaxf(fmaxf(a.x, a.y), fmaxf(a.z, a.w)), fmaxf(fmaxf(bb.x, bb.y), fmaxf(bb.z, bb.w))); }
  float pe[2];
  pe[0] = __builtin_amdgcn_exp2f((sc[0] - M) * LOG2E); pe[1] = __builtin_amdgcn_exp2f((sc[1] - M) * LOG2E);
  { const float lw = wave_sum(pe[0] + pe[1]); if (lane == 0) lbuf[w] = lw; }
  __syncthreads();
  float Lsum;
  { const f4 a = *(LAS f4*)lbuf, bb = *(LAS f4*)(lbuf + 4);
    Lsum = ((a.x + a.y) + (a.z + a.w)) + ((bb.x + bb.y) + (bb.z + bb.w)); }
  const float linv = __builtin_amdgcn_rcpf(Lsum);
  u32 pb[2];
  pb[0] = pack2(pe[0] * linv, 0.f); pb[1] = pack2(pe[1] * linv, 0.f);
  const int x1p = OFF_X1 + (h * 8 + bs) * X1_STRIDE;
  if (tid == 0) { const float lse = M + __builtin_amdgcn_logf(Lsum) * 0.6931471805599453f; st_sc1(wsr, x1p + 128 * 8, enc2(lse, lse, tlo, thi)); }
  {
    f4 acc = {0.f, 0.f, 0.f, 0.f};
#define PV_STEP(i) { const v4u vv = ring[(32 + (i)) % RD]; \
      const u32 x = (u32)__builtin_amdgcn_update_dpp(0, (int)pb[(i) >> 4], 0x150 + ((i) & 15), 0xf, 0xf, false); \
      v4u a; a[0] = __builtin_amdgcn_perm(x, x, selA[0]); a[1] = __builtin_amdgcn_perm(x, x, selA[1]); a[2] = __builtin_amdgcn_perm(x, x, selA[2]); a[3] = __builtin_amdgcn_perm(x, x, selA[3]); \
      acc = mfma(a, vv, acc); \
      if ((i) + 32 + RD < 64) ring[(32 + (i)) % RD] = ld_nt(vr, kvo, ((i) + RD) * 8192); \
      SB(); }
#define PV4(i) PV_STEP(i) PV_STEP((i) + 1) PV_STEP((i) + 2) PV_STEP((i) + 3)
    PV4(0) PV4(4) PV4(8) PV4(12) PV4(16) PV4(20) PV4(24) PV4(28)
    if (lane < 32) *(LAS f4*)(L + L_RED + w * 2048 + (8 * c + 4 * r) * 4) = acc;
  }
  STAMP(3);
  __syncthreads();
  if (tid < 64) {
    f2 sum = {0.f, 0.f};
#pragma unroll
    for (int ww = 0; ww < 8; ++ww) sum += *(LAS f2*)(L + L_RED + ww * 2048 + lane * 8);
    st_sc1(wsr, x1p + lane * 16, enc2(sum.x, sum.y, tlo, thi));
  }
  STAMP(4);
  if (bid < 160) {
    const int hh = bid / 5, j = bid - 5 * hh;
    const __amdgpu_buffer_rsrc_t orr = mkrsrc(p.wo + (size_t)hh * DH * DM + 128 * j, DH * DM * 2 - 256 * j);
    v4u wv[4];
    { const int vo = (16 * w + r) * (DM * 2) + c * 16;
#pragma unroll
      for (int i = 0; i < 4; ++i) wv[i] = ld_nt(orr, vo, i * 4 * DM * 2); }
    SB();
    u32 sel[4];
#pragma unroll
    for (int k = 0; k < 4; ++k) sel[k] = mksel(lane & 7, k, r & 1, true);
    const int pp = lane >> 3, q = lane & 7;
    const int go = OFF_X1 + (hh * 8 + pp) * X1_STRIDE;
    v4u g0, g1;
    for (u32 spin = 0;; ++spin) {
      asm volatile("" ::: "memory");
      g0 = ld_sc1(wsr, go + (16 * w + 2 * q) * 8);
      g1 = ld_sc1(wsr, go + 128 * 8);
      if (__all(ok2(g0, tlo, thi) && ok2(g1, tlo, thi)) || spin > SPIN_CAP) break;
    }
    STAMP(5);
    const float lse = dec(g1.x), lso = dppf<XOR8>(lse);
    const float wt = __builtin_amdgcn_rcpf(1.0f + __builtin_amdgcn_exp2f((lso - lse) * LOG2E));
    float o0 = wt * dec(g0.x), o1 = wt * dec(g0.z);
    o0 += dppf<XOR8>(o0); o1 += dppf<XOR8>(o1);
    const u32 ob = pack2(o0, o1);
    f4 acc[2] = {{0.f, 0.f, 0.f, 0.f}, {0.f, 0.f, 0.f, 0.f}};
#pragma unroll
    for (int i = 0; i < 4; ++i)
#pragma unroll
      for (int P = 0; P < 2; ++P) {
        const int bb = 2 * P + ((lane >> 3) & 1);
        const u32 x = (u32)__builtin_amdgcn_ds_bpermute((16 * bb + 2 * i + (r >> 1)) * 4, (int)ob);
        v4u a;
#pragma unroll
        for (int k = 0; k < 4; ++k) a[k] = __builtin_amdgcn_perm(x, x, sel[k]);
        acc[P] = mfma(a, wv[i], acc[P]);
      }
    __syncthreads();
#pragma unroll
    for (int P = 0; P < 2; ++P) *(LAS f4*)(L + L_RED + w * 2048 + P * 1024 + lane * 16) = acc[P];
    __syncthreads();
    if (tid < 256) {
      const int bb = w, col = 2 * lane;
      const int P = bb >> 1, g = 2 * (bb & 1) + ((col >> 2) & 1), cc = col >> 3, reg = col & 3;
      f2 sum = {0.f, 0.f};
#pragma unroll
      for (int ww = 0; ww < 8; ++ww) sum += *(LAS f2*)(L + L_RED + ww * 2048 + P * 1024 + (16 * g + cc) * 16 + reg * 4);
      const int cs = 8 * j + (col >> 4);
      st_sc1(wsr, OFF_X2 + ((cs * 4 + bb) * 32 + hh) * 128 + (col & 15) * 8, enc2(sum.x, sum.y, tlo, thi));
    }
    STAMP(6);
  } else if (bid < 216) {
    const int k8 = bid - 160, xc = k8 & 7, m = k8 >> 3;
    const int u = m < 6 ? 2 * (xc + 8 * (m >> 1)) + (m & 1) : 48 + xc;
    if (u < NFU) {
      const int g = lane >> 4, rsub = (lane >> 3) & 1, mat = (lane >> 2) & 1, cc = lane & 3;
      v4u wa[10];
      { const GAS u16* base = (const GAS u16*)(mat ? p.w3 : p.w1) + (size_t)(80 * w + 2 * g + rsub) * FF + 32 * u + 8 * cc;
#pragma unroll
        for (int i = 0; i < 10; ++i) wa[i] = __builtin_nontemporal_load((const GAS v4u*)(base + (size_t)i * 8 * FF)); }
      v4u wb[8];
      const __amdgpu_buffer_rsrc_t w2r = mkrsrc(p.w2 + (size_t)32 * u * DM, 32 * DM * 2);
      if (w < 5) {
        const int vo = r * (DM * 2) + w * 256 + c * 16;
#pragma unroll
        for (int i = 0; i < 8; ++i) wb[i] = ld_nt(w2r, vo, i * 4 * DM * 2);
      }
      SB();
      LAS u32* ln2 = (LAS u32*)(L + L_LN);
      if (w < 4) {
        u32 hr[5];
        const GAS u64* hs = (const GAS u64*)(p.ws + OFF_X3) + w * 320 + lane;
        for (u32 spin = 0;; ++spin) {
      asm volatile("" ::: "memory");
          bool ok = true;
#pragma unroll
          for (int k = 0; k < 5; ++k) { const u64 gq = __hip_atomic_load(hs + 64 * k, RLX_AGENT); hr[k] = (u32)gq; ok &= (u32)(gq >> 32) == t32; }
          if (__all(ok) || spin > SPIN_CAP) break;
        }
        STAMP(5);
        u32 ln[5];
        layernorm5(hr, ln);
#pragma unroll
        for (int k = 0; k < 5; ++k) ln2[(lane + 64 * k) * 4 + w] = ln[k];
      }
      __syncthreads();
      u32 sel[4];
#pragma unroll
      for (int k = 0; k < 4; ++k) sel[k] = mksel(lane & 7, k, rsub, true);
      f4 acc[4];
#pragma unroll
      for (int bb = 0; bb < 4; ++bb) acc[bb] = f4{0.f, 0.f, 0.f, 0.f};
#pragma unroll
      for (int i = 0; i < 10; ++i) {
        const v4u xs = *(LAS v4u*)(ln2 + (40 * w + 4 * i + g) * 4);
#pragma unroll
        for (int bb = 0; bb < 4; ++bb) {
          v4u a;
#pragma unroll
          for (int k = 0; k < 4; ++k) a[k] = __builtin_amdgcn_perm(xs[bb], xs[bb], sel[k]);
          acc[bb] = mfma(a, wa[i], acc[bb]);
        }
      }
      if ((lane >> 5) == rsub) {
#pragma unroll
        for (int bb = 0; bb < 4; ++bb)
          *(LAS f4*)(L + L_RED + w * 2048 + bb * 512 + rsub * 256 + mat * 128 + (8 * cc + 4 * (g & 1)) * 4) = acc[bb];
      }
      __syncthreads();
      LAS u16* hhs = (LAS u16*)(L + L_MISC + 512);
      if (tid < 128) {
        const int bb = tid >> 5, f = tid & 31;
        float gs = 0.f, us = 0.f;
#pragma unroll
        for (int ww = 0; ww < 8; ++ww) {
          const LAS float* base = (const LAS float*)(L + L_RED + ww * 2048 + bb * 512) + f;
          gs += base[0] + base[64]; us += base[32] + base[96];
        }
        gs = rb(gs); us = rb(us);
        const float si = rb(gs * __builtin_amdgcn_rcpf(1.0f + __builtin_amdgcn_exp2f(-gs * LOG2E)));
        hhs[f * 4 + bb] = (u16)(pack2(si * us, 0.f) & 0xffffu);
      }
      __syncthreads();
      if (w < 5) {
        const int bsel = (lane >> 3) & 1;
        u32 s2[4];
#pragma unroll
        for (int k = 0; k < 4; ++k) s2[k] = mksel(lane & 7, k, bsel, true);
        f4 d[2] = {{0.f, 0.f, 0.f, 0.f}, {0.f, 0.f, 0.f, 0.f}};
#pragma unroll
        for (int i = 0; i < 8; ++i) {
          const v2u xs = *(LAS v2u*)(hhs + (4 * i + r) * 4);
#pragma unroll
          for (int P = 0; P < 2; ++P) {
            const u32 x = P ? xs.y : xs.x;
            v4u a;
#pragma unroll
            for (int k = 0; k < 4; ++k) a[k] = __builtin_amdgcn_perm(x, x, s2[k]);
            d[P] = mfma(a, wb[i], d[P]);
          }
        }
#pragma unroll
        for (int P = 0; P < 2; ++P) {
          const int bb = 2 * P + (r >> 1), cs = 8 * w + (c >> 1);
          const int o = OFF_X4 + ((cs * 4 + bb) * NFU + u) * 128 + (8 * (c & 1) + 4 * (r & 1)) * 8;
          st_sc1(wsr, o, enc2(d[P].x, d[P].y, tlo, thi));
          st_sc1(wsr, o + 16, enc2(d[P].z, d[P].w, tlo, thi));
        }
      }
      STAMP(6);
    }
  } else if (w < 4) {
    const int cs = bid - 216, bb = w;
    const int pp = lane >> 3, q = lane & 7;
    const u32 xv = ((const GAS u32*)p.x)[bb * 320 + 8 * cs + q];
    float s0, s1;
    {
      const int go = OFF_X2 + ((cs * 4 + bb) * 32 + pp) * 128 + q * 16;
      v4u g[4];
      for (u32 spin = 0;; ++spin) {
      asm volatile("" ::: "memory");
        bool ok = true;
#pragma unroll
        for (int m = 0; m < 4; ++m) g[m] = ld_sc1(wsr, go + m * 1024);
#pragma unroll
        for (int m = 0; m < 4; ++m) ok &= ok2(g[m], tlo, thi);
        if (__all(ok) || spin > SPIN_CAP) break;
      }
      s0 = (dec(g[0].x) + dec(g[1].x)) + (dec(g[2].x) + dec(g[3].x));
      s1 = (dec(g[0].z) + dec(g[1].z)) + (dec(g[2].z) + dec(g[3].z));
      s0 += dppf<XOR8>(s0); s1 += dppf<XOR8>(s1);
      s0 = xrow_sum(s0); s1 = xrow_sum(s1);
    }
    STAMP(5);
    const float h0 = rb(blo(xv) + rb(s0)), h1 = rb(bhi(xv) + rb(s1));
    if (lane < 8) __hip_atomic_store((GAS u64*)(p.ws + OFF_X3) + bb * 320 + 8 * cs + q, ((u64)t32 << 32) | pack2(h0, h1), RLX_AGENT);
    {
      const int go = OFF_X4 + ((cs * 4 + bb) * NFU + pp) * 128 + q * 16;
      v4u g[7];
      const bool last = pp < NFU - 48;
      for (u32 spin = 0;; ++spin) {
      asm volatile("" ::: "memory");
        bool ok = true;
#pragma unroll
        for (int m = 0; m < 6; ++m) g[m] = ld_sc1(wsr, go + m * 1024);
        g[6] = ld_sc1(wsr, go + (last ? 6 * 1024 : 0));
#pragma unroll
        for (int m = 0; m < 7; ++m) ok &= ok2(g[m], tlo, thi);
        if (__all(ok) || spin > SPIN_CAP) break;
      }
      s0 = ((dec(g[0].x) + dec(g[1].x)) + (dec(g[2].x) + dec(g[3].x))) + ((dec(g[4].x) + dec(g[5].x)) + (last ? dec(g[6].x) : 0.f));
      s1 = ((dec(g[0].z) + dec(g[1].z)) + (dec(g[2].z) + dec(g[3].z))) + ((dec(g[4].z) + dec(g[5].z)) + (last ? dec(g[6].z) : 0.f));
      s0 += dppf<XOR8>(s0); s1 += dppf<XOR8>(s1);
      s0 = xrow_sum(s0); s1 = xrow_sum(s1);
    }
    if (lane < 8) __hip_atomic_store((GAS u32*)p.out + bb * 320 + 8 * cs + q, pack2(h0 + rb(s0), h1 + rb(s1)), RLX_AGENT);
    if (bid == 216 && tid == 0) __hip_atomic_store((GAS u64*)p.ws, tag, RLX_AGENT);
    STAMP(6);
  }
  STAMP(7);
  if (bid == 216 && w == 0) { while (__builtin_amdgcn_s_memrealtime() < pst[0] + 6000) __builtin_amdgcn_s_sleep(4); }
}
}

extern "C" void kernel_launch(void* const* d_in, const int* in_sizes, int n_in, void* d_out, int out_size, void* d_ws, size_t ws_size, hipStream_t stream) {
  Params p;
  p.x = (const u16*)d_in[0]; p.K = (const u16*)d_in[1]; p.V = (const u16*)d_in[2]; p.wq = (const u16*)d_in[3]; p.wo = (const u16*)d_in[4];
  p.w1 = (const u16*)d_in[6]; p.w3 = (const u16*)d_in[7]; p.w2 = (const u16*)d_in[8];
  p.out = (u16*)d_out; p.ws = (unsigned char*)d_ws;
  hipLaunchKernelGGL(fused_block, dim3(256), dim3(NTHR), 0, stream, p);
}
```

```cpp
#define PROBE 10
#include <hip/hip_runtime.h>
#include <stdint.h>

typedef unsigned short u16;
typedef unsigned u32;
typedef unsigned long long u64;
typedef u32 v4u __attribute__((ext_vector_type(4)));
typedef u32 v2u __attribute__((ext_vector_type(2)));
typedef float f4 __attribute__((ext_vector_type(4)));
typedef float f2 __attribute__((ext_vector_type(2)));
typedef __bf16 bf2 __attribute__((ext_vector_type(2)));
typedef __bf16 bf8 __attribute__((ext_vector_type(8)));
#define GAS __attribute__((address_space(1)))
#define LAS __attribute__((address_space(3)))
#define RLX_AGENT __ATOMIC_RELAXED, __HIP_MEMORY_SCOPE_AGENT

#define RD 16
#define STAMPS 0

namespace {
constexpr int DM = 640, NH = 32, DH = 128, SK = 8192, WIN = 2048, FF = 1728;
constexpr int NTHR = 512;
constexpr int AUX_NT = 2, AUX_SC1 = 16, RSRC_W3 = 0x00020000;
constexpr int OFF_X1 = 4096;
constexpr int X1_STRIDE = 136 * 8;
constexpr int OFF_X2 = 0x80000;
constexpr int OFF_X3 = 0x130000;
constexpr int OFF_X4 = 0x140000;
constexpr int NFU = 54;
constexpr int WS_USED = 0x280000;
constexpr size_t OFF_STAMP = 8u << 20;
constexpr u32 SPIN_CAP = 1u << 16;
constexpr float LOG2E = 1.4426950408889634f;

struct Params {
  const u16 *x, *K, *V, *wq, *wo, *w1, *w3, *w2;
  u16* out;
  unsigned char* ws;
};

__device__ __forceinline__ float rb(float f) { return (float)(__bf16)f; }
__device__ __forceinline__ u32 pack2(float lo, float hi) { bf2 v = {(__bf16)lo, (__bf16)hi}; return __builtin_bit_cast(u32, v); }
__device__ __forceinline__ float blo(u32 v) { return __uint_as_float(v << 16); }
__device__ __forceinline__ float bhi(u32 v) { return __uint_as_float(v & 0xffff0000u); }
__device__ __forceinline__ float dot2(u32 a, u32 b, float c) {
  return __builtin_amdgcn_fdot2_f32_bf16(__builtin_bit_cast(bf2, a), __builtin_bit_cast(bf2, b), c, false);
}
__device__ __forceinline__ f4 mfma(v4u a, v4u b, f4 c) {
  return __builtin_amdgcn_mfma_f32_16x16x32_bf16(__builtin_bit_cast(bf8, a), __builtin_bit_cast(bf8, b), c, 0, 0, 0);
}
template <int CTRL> __device__ __forceinline__ float dppf(float x) {
  return __builtin_bit_cast(float, __builtin_amdgcn_mov_dpp(__builtin_bit_cast(int, x), CTRL, 0xf, 0xf, true));
}
template <int CTRL> __device__ __forceinline__ u32 dppu(u32 x) { return (u32)__builtin_amdgcn_mov_dpp((int)x, CTRL, 0xf, 0xf, true); }
constexpr int XOR1 = 0xB1, XOR2 = 0x4E, XOR7 = 0x141, XOR8 = 0x128, XOR15 = 0x140;
__device__ __forceinline__ float xrow_sum(float x) {
  auto s = __builtin_amdgcn_permlane16_swap(__float_as_uint(x), __float_as_uint(x), false, false);
  x = __uint_as_float(s[0]) + __uint_as_float(s[1]);
  auto t = __builtin_amdgcn_permlane32_swap(__float_as_uint(x), __float_as_uint(x), false, false);
  return __uint_as_float(t[0]) + __uint_as_float(t[1]);
}
__device__ __forceinline__ float xrow_max(float x) {
  auto s = __builtin_amdgcn_permlane16_swap(__float_as_uint(x), __float_as_uint(x), false, false);
  x = fmaxf(__uint_as_float(s[0]), __uint_as_float(s[1]));
  auto t = __builtin_amdgcn_permlane32_swap(__float_as_uint(x), __float_as_uint(x), false, false);
  return fmaxf(__uint_as_float(t[0]), __uint_as_float(t[1]));
}
__device__ __forceinline__ float wave_sum(float x) {
  x += dppf<XOR1>(x); x += dppf<XOR2>(x); x += dppf<XOR7>(x); x += dppf<XOR15>(x);
  return xrow_sum(x);
}
__device__ __forceinline__ float wave_max(float x) {
  x = fmaxf(x, dppf<XOR1>(x)); x = fmaxf(x, dppf<XOR2>(x)); x = fmaxf(x, dppf<XOR7>(x)); x = fmaxf(x, dppf<XOR15>(x));
  return xrow_max(x);
}
__device__ __forceinline__ v4u ld_nt(__amdgpu_buffer_rsrc_t r, int voff, int soff) { return __builtin_amdgcn_raw_buffer_load_b128(r, voff, soff, AUX_NT); }
__device__ __forceinline__ v4u ld_sc1(__amdgpu_buffer_rsrc_t r, int voff) { return __builtin_amdgcn_raw_buffer_load_b128(r, voff, 0, AUX_SC1); }
__device__ __forceinline__ void st_sc1(__amdgpu_buffer_rsrc_t r, int voff, v4u v) { __builtin_amdgcn_raw_buffer_store_b128(v, r, voff, 0, AUX_SC1); }
__device__ __forceinline__ __amdgpu_buffer_rsrc_t mkrsrc(const void* p, int bytes) { return __builtin_amdgcn_make_buffer_rsrc((void*)p, 0, bytes, RSRC_W3); }
__device__ __forceinline__ v4u enc2(float a, float b, u32 tlo, u32 thi) {
  v4u g;
  g.x = __builtin_amdgcn_perm(tlo, __float_as_uint(a), 0x04030201u); g.y = thi;
  g.z = __builtin_amdgcn_perm(tlo, __float_as_uint(b), 0x04030201u); g.w = thi;
  return g;
}
__device__ __forceinline__ bool ok2(v4u g, u32 tlo, u32 thi) { return g.y == thi && g.w == thi && (g.x >> 24) == tlo && (g.z >> 24) == tlo; }
__device__ __forceinline__ float dec(u32 w) { return __uint_as_float(w << 8); }

__device__ __forceinline__ void layernorm5(const u32 (&xr)[5], u32 (&ln)[5]) {
  float v[10], s = 0.f;
#pragma unroll
  for (int k = 0; k < 5; ++k) { v[2 * k] = blo(xr[k]); v[2 * k + 1] = bhi(xr[k]); s += v[2 * k] + v[2 * k + 1]; }
  const float mean = wave_sum(s) * (1.0f / DM);
  float q = 0.f;
#pragma unroll
  for (int k = 0; k < 10; ++k) { const float d = v[k] - mean; q = fmaf(d, d, q); }
  const float var = wave_sum(q) * (1.0f / DM);
  const float mb = rb(mean), t = rb(rb(var) + 1e-5f), r = rb(__builtin_amdgcn_sqrtf(t)), ri = __builtin_amdgcn_rcpf(r);
#pragma unroll
  for (int k = 0; k < 5; ++k) ln[k] = pack2(rb(v[2 * k] - mb) * ri, rb(v[2 * k + 1] - mb) * ri);
}
__device__ __forceinline__ u32 mksel(int j, int k, int half, bool act) {
  const u32 src = half ? 0x0302u : 0x0100u;
  const u32 s = (j & 1) ? ((src << 16) | 0x0c0cu) : (0x0c0c0000u | src);
  return (act && (j >> 1) == k) ? s : 0x0c0c0c0cu;
}
#define STAMP(n) do { pst[n] = __builtin_amdgcn_s_memrealtime(); } while (0)
#define SB() __builtin_amdgcn_sched_barrier(0)

constexpr int L_LN = 0;
constexpr int L_RED = 8 * 1280;
constexpr int L_MISC = L_RED + 8 * 2048;
constexpr int L_BYTES = L_MISC + 4096;

__global__ void __launch_bounds__(NTHR, 2) fused_block(Params p) {
  __shared__ __attribute__((aligned(16))) unsigned char smem[L_BYTES];
  LAS unsigned char* L = (LAS unsigned char*)smem;
  const int tid = threadIdx.x, lane = tid & 63, w = __builtin_amdgcn_readfirstlane(tid >> 6);
  const int bid = blockIdx.x;
  const int r = lane >> 4, c = lane & 15;
  u64 pst[16] = {};
  STAMP(0);
  if (tid == 0) __hip_atomic_store((GAS u64*)(p.ws + (12u << 20)) + bid, pst[0], RLX_AGENT);
  const __amdgpu_buffer_rsrc_t wsr = mkrsrc(p.ws, WS_USED);
  const int h = (bid & 7) + 8 * (bid >> 6), bs = (bid >> 3) & 7, b = bs >> 1, s = bs & 1;
  const size_t kvoff = ((size_t)(b * NH + h) * SK + (SK - WIN) + s * 1024) * DH;
  const __amdgpu_buffer_rsrc_t kr = mkrsrc(p.K + kvoff, 1024 * DH * 2), vr = mkrsrc(p.V + kvoff, 1024 * DH * 2);
  const __amdgpu_buffer_rsrc_t qr = mkrsrc(p.wq + h * DH, DM * NH * DH * 2 - h * DH * 2);
  const u64 epoch = __hip_atomic_load((GAS u64*)p.ws, RLX_AGENT);
  u32 xr[5];
#pragma unroll
  for (int k = 0; k < 5; ++k) xr[k] = ((const GAS u32*)p.x)[b * 320 + lane + 64 * k];
  SB();
  v4u wqv[20];
  {
    const int vo = (80 * w + 20 * r) * (NH * DH * 2) + c * 16;
#pragma unroll
    for (int i = 0; i < 20; ++i) wqv[i] = ld_nt(qr, vo, i * (NH * DH * 2));
  }
  SB();
  v4u ring[RD];
  const int kvo = w * 1024 + lane * 16;
#pragma unroll
  for (int n = 0; n < RD; ++n) ring[n] = ld_nt(kr, kvo, n * 8192);
  SB();
  const u64 tag = epoch + 1;
  const u32 tlo = __builtin_amdgcn_readfirstlane((u32)tag & 0xffu), thi = __builtin_amdgcn_readfirstlane((u32)(tag >> 8));
  const u32 t32 = __builtin_amdgcn_readfirstlane((u32)tag);
  u32 selA[4], selB[4];
#pragma unroll
  for (int k = 0; k < 4; ++k) { selA[k] = mksel(c & 7, k, 0, c < 8); selB[k] = mksel(c & 7, k, 1, c < 8); }
  {
    u32 ln[5];
    layernorm5(xr, ln);
    LAS u32* lnw = (LAS u32*)(L + L_LN + w * 1280);
#pragma unroll
    for (int k = 0; k < 5; ++k) lnw[lane + 64 * k] = ln[k];
    v2u lv[5];
#pragma unroll
    for (int k = 0; k < 5; ++k) lv[k] = *(LAS v2u*)(lnw + 40 * w + 10 * r + 2 * k);
    f4 acc = {0.f, 0.f, 0.f, 0.f};
#pragma unroll
    for (int i = 0; i < 20; ++i) {
      const u32 x = (i & 2) ? lv[i >> 2].y : lv[i >> 2].x;
      v4u a;
#pragma unroll
      for (int k = 0; k < 4; ++k) a[k] = __builtin_amdgcn_perm(x, x, (i & 1) ? selB[k] : selA[k]);
      acc = mfma(a, wqv[i], acc);
    }
    if (lane < 32) *(LAS f4*)(L + L_RED + w * 2048 + (8 * c + 4 * r) * 4) = acc;
  }
  __syncthreads();
  if (tid < 64) {
    f2 sum = {0.f, 0.f};
#pragma unroll
    for (int ww = 0; ww < 8; ++ww) sum += *(LAS f2*)(L + L_RED + ww * 2048 + lane * 8);
    *(LAS u32*)(L + L_MISC + lane * 4) = pack2(sum.x, sum.y);
  }
  __syncthreads();
  const v4u qv = *(LAS v4u*)(L + L_MISC + c * 16);
  STAMP(1);
  float sc[2];
#pragma unroll
  for (int g = 0; g < 2; ++g) {
    float x[16];
#pragma unroll
    for (int i = 0; i < 16; ++i) {
      const int n = 16 * g + i;
      const v4u kk = ring[n % RD];
      x[i] = dot2(kk.w, qv.w, dot2(kk.z, qv.z, dot2(kk.y, qv.y, dot2(kk.x, qv.x, 0.f))));
      const int nn = n + RD;
      if (nn < 32) ring[n % RD] = ld_nt(kr, kvo, nn * 8192);
      else if (nn < 64) ring[n % RD] = ld_nt(vr, kvo, (nn - 32) * 8192);
      SB();
    }
#define TR_STEP(HALF, CTRL, BIT) _Pragma("unroll") for (int hh = 0; hh < (HALF); ++hh) { \
      const float lo_ = x[hh], hi_ = x[hh + (HALF)]; const float t1_ = lo_ + dppf<CTRL>(lo_), t2_ = hi_ + dppf<CTRL>(hi_); x[hh] = (c & (BIT)) ? t2_ : t1_; }
    TR_STEP(8, XOR8, 8) TR_STEP(4, XOR7, 4) TR_STEP(2, XOR2, 2) TR_STEP(1, XOR1, 1)
#undef TR_STEP
    sc[g] = rb(rb(x[0]) * 0.08838834764831845f);
  }
  STAMP(2);
  LAS float* mbuf = (LAS float*)(L + L_MISC + 256);
  LAS float* lbuf = mbuf + 8;
  {
    const float mw = wave_max(fmaxf(sc[0], sc[1]));
    if (lane == 0) mbuf[w] = mw;
  }
  __syncthreads();
  float M;
  { const f4 a = *(LAS f4*)mbuf, bb = *(LAS f4*)(mbuf + 4);
    M = fmaxf(fmaxf(fmaxf(a.x, a.y), fmaxf(a.z, a.w)), fmaxf(fmaxf(bb.x, bb.y), fmaxf(bb.z, bb.w))); }
  float pe[2];
  pe[0] = __builtin_amdgcn_exp2f((sc[0] - M) * LOG2E); pe[1] = __builtin_amdgcn_exp2f((sc[1] - M) * LOG2E);
  { const float lw = wave_sum(pe[0] + pe[1]); if (lane == 0) lbuf[w] = lw; }
  __syncthreads();
  float Lsum;
  { const f4 a = *(LAS f4*)lbuf, bb = *(LAS f4*)(lbuf + 4);
    Lsum = ((a.x + a.y) + (a.z + a.w)) + ((bb.x + bb.y) + (bb.z + bb.w)); }
  const float linv = __builtin_amdgcn_rcpf(Lsum);
  u32 pb[2];
  pb[0] = pack2(pe[0] * linv, 0.f); pb[1] = pack2(pe[1] * linv, 0.f);
  const int x1p = OFF_X1 + (h * 8 + bs) * X1_STRIDE;
  if (tid == 0) { const float lse = M + __builtin_amdgcn_logf(Lsum) * 0.6931471805599453f; st_sc1(wsr, x1p + 128 * 8, enc2(lse, lse, tlo, thi)); }
  {
    f4 acc = {0.f, 0.f, 0.f, 0.f};
#define PV_STEP(i) { const v4u vv = ring[(32 + (i)) % RD]; \
      const u32 x = (u32)__builtin_amdgcn_update_dpp(0, (int)pb[(i) >> 4], 0x150 + ((i) & 15), 0xf, 0xf, false); \
      v4u a; a[0] = __builtin_amdgcn_perm(x, x, selA[0]); a[1] = __builtin_amdgcn_perm(x, x, selA[1]); a[2] = __builtin_amdgcn_perm(x, x, selA[2]); a[3] = __builtin_amdgcn_perm(x, x, selA[3]); \
      acc = mfma(a, vv, acc); \
      if ((i) + 32 + RD < 64) ring[(32 + (i)) % RD] = ld_nt(vr, kvo, ((i) + RD) * 8192); \
      SB(); }
#define PV4(i) PV_STEP(i) PV_STEP((i) + 1) PV_STEP((i) + 2) PV_STEP((i) + 3)
    PV4(0) PV4(4) PV4(8) PV4(12) PV4(16) PV4(20) PV4(24) PV4(28)
    if (lane < 32) *(LAS f4*)(L + L_RED + w * 2048 + (8 * c + 4 * r) * 4) = acc;
  }
  STAMP(3);
  __syncthreads();
  if (tid < 64) {
    f2 sum = {0.f, 0.f};
#pragma unroll
    for (int ww = 0; ww < 8; ++ww) sum += *(LAS f2*)(L + L_RED + ww * 2048 + lane * 8);
    st_sc1(wsr, x1p + lane * 16, enc2(sum.x, sum.y, tlo, thi));
  }
  STAMP(4);
  if (bid < 160) {
    const int hh = bid / 5, j = bid - 5 * hh;
    const __amdgpu_buffer_rsrc_t orr = mkrsrc(p.wo + (size_t)hh * DH * DM + 128 * j, DH * DM * 2 - 256 * j);
    v4u wv[4];
    { const int vo = (16 * w + r) * (DM * 2) + c * 16;
#pragma unroll
      for (int i = 0; i < 4; ++i) wv[i] = ld_nt(orr, vo, i * 4 * DM * 2); }
    SB();
    u32 sel[4];
#pragma unroll
    for (int k = 0; k < 4; ++k) sel[k] = mksel(lane & 7, k, r & 1, true);
    const int pp = lane >> 3, q = lane & 7;
    const int go = OFF_X1 + (hh * 8 + pp) * X1_STRIDE;
    v4u g0, g1;
    for (u32 spin = 0;; ++spin) {
      asm volatile("" ::: "memory");
      g0 = ld_sc1(wsr, go + (16 * w + 2 * q) * 8);
      g1 = ld_sc1(wsr, go + 128 * 8);
      if (__all(ok2(g0, tlo, thi) && ok2(g1, tlo, thi)) || spin > SPIN_CAP) break;
    }
    STAMP(5);
    const float lse = dec(g1.x), lso = dppf<XOR8>(lse);
    const float wt = __builtin_amdgcn_rcpf(1.0f + __builtin_amdgcn_exp2f((lso - lse) * LOG2E));
    float o0 = wt * dec(g0.x), o1 = wt * dec(g0.z);
    o0 += dppf<XOR8>(o0); o1 += dppf<XOR8>(o1);
    const u32 ob = pack2(o0, o1);
    f4 acc[2] = {{0.f, 0.f, 0.f, 0.f}, {0.f, 0.f, 0.f, 0.f}};
#pragma unroll
    for (int i = 0; i < 4; ++i)
#pragma unroll
      for (int P = 0; P < 2; ++P) {
        const int bb = 2 * P + ((lane >> 3) & 1);
        const u32 x = (u32)__builtin_amdgcn_ds_bpermute((16 * bb + 2 * i + (r >> 1)) * 4, (int)ob);
        v4u a;
#pragma unroll
        for (int k = 0; k < 4; ++k) a[k] = __builtin_amdgcn_perm(x, x, sel[k]);
        acc[P] = mfma(a, wv[i], acc[P]);
      }
    __syncthreads();
#pragma unroll
    for (int P = 0; P < 2; ++P) *(LAS f4*)(L + L_RED + w * 2048 + P * 1024 + lane * 16) = acc[P];
    __syncthreads();
    if (tid < 256) {
      const int bb = w, col = 2 * lane;
      const int P = bb >> 1, g = 2 * (bb & 1) + ((col >> 2) & 1), cc = col >> 3, reg = col & 3;
      f2 sum = {0.f, 0.f};
#pragma unroll
      for (int ww = 0; ww < 8; ++ww) sum += *(LAS f2*)(L + L_RED + ww * 2048 + P * 1024 + (16 * g + cc) * 16 + reg * 4);
      const int cs = 8 * j + (col >> 4);
      st_sc1(wsr, OFF_X2 + ((cs * 4 + bb) * 32 + hh) * 128 + (col & 15) * 8, enc2(sum.x, sum.y, tlo, thi));
    }
    STAMP(6);
  } else if (bid < 216) {
    const int k8 = bid - 160, xc = k8 & 7, m = k8 >> 3;
    const int u = m < 6 ? 2 * (xc + 8 * (m >> 1)) + (m & 1) : 48 + xc;
    if (u < NFU) {
      const int g = lane >> 4, rsub = (lane >> 3) & 1, mat = (lane >> 2) & 1, cc = lane & 3;
      v4u wa[10];
      { const GAS u16* base = (const GAS u16*)(mat ? p.w3 : p.w1) + (size_t)(80 * w + 2 * g + rsub) * FF + 32 * u + 8 * cc;
#pragma unroll
        for (int i = 0; i < 10; ++i) wa[i] = __builtin_nontemporal_load((const GAS v4u*)(base + (size_t)i * 8 * FF)); }
      v4u wb[8];
      const __amdgpu_buffer_rsrc_t w2r = mkrsrc(p.w2 + (size_t)32 * u * DM, 32 * DM * 2);
      if (w < 5) {
        const int vo = r * (DM * 2) + w * 256 + c * 16;
#pragma unroll
        for (int i = 0; i < 8; ++i) wb[i] = ld_nt(w2r, vo, i * 4 * DM * 2);
      }
      SB();
      LAS u32* ln2 = (LAS u32*)(L + L_LN);
      if (w < 4) {
        u32 hr[5];
        const GAS u64* hs = (const GAS u64*)(p.ws + OFF_X3) + w * 320 + lane;
        for (u32 spin = 0;; ++spin) {
      asm volatile("" ::: "memory");
          bool ok = true;
#pragma unroll
          for (int k = 0; k < 5; ++k) { const u64 gq = __hip_atomic_load(hs + 64 * k, RLX_AGENT); hr[k] = (u32)gq; ok &= (u32)(gq >> 32) == t32; }
          if (__all(ok) || spin > SPIN_CAP) break;
        }
        STAMP(5);
        u32 ln[5];
        layernorm5(hr, ln);
#pragma unroll
        for (int k = 0; k < 5; ++k) ln2[(lane + 64 * k) * 4 + w] = ln[k];
      }
      __syncthreads();
      u32 sel[4];
#pragma unroll
      for (int k = 0; k < 4; ++k) sel[k] = mksel(lane & 7, k, rsub, true);
      f4 acc[4];
#pragma unroll
      for (int bb = 0; bb < 4; ++bb) acc[bb] = f4{0.f, 0.f, 0.f, 0.f};
#pragma unroll
      for (int i = 0; i < 10; ++i) {
        const v4u xs = *(LAS v4u*)(ln2 + (40 * w + 4 * i + g) * 4);
#pragma unroll
        for (int bb = 0; bb < 4; ++bb) {
          v4u a;
#pragma unroll
          for (int k = 0; k < 4; ++k) a[k] = __builtin_amdgcn_perm(xs[bb], xs[bb], sel[k]);
          acc[bb] = mfma(a, wa[i], acc[bb]);
        }
      }
      if ((lane >> 5) == rsub) {
#pragma unroll
        for (int bb = 0; bb < 4; ++bb)
          *(LAS f4*)(L + L_RED + w * 2048 + bb * 512 + rsub * 256 + mat * 128 + (8 * cc + 4 * (g & 1)) * 4) = acc[bb];
      }
      __syncthreads();
      LAS u16* hhs = (LAS u16*)(L + L_MISC + 512);
      if (tid < 128) {
        const int bb = tid >> 5, f = tid & 31;
        float gs = 0.f, us = 0.f;
#pragma unroll
        for (int ww = 0; ww < 8; ++ww) {
          const LAS float* base = (const LAS float*)(L + L_RED + ww * 2048 + bb * 512) + f;
          gs += base[0] + base[64]; us += base[32] + base[96];
        }
        gs = rb(gs); us = rb(us);
        const float si = rb(gs * __builtin_amdgcn_rcpf(1.0f + __builtin_amdgcn_exp2f(-gs * LOG2E)));
        hhs[f * 4 + bb] = (u16)(pack2(si * us, 0.f) & 0xffffu);
      }
      __syncthreads();
      if (w < 5) {
        const int bsel = (lane >> 3) & 1;
        u32 s2[4];
#pragma unroll
        for (int k = 0; k < 4; ++k) s2[k] = mksel(lane & 7, k, bsel, true);
        f4 d[2] = {{0.f, 0.f, 0.f, 0.f}, {0.f, 0.f, 0.f, 0.f}};
#pragma unroll
        for (int i = 0; i < 8; ++i) {
          const v2u xs = *(LAS v2u*)(hhs + (4 * i + r) * 4);
#pragma unroll
          for (int P = 0; P < 2; ++P) {
            const u32 x = P ? xs.y : xs.x;
            v4u a;
#pragma unroll
            for (int k = 0; k < 4; ++k) a[k] = __builtin_amdgcn_perm(x, x, s2[k]);
            d[P] = mfma(a, wb[i], d[P]);
          }
        }
#pragma unroll
        for (int P = 0; P < 2; ++P) {
          const int bb = 2 * P + (r >> 1), cs = 8 * w + (c >> 1);
          const int o = OFF_X4 + ((cs * 4 + bb) * NFU + u) * 128 + (8 * (c & 1) + 4 * (r & 1)) * 8;
          st_sc1(wsr, o, enc2(d[P].x, d[P].y, tlo, thi));
          st_sc1(wsr, o + 16, enc2(d[P].z, d[P].w, tlo, thi));
        }
      }
      STAMP(6);
    }
  } else if (w < 4) {
    const int cs = bid - 216, bb = w;
    const int pp = lane >> 3, q = lane & 7;
    const u32 xv = ((const GAS u32*)p.x)[bb * 320 + 8 * cs + q];
    float s0, s1;
    {
      const int go = OFF_X2 + ((cs * 4 + bb) * 32 + pp) * 128 + q * 16;
      v4u g[4];
      for (u32 spin = 0;; ++spin) {
      asm volatile("" ::: "memory");
        bool ok = true;
#pragma unroll
        for (int m = 0; m < 4; ++m) g[m] = ld_sc1(wsr, go + m * 1024);
#pragma unroll
        for (int m = 0; m < 4; ++m) ok &= ok2(g[m], tlo, thi);
        if (__all(ok) || spin > SPIN_CAP) break;
      }
      s0 = (dec(g[0].x) + dec(g[1].x)) + (dec(g[2].x) + dec(g[3].x));
      s1 = (dec(g[0].z) + dec(g[1].z)) + (dec(g[2].z) + dec(g[3].z));
      s0 += dppf<XOR8>(s0); s1 += dppf<XOR8>(s1);
      s0 = xrow_sum(s0); s1 = xrow_sum(s1);
    }
    STAMP(5);
    const float h0 = rb(blo(xv) + rb(s0)), h1 = rb(bhi(xv) + rb(s1));
    if (lane < 8) __hip_atomic_store((GAS u64*)(p.ws + OFF_X3) + bb * 320 + 8 * cs + q, ((u64)t32 << 32) | pack2(h0, h1), RLX_AGENT);
    {
      const int go = OFF_X4 + ((cs * 4 + bb) * NFU + pp) * 128 + q * 16;
      v4u g[7];
      const bool last = pp < NFU - 48;
      for (u32 spin = 0;; ++spin) {
      asm volatile("" ::: "memory");
        bool ok = true;
#pragma unroll
        for (int m = 0; m < 6; ++m) g[m] = ld_sc1(wsr, go + m * 1024);
        g[6] = ld_sc1(wsr, go + (last ? 6 * 1024 : 0));
#pragma unroll
        for (int m = 0; m < 7; ++m) ok &= ok2(g[m], tlo, thi);
        if (__all(ok) || spin > SPIN_CAP) break;
      }
      s0 = ((dec(g[0].x) + dec(g[1].x)) + (dec(g[2].x) + dec(g[3].x))) + ((dec(g[4].x) + dec(g[5].x)) + (last ? dec(g[6].x) : 0.f));
      s1 = ((dec(g[0].z) + dec(g[1].z)) + (dec(g[2].z) + dec(g[3].z))) + ((dec(g[4].z) + dec(g[5].z)) + (last ? dec(g[6].z) : 0.f));
      s0 += dppf<XOR8>(s0); s1 += dppf<XOR8>(s1);
      s0 = xrow_sum(s0); s1 = xrow_sum(s1);
    }
    if (lane < 8) ((GAS u32*)p.out)[bb * 320 + 8 * cs + q] = pack2(h0 + rb(s0), h1 + rb(s1));
    if (bid == 216 && tid == 0) __hip_atomic_store((GAS u64*)p.ws, tag, RLX_AGENT);
    STAMP(6);
  }
  STAMP(7);
  if (bid == 216 && w == 0) { u64 mn = ~0ull, mx = 0;
    for (int k = 0; k < 4; ++k) { u64 t = __hip_atomic_load((GAS u64*)(p.ws + (12u << 20)) + lane + 64 * k, RLX_AGENT); mn = t < mn ? t : mn; mx = t > mx ? t : mx; }
    for (int o = 32; o > 0; o >>= 1) { u64 a = __shfl_xor(mn, o), bq = __shfl_xor(mx, o); mn = a < mn ? a : mn; mx = bq > mx ? bq : mx; }
    const u64 base = PROBE == 10 ? mn : mx;
    while (__builtin_amdgcn_s_memrealtime() < base + 6000) __builtin_amdgcn_s_sleep(4); }
}
}

extern "C" void kernel_launch(void* const* d_in, const int* in_sizes, int n_in, void* d_out, int out_size, void* d_ws, size_t ws_size, hipStream_t stream) {
  Params p;
  p.x = (const u16*)d_in[0]; p.K = (const u16*)d_in[1]; p.V = (const u16*)d_in[2]; p.wq = (const u16*)d_in[3]; p.wo = (const u16*)d_in[4];
  p.w1 = (const u16*)d_in[6]; p.w3 = (const u16*)d_in[7]; p.w2 = (const u16*)d_in[8];
  p.out = (u16*)d_out; p.ws = (unsigned char*)d_ws;
  hipLaunchKernelGGL(fused_block, dim3(256), dim3(NTHR), 0, stream, p);
}
```

```cpp
#define PROBE 12
#define KF_GRID 1
#include <hip/hip_runtime.h>
#include <stdint.h>

typedef unsigned short u16;
typedef unsigned u32;
typedef unsigned long long u64;
typedef u32 v4u __attribute__((ext_vector_type(4)));
typedef u32 v2u __attribute__((ext_vector_type(2)));
typedef float f4 __attribute__((ext_vector_type(4)));
typedef float f2 __attribute__((ext_vector_type(2)));
typedef __bf16 bf2 __attribute__((ext_vector_type(2)));
typedef __bf16 bf8 __attribute__((ext_vector_type(8)));
#define GAS __attribute__((address_space(1)))
#define LAS __attribute__((address_space(3)))
#define RLX_AGENT __ATOMIC_RELAXED, __HIP_MEMORY_SCOPE_AGENT

#define RD 16
#define STAMPS 0

namespace {
constexpr int DM = 640, NH = 32, DH = 128, SK = 8192, WIN = 2048, FF = 1728;
constexpr int NTHR = 512;
constexpr int AUX_NT = 2, AUX_SC1 = 16, RSRC_W3 = 0x00020000;
constexpr int OFF_X1 = 4096;
constexpr int X1_STRIDE = 136 * 8;
constexpr int OFF_X2 = 0x80000;
constexpr int OFF_X3 = 0x130000;
constexpr int OFF_X4 = 0x140000;
constexpr int NFU = 54;
constexpr int WS_USED = 0x280000;
constexpr size_t OFF_STAMP = 8u << 20;
constexpr u32 SPIN_CAP = 1u << 16;
constexpr float LOG2E = 1.4426950408889634f;

struct Params {
  const u16 *x, *K, *V, *wq, *wo, *w1, *w3, *w2;
  u16* out;
  unsigned char* ws;
};

__device__ __forceinline__ float rb(float f) { return (float)(__bf16)f; }
__device__ __forceinline__ u32 pack2(float lo, float hi) { bf2 v = {(__bf16)lo, (__bf16)hi}; return __builtin_bit_cast(u32, v); }
__device__ __forceinline__ float blo(u32 v) { return __uint_as_float(v << 16); }
__device__ __forceinline__ float bhi(u32 v) { return __uint_as_float(v & 0xffff0000u); }
__device__ __forceinline__ float dot2(u32 a, u32 b, float c) {
  return __builtin_amdgcn_fdot2_f32_bf16(__builtin_bit_cast(bf2, a), __builtin_bit_cast(bf2, b), c, false);
}
__device__ __forceinline__ f4 mfma(v4u a, v4u b, f4 c) {
  return __builtin_amdgcn_mfma_f32_16x16x32_bf16(__builtin_bit_cast(bf8, a), __builtin_bit_cast(bf8, b), c, 0, 0, 0);
}
template <int CTRL> __device__ __forceinline__ float dppf(float x) {
  return __builtin_bit_cast(float, __builtin_amdgcn_mov_dpp(__builtin_bit_cast(int, x), CTRL, 0xf, 0xf, true));
}
template <int CTRL> __device__ __forceinline__ u32 dppu(u32 x) { return (u32)__builtin_amdgcn_mov_dpp((int)x, CTRL, 0xf, 0xf, true); }
constexpr int XOR1 = 0xB1, XOR2 = 0x4E, XOR7 = 0x141, XOR8 = 0x128, XOR15 = 0x140;
__device__ __forceinline__ float xrow_sum(float x) {
  auto s = __builtin_amdgcn_permlane16_swap(__float_as_uint(x), __float_as_uint(x), false, false);
  x = __uint_as_float(s[0]) + __uint_as_float(s[1]);
  auto t = __builtin_amdgcn_permlane32_swap(__float_as_uint(x), __float_as_uint(x), false, false);
  return __uint_as_float(t[0]) + __uint_as_float(t[1]);
}
__device__ __forceinline__ float xrow_max(float x) {
  auto s = __builtin_amdgcn_permlane16_swap(__float_as_uint(x), __float_as_uint(x), false, false);
  x = fmaxf(__uint_as_float(s[0]), __uint_as_float(s[1]));
  auto t = __builtin_amdgcn_permlane32_swap(__float_as_uint(x), __float_as_uint(x), false, false);
  return fmaxf(__uint_as_float(t[0]), __uint_as_float(t[1]));
}
__device__ __forceinline__ float wave_sum(float x) {
  x += dppf<XOR1>(x); x += dppf<XOR2>(x); x += dppf<XOR7>(x); x += dppf<XOR15>(x);
  return xrow_sum(x);
}
__device__ __forceinline__ float wave_max(float x) {
  x = fmaxf(x, dppf<XOR1>(x)); x = fmaxf(x, dppf<XOR2>(x)); x = fmaxf(x, dppf<XOR7>(x)); x = fmaxf(x, dppf<XOR15>(x));
  return xrow_max(x);
}
__device__ __forceinline__ v4u ld_nt(__amdgpu_buffer_rsrc_t r, int voff, int soff) { return __builtin_amdgcn_raw_buffer_load_b128(r, voff, soff, AUX_NT); }
__device__ __forceinline__ v4u ld_sc1(__amdgpu_buffer_rsrc_t r, int voff) { return __builtin_amdgcn_raw_buffer_load_b128(r, voff, 0, AUX_SC1); }
__device__ __forceinline__ void st_sc1(__amdgpu_buffer_rsrc_t r, int voff, v4u v) { __builtin_amdgcn_raw_buffer_store_b128(v, r, voff, 0, AUX_SC1); }
__device__ __forceinline__ __amdgpu_buffer_rsrc_t mkrsrc(const void* p, int bytes) { return __builtin_amdgcn_make_buffer_rsrc((void*)p, 0, bytes, RSRC_W3); }
__device__ __forceinline__ v4u enc2(float a, float b, u32 tlo, u32 thi) {
  v4u g;
  g.x = __builtin_amdgcn_perm(tlo, __float_as_uint(a), 0x04030201u); g.y = thi;
  g.z = __builtin_amdgcn_perm(tlo, __float_as_uint(b), 0x04030201u); g.w = thi;
  return g;
}
__device__ __forceinline__ bool ok2(v4u g, u32 tlo, u32 thi) { return g.y == thi && g.w == thi && (g.x >> 24) == tlo && (g.z >> 24) == tlo; }
__device__ __forceinline__ float dec(u32 w) { return __uint_as_float(w << 8); }

__device__ __forceinline__ void layernorm5(const u32 (&xr)[5], u32 (&ln)[5]) {
  float v[10], s = 0.f;
#pragma unroll
  for (int k = 0; k < 5; ++k) { v[2 * k] = blo(xr[k]); v[2 * k + 1] = bhi(xr[k]); s += v[2 * k] + v[2 * k + 1]; }
  const float mean = wave_sum(s) * (1.0f / DM);
  float q = 0.f;
#pragma unroll
  for (int k = 0; k < 10; ++k) { const float d = v[k] - mean; q = fmaf(d, d, q); }
  const float var = wave_sum(q) * (1.0f / DM);
  const float mb = rb(mean), t = rb(rb(var) + 1e-5f), r = rb(__builtin_amdgcn_sqrtf(t)), ri = __builtin_amdgcn_rcpf(r);
#pragma unroll
  for (int k = 0; k < 5; ++k) ln[k] = pack2(rb(v[2 * k] - mb) * ri, rb(v[2 * k + 1] - mb) * ri);
}
__device__ __forceinline__ u32 mksel(int j, int k, int half, bool act) {
  const u32 src = half ? 0x0302u : 0x0100u;
  const u32 s = (j & 1) ? ((src << 16) | 0x0c0cu) : (0x0c0c0000u | src);
  return (act && (j >> 1) == k) ? s : 0x0c0c0c0cu;
}
#define STAMP(n) do { pst[n] = __builtin_amdgcn_s_memrealtime(); } while (0)
#define SB() __builtin_amdgcn_sched_barrier(0)

constexpr int L_LN = 0;
constexpr int L_RED = 8 * 1280;
constexpr int L_MISC = L_RED + 8 * 2048;
constexpr int L_BYTES = L_MISC + 4096;

__global__ void __launch_bounds__(NTHR, 2) fused_block(Params p) {
  __shared__ __attribute__((aligned(16))) unsigned char smem[L_BYTES];
  LAS unsigned char* L = (LAS unsigned char*)smem;
  const int tid = threadIdx.x, lane = tid & 63, w = __builtin_amdgcn_readfirstlane(tid >> 6);
  const int bid = blockIdx.x;
  const int r = lane >> 4, c = lane & 15;
  u64 pst[16] = {};
  STAMP(0);
  const __amdgpu_buffer_rsrc_t wsr = mkrsrc(p.ws, WS_USED);
  const int h = (bid & 7) + 8 * (bid >> 6), bs = (bid >> 3) & 7, b = bs >> 1, s = bs & 1;
  const size_t kvoff = ((size_t)(b * NH + h) * SK + (SK - WIN) + s * 1024) * DH;
  const __amdgpu_buffer_rsrc_t kr = mkrsrc(p.K + kvoff, 1024 * DH * 2), vr = mkrsrc(p.V + kvoff, 1024 * DH * 2);
  const __amdgpu_buffer_rsrc_t qr = mkrsrc(p.wq + h * DH, DM * NH * DH * 2 - h * DH * 2);
  const u64 epoch = __hip_atomic_load((GAS u64*)p.ws, RLX_AGENT);
  u32 xr[5];
#pragma unroll
  for (int k = 0; k < 5; ++k) xr[k] = ((const GAS u32*)p.x)[b * 320 + lane + 64 * k];
  SB();
  v4u wqv[20];
  {
    const int vo = (80 * w + 20 * r) * (NH * DH * 2) + c * 16;
#pragma unroll
    for (int i = 0; i < 20; ++i) wqv[i] = ld_nt(qr, vo, i * (NH * DH * 2));
  }
  SB();
  v4u ring[RD];
  const int kvo = w * 1024 + lane * 16;
#pragma unroll
  for (int n = 0; n < RD; ++n) ring[n] = ld_nt(kr, kvo, n * 8192);
  SB();
  const u64 tag = epoch + 1;
  const u32 tlo = __builtin_amdgcn_readfirstlane((u32)tag & 0xffu), thi = __builtin_amdgcn_readfirstlane((u32)(tag >> 8));
  const u32 t32 = __builtin_amdgcn_readfirstlane((u32)tag);
  u32 selA[4], selB[4];
#pragma unroll
  for (int k = 0; k < 4; ++k) { selA[k] = mksel(c & 7, k, 0, c < 8); selB[k] = mksel(c & 7, k, 1, c < 8); }
  {
    u32 ln[5];
    layernorm5(xr, ln);
    LAS u32* lnw = (LAS u32*)(L + L_LN + w * 1280);
#pragma unroll
    for (int k = 0; k < 5; ++k) lnw[lane + 64 * k] = ln[k];
    v2u lv[5];
#pragma unroll
    for (int k = 0; k < 5; ++k) lv[k] = *(LAS v2u*)(lnw + 40 * w + 10 * r + 2 * k);
    f4 acc = {0.f, 0.f, 0.f, 0.f};
#pragma unroll
    for (int i = 0; i < 20; ++i) {
      const u32 x = (i & 2) ? lv[i >> 2].y : lv[i >> 2].x;
      v4u a;
#pragma unroll
      for (int k = 0; k < 4; ++k) a[k] = __builtin_amdgcn_perm(x, x, (i & 1) ? selB[k] : selA[k]);
      acc = mfma(a, wqv[i], acc);
    }
    if (lane < 32) *(LAS f4*)(L + L_RED + w * 2048 + (8 * c + 4 * r) * 4) = acc;
  }
  __syncthreads();
  if (tid < 64) {
    f2 sum = {0.f, 0.f};
#pragma unroll
    for (int ww = 0; ww < 8; ++ww) sum += *(LAS f2*)(L + L_RED + ww * 2048 + lane * 8);
    *(LAS u32*)(L + L_MISC + lane * 4) = pack2(sum.x, sum.y);
  }
  __syncthreads();
  const v4u qv = *(LAS v4u*)(L + L_MISC + c * 16);
  STAMP(1);
  float sc[2];
#pragma unroll
  for (int g = 0; g < 2; ++g) {
    float x[16];
#pragma unroll
    for (int i = 0; i < 16; ++i) {
      const int n = 16 * g + i;
      const v4u kk = ring[n % RD];
      x[i] = dot2(kk.w, qv.w, dot2(kk.z, qv.z, dot2(kk.y, qv.y, dot2(kk.x, qv.x, 0.f))));
      const int nn = n + RD;
      if (nn < 32) ring[n % RD] = ld_nt(kr, kvo, nn * 8192);
      else if (nn < 64) ring[n % RD] = ld_nt(vr, kvo, (nn - 32) * 8192);
      SB();
    }
#define TR_STEP(HALF, CTRL, BIT) _Pragma("unroll") for (int hh = 0; hh < (HALF); ++hh) { \
      const float lo_ = x[hh], hi_ = x[hh + (HALF)]; const float t1_ = lo_ + dppf<CTRL>(lo_), t2_ = hi_ + dppf<CTRL>(hi_); x[hh] = (c & (BIT)) ? t2_ : t1_; }
    TR_STEP(8, XOR8, 8) TR_STEP(4, XOR7, 4) TR_STEP(2, XOR2, 2) TR_STEP(1, XOR1, 1)
#undef TR_STEP
    sc[g] = rb(rb(x[0]) * 0.08838834764831845f);
  }
  STAMP(2);
  LAS float* mbuf = (LAS float*)(L + L_MISC + 256);
  LAS float* lbuf = mbuf + 8;
  {
    const float mw = wave_max(fmaxf(sc[0], sc[1]));
    if (lane == 0) mbuf[w] = mw;
  }
  __syncthreads();
  float M;
  { const f4 a = *(LAS f4*)mbuf, bb = *(LAS f4*)(mbuf + 4);
    M = fmaxf(fmaxf(fmaxf(a.x, a.y), fmaxf(a.z, a.w)), fmaxf(fmaxf(bb.x, bb.y), fmaxf(bb.z, bb.w))); }
  float pe[2];
  pe[0] = __builtin_amdgcn_exp2f((sc[0] - M) * LOG2E); pe[1] = __builtin_amdgcn_exp2f((sc[1] - M) * LOG2E);
  { const float lw = wave_sum(pe[0] + pe[1]); if (lane == 0) lbuf[w] = lw; }
  __syncthreads();
  float Lsum;
  { const f4 a = *(LAS f4*)lbuf, bb = *(LAS f4*)(lbuf + 4);
    Lsum = ((a.x + a.y) + (a.z + a.w)) + ((bb.x + bb.y) + (bb.z + bb.w)); }
  const float linv = __builtin_amdgcn_rcpf(Lsum);
  u32 pb[2];
  pb[0] = pack2(pe[0] * linv, 0.f); pb[1] = pack2(pe[1] * linv, 0.f);
  const int x1p = OFF_X1 + (h * 8 + bs) * X1_STRIDE;
  if (tid == 0) { const float lse = M + __builtin_amdgcn_logf(Lsum) * 0.6931471805599453f; st_sc1(wsr, x1p + 128 * 8, enc2(lse, lse, tlo, thi)); }
  {
    f4 acc = {0.f, 0.f, 0.f, 0.f};
#define PV_STEP(i) { const v4u vv = ring[(32 + (i)) % RD]; \
      const u32 x = (u32)__builtin_amdgcn_update_dpp(0, (int)pb[(i) >> 4], 0x150 + ((i) & 15), 0xf, 0xf, false); \
      v4u a; a[0] = __builtin_amdgcn_perm(x, x, selA[0]); a[1] = __builtin_amdgcn_perm(x, x, selA[1]); a[2] = __builtin_amdgcn_perm(x, x, selA[2]); a[3] = __builtin_amdgcn_perm(x, x, selA[3]); \
      acc = mfma(a, vv, acc); \
      if ((i) + 32 + RD < 64) ring[(32 + (i)) % RD] = ld_nt(vr, kvo, ((i) + RD) * 8192); \
      SB(); }
#define PV4(i) PV_STEP(i) PV_STEP((i) + 1) PV_STEP((i) + 2) PV_STEP((i) + 3)
    PV4(0) PV4(4) PV4(8) PV4(12) PV4(16) PV4(20) PV4(24) PV4(28)
    if (lane < 32) *(LAS f4*)(L + L_RED + w * 2048 + (8 * c + 4 * r) * 4) = acc;
  }
  STAMP(3);
  __syncthreads();
  if (tid < 64) {
    f2 sum = {0.f, 0.f};
#pragma unroll
    for (int ww = 0; ww < 8; ++ww) sum += *(LAS f2*)(L + L_RED + ww * 2048 + lane * 8);
    st_sc1(wsr, x1p + lane * 16, enc2(sum.x, sum.y, tlo, thi));
  }
  STAMP(4);
  if (bid < 160) {
    const int hh = bid / 5, j = bid - 5 * hh;
    const __amdgpu_buffer_rsrc_t orr = mkrsrc(p.wo + (size_t)hh * DH * DM + 128 * j, DH * DM * 2 - 256 * j);
    v4u wv[4];
    { const int vo = (16 * w + r) * (DM * 2) + c * 16;
#pragma unroll
      for (int i = 0; i < 4; ++i) wv[i] = ld_nt(orr, vo, i * 4 * DM * 2); }
    SB();
    u32 sel[4];
#pragma unroll
    for (int k = 0; k < 4; ++k) sel[k] = mksel(lane & 7, k, r & 1, true);
    const int pp = lane >> 3, q = lane & 7;
    const int go = OFF_X1 + (hh * 8 + pp) * X1_STRIDE;
    v4u g0, g1;
    for (u32 spin = 0;; ++spin) {
      asm volatile("" ::: "memory");
      g0 = ld_sc1(wsr, go + (16 * w + 2 * q) * 8);
      g1 = ld_sc1(wsr, go + 128 * 8);
      if (__all(ok2(g0, tlo, thi) && ok2(g1, tlo, thi)) || spin > SPIN_CAP) break;
    }
    STAMP(5);
    const float lse = dec(g1.x), lso = dppf<XOR8>(lse);
    const float wt = __builtin_amdgcn_rcpf(1.0f + __builtin_amdgcn_exp2f((lso - lse) * LOG2E));
    float o0 = wt * dec(g0.x), o1 = wt * dec(g0.z);
    o0 += dppf<XOR8>(o0); o1 += dppf<XOR8>(o1);
    const u32 ob = pack2(o0, o1);
    f4 acc[2] = {{0.f, 0.f, 0.f, 0.f}, {0.f, 0.f, 0.f, 0.f}};
#pragma unroll
    for (int i = 0; i < 4; ++i)
#pragma unroll
      for (int P = 0; P < 2; ++P) {
        const int bb = 2 * P + ((lane >> 3) & 1);
        const u32 x = (u32)__builtin_amdgcn_ds_bpermute((16 * bb + 2 * i + (r >> 1)) * 4, (int)ob);
        v4u a;
#pragma unroll
        for (int k = 0; k < 4; ++k) a[k] = __builtin_amdgcn_perm(x, x, sel[k]);
        acc[P] = mfma(a, wv[i], acc[P]);
      }
    __syncthreads();
#pragma unroll
    for (int P = 0; P < 2; ++P) *(LAS f4*)(L + L_RED + w * 2048 + P * 1024 + lane * 16) = acc[P];
    __syncthreads();
    if (tid < 256) {
      const int bb = w, col = 2 * lane;
      const int P = bb >> 1, g = 2 * (bb & 1) + ((col >> 2) & 1), cc = col >> 3, reg = col & 3;
      f2 sum = {0.f, 0.f};
#pragma unroll
      for (int ww = 0; ww < 8; ++ww) sum += *(LAS f2*)(L + L_RED + ww * 2048 + P * 1024 + (16 * g + cc) * 16 + reg * 4);
      const int cs = 8 * j + (col >> 4);
      st_sc1(wsr, OFF_X2 + ((cs * 4 + bb) * 32 + hh) * 128 + (col & 15) * 8, enc2(sum.x, sum.y, tlo, thi));
    }
    STAMP(6);
  } else if (bid < 216) {
    const int k8 = bid - 160, xc = k8 & 7, m = k8 >> 3;
    const int u = m < 6 ? 2 * (xc + 8 * (m >> 1)) + (m & 1) : 48 + xc;
    if (u < NFU) {
      const int g = lane >> 4, rsub = (lane >> 3) & 1, mat = (lane >> 2) & 1, cc = lane & 3;
      v4u wa[10];
      { const GAS u16* base = (const GAS u16*)(mat ? p.w3 : p.w1) + (size_t)(80 * w + 2 * g + rsub) * FF + 32 * u + 8 * cc;
#pragma unroll
        for (int i = 0; i < 10; ++i) wa[i] = __builtin_nontemporal_load((const GAS v4u*)(base + (size_t)i * 8 * FF)); }
      v4u wb[8];
      const __amdgpu_buffer_rsrc_t w2r = mkrsrc(p.w2 + (size_t)32 * u * DM, 32 * DM * 2);
      if (w < 5) {
        const int vo = r * (DM * 2) + w * 256 + c * 16;
#pragma unroll
        for (int i = 0; i < 8; ++i) wb[i] = ld_nt(w2r, vo, i * 4 * DM * 2);
      }
      SB();
      LAS u32* ln2 = (LAS u32*)(L + L_LN);
      if (w < 4) {
        u32 hr[5];
        const GAS u64* hs = (const GAS u64*)(p.ws + OFF_X3) + w * 320 + lane;
        for (u32 spin = 0;; ++spin) {
      asm volatile("" ::: "memory");
          bool ok = true;
#pragma unroll
          for (int k = 0; k < 5; ++k) { const u64 gq = __hip_atomic_load(hs + 64 * k, RLX_AGENT); hr[k] = (u32)gq; ok &= (u32)(gq >> 32) == t32; }
          if (__all(ok) || spin > SPIN_CAP) break;
        }
        STAMP(5);
        u32 ln[5];
        layernorm5(hr, ln);
#pragma unroll
        for (int k = 0; k < 5; ++k) ln2[(lane + 64 * k) * 4 + w] = ln[k];
      }
      __syncthreads();
      u32 sel[4];
#pragma unroll
      for (int k = 0; k < 4; ++k) sel[k] = mksel(lane & 7, k, rsub, true);
      f4 acc[4];
#pragma unroll
      for (int bb = 0; bb < 4; ++bb) acc[bb] = f4{0.f, 0.f, 0.f, 0.f};
#pragma unroll
      for (int i = 0; i < 10; ++i) {
        const v4u xs = *(LAS v4u*)(ln2 + (40 * w + 4 * i + g) * 4);
#pragma unroll
        for (int bb = 0; bb < 4; ++bb) {
          v4u a;
#pragma unroll
          for (int k = 0; k < 4; ++k) a[k] = __builtin_amdgcn_perm(xs[bb], xs[bb], sel[k]);
          acc[bb] = mfma(a, wa[i], acc[bb]);
        }
      }
      if ((lane >> 5) == rsub) {
#pragma unroll
        for (int bb = 0; bb < 4; ++bb)
          *(LAS f4*)(L + L_RED + w * 2048 + bb * 512 + rsub * 256 + mat * 128 + (8 * cc + 4 * (g & 1)) * 4) = acc[bb];
      }
      __syncthreads();
      LAS u16* hhs = (LAS u16*)(L + L_MISC + 512);
      if (tid < 128) {
        const int bb = tid >> 5, f = tid & 31;
        float gs = 0.f, us = 0.f;
#pragma unroll
        for (int ww = 0; ww < 8; ++ww) {
          const LAS float* base = (const LAS float*)(L + L_RED + ww * 2048 + bb * 512) + f;
          gs += base[0] + base[64]; us += base[32] + base[96];
        }
        gs = rb(gs); us = rb(us);
        const float si = rb(gs * __builtin_amdgcn_rcpf(1.0f + __builtin_amdgcn_exp2f(-gs * LOG2E)));
        hhs[f * 4 + bb] = (u16)(pack2(si * us, 0.f) & 0xffffu);
      }
      __syncthreads();
      if (w < 5) {
        const int bsel = (lane >> 3) & 1;
        u32 s2[4];
#pragma unroll
        for (int k = 0; k < 4; ++k) s2[k] = mksel(lane & 7, k, bsel, true);
        f4 d[2] = {{0.f, 0.f, 0.f, 0.f}, {0.f, 0.f, 0.f, 0.f}};
#pragma unroll
        for (int i = 0; i < 8; ++i) {
          const v2u xs = *(LAS v2u*)(hhs + (4 * i + r) * 4);
#pragma unroll
          for (int P = 0; P < 2; ++P) {
            const u32 x = P ? xs.y : xs.x;
            v4u a;
#pragma unroll
            for (int k = 0; k < 4; ++k) a[k] = __builtin_amdgcn_perm(x, x, s2[k]);
            d[P] = mfma(a, wb[i], d[P]);
          }
        }
#pragma unroll
        for (int P = 0; P < 2; ++P) {
          const int bb = 2 * P + (r >> 1), cs = 8 * w + (c >> 1);
          const int o = OFF_X4 + ((cs * 4 + bb) * NFU + u) * 128 + (8 * (c & 1) + 4 * (r & 1)) * 8;
          st_sc1(wsr, o, enc2(d[P].x, d[P].y, tlo, thi));
          st_sc1(wsr, o + 16, enc2(d[P].z, d[P].w, tlo, thi));
        }
      }
      STAMP(6);
    }
  } else if (w < 4) {
    const int cs = bid - 216, bb = w;
    const int pp = lane >> 3, q = lane & 7;
    const u32 xv = ((const GAS u32*)p.x)[bb * 320 + 8 * cs + q];
    float s0, s1;
    {
      const int go = OFF_X2 + ((cs * 4 + bb) * 32 + pp) * 128 + q * 16;
      v4u g[4];
      for (u32 spin = 0;; ++spin) {
      asm volatile("" ::: "memory");
        bool ok = true;
#pragma unroll
        for (int m = 0; m < 4; ++m) g[m] = ld_sc1(wsr, go + m * 1024);
#pragma unroll
        for (int m = 0; m < 4; ++m) ok &= ok2(g[m], tlo, thi);
        if (__all(ok) || spin > SPIN_CAP) break;
      }
      s0 = (dec(g[0].x) + dec(g[1].x)) + (dec(g[2].x) + dec(g[3].x));
      s1 = (dec(g[0].z) + dec(g[1].z)) + (dec(g[2].z) + dec(g[3].z));
      s0 += dppf<XOR8>(s0); s1 += dppf<XOR8>(s1);
      s0 = xrow_sum(s0); s1 = xrow_sum(s1);
    }
    STAMP(5);
    const float h0 = rb(blo(xv) + rb(s0)), h1 = rb(bhi(xv) + rb(s1));
    if (lane < 8) __hip_atomic_store((GAS u64*)(p.ws + OFF_X3) + bb * 320 + 8 * cs + q, ((u64)t32 << 32) | pack2(h0, h1), RLX_AGENT);
    {
      const int go = OFF_X4 + ((cs * 4 + bb) * NFU + pp) * 128 + q * 16;
      v4u g[7];
      const bool last = pp < NFU - 48;
      for (u32 spin = 0;; ++spin) {
      asm volatile("" ::: "memory");
        bool ok = true;
#pragma unroll
        for (int m = 0; m < 6; ++m) g[m] = ld_sc1(wsr, go + m * 1024);
        g[6] = ld_sc1(wsr, go + (last ? 6 * 1024 : 0));
#pragma unroll
        for (int m = 0; m < 7; ++m) ok &= ok2(g[m], tlo, thi);
        if (__all(ok) || spin > SPIN_CAP) break;
      }
      s0 = ((dec(g[0].x) + dec(g[1].x)) + (dec(g[2].x) + dec(g[3].x))) + ((dec(g[4].x) + dec(g[5].x)) + (last ? dec(g[6].x) : 0.f));
      s1 = ((dec(g[0].z) + dec(g[1].z)) + (dec(g[2].z) + dec(g[3].z))) + ((dec(g[4].z) + dec(g[5].z)) + (last ? dec(g[6].z) : 0.f));
      s0 += dppf<XOR8>(s0); s1 += dppf<XOR8>(s1);
      s0 = xrow_sum(s0); s1 = xrow_sum(s1);
    }
    if (lane < 8) ((GAS u32*)p.out)[bb * 320 + 8 * cs + q] = pack2(h0 + rb(s0), h1 + rb(s1));
    if (bid == 216 && tid == 0) __hip_atomic_store((GAS u64*)p.ws, tag, RLX_AGENT);
    STAMP(6);
  }
  STAMP(7);
  if (bid == 216 && w == 0) { const u64 tA = __hip_atomic_load((GAS u64*)(p.ws + (13u << 20)), RLX_AGENT);
    while (__builtin_amdgcn_s_memrealtime() < tA + 6000) __builtin_amdgcn_s_sleep(4);
  }
}
}

__global__ void k_first(unsigned char* ws) { if (threadIdx.x == 0) __hip_atomic_store((GAS u64*)(ws + (13u << 20)), (u64)__builtin_amdgcn_s_memrealtime(), RLX_AGENT); }
extern "C" void kernel_launch(void* const* d_in, const int* in_sizes, int n_in, void* d_out, int out_size, void* d_ws, size_t ws_size, hipStream_t stream) {
  Params p;
  p.x = (const u16*)d_in[0]; p.K = (const u16*)d_in[1]; p.V = (const u16*)d_in[2]; p.wq = (const u16*)d_in[3]; p.wo = (const u16*)d_in[4];
  p.w1 = (const u16*)d_in[6]; p.w3 = (const u16*)d_in[7]; p.w2 = (const u16*)d_in[8];
  p.out = (u16*)d_out; p.ws = (unsigned char*)d_ws;
  hipLaunchKernelGGL(k_first, dim3(KF_GRID), dim3(64), 0, stream, (unsigned char*)d_ws);
  hipLaunchKernelGGL(fused_block, dim3(256), dim3(NTHR), 0, stream, p);
}
```

```cpp
#include <hip/hip_runtime.h>
#include <stdint.h>

typedef unsigned short u16;
typedef unsigned u32;
typedef unsigned long long u64;
typedef u32 v4u __attribute__((ext_vector_type(4)));
typedef u32 v2u __attribute__((ext_vector_type(2)));
typedef float f4 __attribute__((ext_vector_type(4)));
typedef float f2 __attribute__((ext_vector_type(2)));
typedef short s4 __attribute__((ext_vector_type(4)));
typedef __bf16 bf2 __attribute__((ext_vector_type(2)));
typedef __bf16 bf8 __attribute__((ext_vector_type(8)));
#define GAS __attribute__((address_space(1)))
#define LAS __attribute__((address_space(3)))
#define RLX_AGENT __ATOMIC_RELAXED, __HIP_MEMORY_SCOPE_AGENT
#define RLX_WG __ATOMIC_RELAXED, __HIP_MEMORY_SCOPE_WORKGROUP

#define RD 16
#define W0 2
#define W1 4
#define WTRIG (31 - W1)
#define RTRIG 16
#define DYNROLE 1
#define QSLEEP 16
#define PST 0
#define CODEPF 1
#define CODE_BYTES 20864u
#define TAILNT 0
#define W13AUX 2
#define SONLY 0
#define STAMPS 0

namespace {
constexpr int DM = 640, NH = 32, DH = 128, SK = 8192, WIN = 2048, FF = 1728;
constexpr int NTHR = 768;
constexpr int AUX_NT = 2, AUX_SC1 = 16, RSRC_W3 = 0x00020000;
constexpr int OFF_CNT = 0x800;
constexpr int OFF_XL = 0x1000;
constexpr int OFF_XQ = 0x10000;
constexpr int OFF_X1 = 0x90000;
constexpr int OFF_X2 = 0xB0000;
constexpr int OFF_X3 = 0xD8000;
constexpr int OFF_X4 = 0xE0000;
constexpr int NFU = 54;
constexpr int WS_USED = 0x170000;
constexpr size_t OFF_STAMP = 8u << 20;
constexpr u32 SPIN_CAP = 1u << 16;
constexpr float LOG2E = 1.4426950408889634f;

struct Params {
  const u16 *x, *K, *V, *wq, *wo, *w1, *w3, *w2;
  u16* out;
  unsigned char* ws;
};

__device__ __forceinline__ float rb(float f) { return (float)(__bf16)f; }
__device__ __forceinline__ u32 pack2(float lo, float hi) { bf2 v = {(__bf16)lo, (__bf16)hi}; return __builtin_bit_cast(u32, v); }
__device__ __forceinline__ float blo(u32 v) { return __uint_as_float(v << 16); }
__device__ __forceinline__ float bhi(u32 v) { return __uint_as_float(v & 0xffff0000u); }
__device__ __forceinline__ float dot2(u32 a, u32 b, float c) {
  return __builtin_amdgcn_fdot2_f32_bf16(__builtin_bit_cast(bf2, a), __builtin_bit_cast(bf2, b), c, false);
}
__device__ __forceinline__ f4 mfma(v4u a, v4u b, f4 c) {
  return __builtin_amdgcn_mfma_f32_16x16x32_bf16(__builtin_bit_cast(bf8, a), __builtin_bit_cast(bf8, b), c, 0, 0, 0);
}
template <int CTRL> __device__ __forceinline__ u32 dppu(u32 x) { return (u32)__builtin_amdgcn_mov_dpp((int)x, CTRL, 0xf, 0xf, true); }
template <int CTRL> __device__ __forceinline__ float dppf(float x) {
  return __builtin_bit_cast(float, __builtin_amdgcn_mov_dpp(__builtin_bit_cast(int, x), CTRL, 0xf, 0xf, true));
}
constexpr int XOR1 = 0xB1, XOR2 = 0x4E, XOR7 = 0x141, XOR8 = 0x128, XOR15 = 0x140, ROR4 = 0x124;
constexpr int QB1 = 0x55, QB2 = 0xAA, QB3 = 0xFF;
__device__ __forceinline__ float xrow_sum(float x) {
  auto s = __builtin_amdgcn_permlane16_swap(__float_as_uint(x), __float_as_uint(x), false, false);
  x = __uint_as_float(s[0]) + __uint_as_float(s[1]);
  auto t = __builtin_amdgcn_permlane32_swap(__float_as_uint(x), __float_as_uint(x), false, false);
  return __uint_as_float(t[0]) + __uint_as_float(t[1]);
}
__device__ __forceinline__ float xhalf_sum(float x) {
  auto t = __builtin_amdgcn_permlane32_swap(__float_as_uint(x), __float_as_uint(x), false, false);
  return __uint_as_float(t[0]) + __uint_as_float(t[1]);
}
__device__ __forceinline__ float xrow_max(float x) {
  auto s = __builtin_amdgcn_permlane16_swap(__float_as_uint(x), __float_as_uint(x), false, false);
  x = fmaxf(__uint_as_float(s[0]), __uint_as_float(s[1]));
  auto t = __builtin_amdgcn_permlane32_swap(__float_as_uint(x), __float_as_uint(x), false, false);
  return fmaxf(__uint_as_float(t[0]), __uint_as_float(t[1]));
}
__device__ __forceinline__ float wave_sum(float x) {
  x += dppf<XOR1>(x); x += dppf<XOR2>(x); x += dppf<XOR7>(x); x += dppf<XOR15>(x);
  return xrow_sum(x);
}
__device__ __forceinline__ float wave_max(float x) {
  x = fmaxf(x, dppf<XOR1>(x)); x = fmaxf(x, dppf<XOR2>(x)); x = fmaxf(x, dppf<XOR7>(x)); x = fmaxf(x, dppf<XOR15>(x));
  return xrow_max(x);
}
__device__ __forceinline__ float sum16q(float x) { x += dppf<ROR4>(x); x += dppf<XOR8>(x); return xrow_sum(x); }
__device__ __forceinline__ v4u ld_nt(__amdgpu_buffer_rsrc_t r, int voff, int soff) { return __builtin_amdgcn_raw_buffer_load_b128(r, voff, soff, AUX_NT); }
__device__ __forceinline__ v4u ld_poll_nt(__amdgpu_buffer_rsrc_t r, int voff) { return __builtin_amdgcn_raw_buffer_load_b128(r, voff, 0, AUX_SC1 | AUX_NT); }
__device__ __forceinline__ v4u ld_poll(__amdgpu_buffer_rsrc_t r, int voff) { return __builtin_amdgcn_raw_buffer_load_b128(r, voff, 0, AUX_SC1 | TAILNT); }
__device__ __forceinline__ void st_sc1(__amdgpu_buffer_rsrc_t r, int voff, v4u v) { __builtin_amdgcn_raw_buffer_store_b128(v, r, voff, 0, AUX_SC1); }
template <int AUX = AUX_NT> __device__ __forceinline__ void dma_nt(__amdgpu_buffer_rsrc_t r, LAS unsigned char* dst, int voff, int soff) {
  __builtin_amdgcn_raw_ptr_buffer_load_lds(r, (LAS void*)dst, 16, voff, soff, 0, AUX);
}
__device__ __forceinline__ __amdgpu_buffer_rsrc_t mkrsrc(const void* p, int bytes) { return __builtin_amdgcn_make_buffer_rsrc((void*)p, 0, bytes, RSRC_W3); }
__device__ __forceinline__ u32 enc1(float a, u32 tag) { return ((__float_as_uint(a) + 0x200u) & ~0x3ffu) | tag; }
__device__ __forceinline__ v4u enc4(float a, float b, float c, float d, u32 tag) { return v4u{enc1(a, tag), enc1(b, tag), enc1(c, tag), enc1(d, tag)}; }
__device__ __forceinline__ bool ok4(v4u g, u32 tag) { return ((((g.x ^ tag) | (g.y ^ tag)) | ((g.z ^ tag) | (g.w ^ tag))) & 0x3ffu) == 0u; }
__device__ __forceinline__ float dec(u32 w) { return __uint_as_float(w & ~0x3ffu); }
__device__ __forceinline__ f4 dec4(v4u g) { return f4{dec(g.x), dec(g.y), dec(g.z), dec(g.w)}; }

__device__ __forceinline__ void ln_finish(float pivot, float s1, float s2, float& mb, float& ri) {
  const float m1 = wave_sum(s1) * (1.0f / DM), m2 = wave_sum(s2) * (1.0f / DM);
  const float var = fmaxf(m2 - m1 * m1, 0.f);
  mb = rb(pivot + m1);
  ri = __builtin_amdgcn_rcpf(rb(__builtin_amdgcn_sqrtf(rb(rb(var) + 1e-5f))));
}
__device__ __forceinline__ float first_lane(float x) { return __uint_as_float(__builtin_amdgcn_readfirstlane(__float_as_uint(x))); }
__device__ __forceinline__ u32 mksel(int j, int k, bool act) {
  const u32 s = (j & 1) ? 0x01000c0cu : 0x0c0c0100u;
  return (act && (j >> 1) == k) ? s : 0x0c0c0c0cu;
}
__device__ __forceinline__ v4u trfrag(LAS unsigned char* a0, int step4) {
  const v2u lo = __builtin_bit_cast(v2u, __builtin_amdgcn_ds_read_tr16_b64_v4i16((LAS s4*)a0));
  const v2u hi = __builtin_bit_cast(v2u, __builtin_amdgcn_ds_read_tr16_b64_v4i16((LAS s4*)(a0 + step4)));
  return v4u{lo.x, lo.y, hi.x, hi.y};
}
__device__ __forceinline__ void lds_arrive(LAS u32* cnt, int lane) {
  asm volatile("s_waitcnt lgkmcnt(0)" ::: "memory");
  if (lane == 0) __hip_atomic_fetch_add(cnt, 1u, RLX_WG);
}
template <int SL> __device__ __forceinline__ void lds_wait(LAS u32* cnt, u32 n) {
  for (u32 spin = 0; spin < (1u << 22); ++spin) { if (__hip_atomic_load(cnt, RLX_WG) >= n) break; __builtin_amdgcn_s_sleep(SL); }
  asm volatile("" ::: "memory");
}
#define STAMP(n) do {} while (0)
#define SB() __builtin_amdgcn_sched_barrier(0)
#define MEMCLOB() asm volatile("" ::: "memory")
template <int N> __device__ __forceinline__ void wait_vm() { asm volatile("s_waitcnt vmcnt(%0)" ::"n"(N) : "memory"); }

constexpr int L_FLAG = 0;
constexpr int L_STAT = 64;
constexpr int L_INFO = 96;
constexpr int L_M = 128;
constexpr int L_Q = 256;
constexpr int L_HH = 512;
constexpr int L_O = 1024;
constexpr int O_RS = 576;
constexpr int L_LN2 = 3328;
constexpr int LN2_RS = 1344;
constexpr int L_RED = 8704;
constexpr int L_W = L_RED + 8192;
constexpr int L_W2 = L_W + 2 * 640 * 64;
constexpr int L_BYTES = L_W2 + 32 * 1280;
}

extern "C" __global__ void __launch_bounds__(NTHR, 3) fused_block(Params p) {
  __shared__ __attribute__((aligned(16))) unsigned char smem[L_BYTES];
  LAS unsigned char* L = (LAS unsigned char*)smem;
  const int tid = threadIdx.x, lane = tid & 63, wid = __builtin_amdgcn_readfirstlane(tid >> 6);
  const int bid = blockIdx.x;
  const int r = lane >> 4, c = lane & 15;
  asm volatile("" :: "s"(p.x), "s"(p.K), "s"(p.V), "s"(p.wq), "s"(p.wo), "s"(p.w1), "s"(p.w3), "s"(p.w2), "s"(p.out), "s"(p.ws));
  STAMP(0);
  const int h = (bid & 7) + 8 * (bid >> 6), bs = (bid >> 3) & 7, b = bs >> 1, s = bs & 1;
  LAS u32* flags = (LAS u32*)(L + L_FLAG);
  LAS float* mbuf = (LAS float*)(L + L_M);
  LAS float* lbuf = mbuf + 8;
  const __amdgpu_buffer_rsrc_t wsr = mkrsrc(p.ws, WS_USED);
  const int t = wid & 3;
  const int bsel = (lane >> 3) & 1;

  const size_t kvoff = ((size_t)(b * NH + h) * SK + (SK - WIN) + s * 1024) * DH;
  const __amdgpu_buffer_rsrc_t kr = mkrsrc(p.K + kvoff, 1024 * DH * 2), vr = mkrsrc(p.V + kvoff, 1024 * DH * 2);
  const int kvo = wid * 1024 + lane * 16;
  v4u ring[RD];
  u32 xr[5];
  v4u wqv[5];
  u64 epoch = 0;
  if (wid < 8) {
#pragma unroll
    for (int n = 0; n < W0; ++n) ring[n] = ld_nt(kr, kvo, n * 8192);
  } else {
    const int row0 = 80 * bs + 20 * t + r;
#pragma unroll
    for (int k = 0; k < 5; ++k) xr[k] = ((const GAS u32*)p.x)[t * 320 + lane + 64 * k];
    const __amdgpu_buffer_rsrc_t qr = mkrsrc(p.wq + h * DH, DM * NH * DH * 2 - h * DH * 2);
#pragma unroll
    for (int i = 0; i < 5; ++i) wqv[i] = ld_nt(qr, row0 * (NH * DH * 2) + c * 16, i * 4 * (NH * DH * 2));
    epoch = __hip_atomic_load((GAS u64*)p.ws, RLX_AGENT);
    if (wid == 8 && lane < 16) flags[lane] = 0u;
  }
  STAMP(9);
  SB();
  __syncthreads();

  if (wid >= 8) {
  u32 pfv = 0;
  {
    u64 pc; u32 off;
    asm volatile("s_getpc_b64 %0\n.Lpf_here%=:\n\ts_mov_b32 %1, .Lpf_here%=-fused_block" : "=s"(pc), "=s"(off));
    if (bid < 8) {
      const u32 o = (u32)t * 8192u + lane * 128u;
      if (o < CODE_BYTES) pfv = *(const GAS u32*)(pc - off + o);
    }
  }
  u32 tag, t16;
  {
    LAS u32* xw = (LAS u32*)(L + L_LN2 + t * LN2_RS);
#pragma unroll
    for (int k = 0; k < 5; ++k) xw[lane + 64 * k] = xr[k];
    const float pivot = first_lane(blo(xr[0]));
    float s1 = 0.f, s2 = 0.f;
#pragma unroll
    for (int k = 0; k < 5; ++k) { const float d0 = blo(xr[k]) - pivot, d1 = bhi(xr[k]) - pivot; s1 += d0 + d1; s2 = fmaf(d0, d0, fmaf(d1, d1, s2)); }
    float mb, ri;
    ln_finish(pivot, s1, s2, mb, ri);
    if (lane == 0) *(LAS f2*)(L + L_STAT + t * 8) = f2{mb, ri};
    lds_arrive(flags + 0, lane);
    u32 sel[4];
#pragma unroll
    for (int k = 0; k < 4; ++k) sel[k] = mksel(lane & 7, k, true);
    lds_wait<0>(flags + 0, 4);
    f2 st[2];
    st[0] = *(LAS f2*)(L + L_STAT + bsel * 8); st[1] = *(LAS f2*)(L + L_STAT + (2 + bsel) * 8);
    u32 xe[5][2];
#pragma unroll
    for (int i = 0; i < 5; ++i)
#pragma unroll
      for (int P = 0; P < 2; ++P) xe[i][P] = *(LAS u16*)(L + L_LN2 + (2 * P + bsel) * LN2_RS + (80 * bs + 20 * t + r + 4 * i) * 2);
    f4 acc[2] = {{0.f, 0.f, 0.f, 0.f}, {0.f, 0.f, 0.f, 0.f}};
#pragma unroll
    for (int i = 0; i < 5; ++i)
#pragma unroll
      for (int P = 0; P < 2; ++P) {
        const u32 lv = pack2(rb(blo(xe[i][P]) - st[P].x) * st[P].y, 0.f);
        v4u a;
#pragma unroll
        for (int k = 0; k < 4; ++k) a[k] = __builtin_amdgcn_perm(lv, lv, sel[k]);
        acc[P] = mfma(a, wqv[i], acc[P]);
      }
#pragma unroll
    for (int P = 0; P < 2; ++P) *(LAS f4*)(L + L_RED + t * 2048 + P * 1024 + lane * 16) = acc[P];
    lds_arrive(flags + 1, lane);
    tag = __builtin_amdgcn_readfirstlane(((u32)epoch + 1u) & 0x3ffu);
    t16 = __builtin_amdgcn_readfirstlane(((u32)epoch + 1u) & 0xffffu);
    lds_wait<0>(flags + 1, 4);
    if (lane < 32) {
      f4 sum = {0.f, 0.f, 0.f, 0.f};
#pragma unroll
      for (int ww = 0; ww < 4; ++ww) sum += *(LAS f4*)(L + L_RED + ww * 2048 + (t >> 1) * 1024 + (16 * (2 * (t & 1) + (lane & 1)) + (lane >> 1)) * 16);
      st_sc1(wsr, OFF_XQ + ((h * 4 + t) * 8 + bs) * 512 + lane * 16, enc4(sum.x, sum.y, sum.z, sum.w, tag));
    }
  }
  STAMP(8);
  if (DYNROLE && bid == 0 && t == 1 && lane == 0) __hip_atomic_store((GAS u32*)(p.ws + OFF_CNT), t16 << 16, RLX_AGENT);
  if (t == 0) {
    if (QSLEEP) __builtin_amdgcn_s_sleep(QSLEEP);
    v4u g[4];
    const int go = OFF_XQ + (h * 4 + b) * 4096 + lane * 16;
    for (u32 spin = 0;; ++spin) {
      MEMCLOB();
      bool ok = true;
#pragma unroll
      for (int k = 0; k < 4; ++k) g[k] = ld_poll_nt(wsr, go + k * 1024);
#pragma unroll
      for (int k = 0; k < 4; ++k) ok &= ok4(g[k], tag);
      if (__all(ok) || spin > SPIN_CAP) break;
      __builtin_amdgcn_s_sleep(4);
    }
    const f4 sum = (dec4(g[0]) + dec4(g[1])) + (dec4(g[2]) + dec4(g[3]));
    const float q0 = xhalf_sum(sum.x), q1 = xhalf_sum(sum.y), q2 = xhalf_sum(sum.z), q3 = xhalf_sum(sum.w);
    if (lane < 32) *(LAS v2u*)(L + L_Q + lane * 8) = v2u{pack2(q0, q1), pack2(q2, q3)};
    lds_arrive(flags + 3, lane);
    STAMP(1);
    lds_wait<16>(flags + 5, 8);
    const f4 a = *(LAS f4*)mbuf, bb = *(LAS f4*)(mbuf + 4), la = *(LAS f4*)lbuf, lb = *(LAS f4*)(lbuf + 4);
    const float M = fmaxf(fmaxf(fmaxf(a.x, a.y), fmaxf(a.z, a.w)), fmaxf(fmaxf(bb.x, bb.y), fmaxf(bb.z, bb.w)));
    const float Ls = ((la.x + la.y) + (la.z + la.w)) + ((lb.x + lb.y) + (lb.z + lb.w));
    const v4u mine = enc4(M, Ls, M, Ls, tag);
    if (lane == 0) st_sc1(wsr, OFF_XL + (h * 8 + bs) * 128, mine);
    v4u gl;
    for (u32 spin = 0;; ++spin) {
      MEMCLOB();
      gl = ld_poll_nt(wsr, OFF_XL + (h * 8 + (bs ^ 1)) * 128);
      if (__all(ok4(gl, tag)) || spin > SPIN_CAP) break;
      __builtin_amdgcn_s_sleep(16);
    }
    const float M0 = dec(mine.x), L0 = dec(mine.y), M1 = dec(gl.x), L1 = dec(gl.y), Mx = fmaxf(M0, M1);
    const float e0 = L0 * __builtin_amdgcn_exp2f((M0 - Mx) * LOG2E), e1 = L1 * __builtin_amdgcn_exp2f((M1 - Mx) * LOG2E);
    if (lane == 0) *(LAS f2*)(L + L_INFO) = f2{e0 * __builtin_amdgcn_rcpf(e0 + e1), __uint_as_float(tag)};
    lds_arrive(flags + 8, lane);
    STAMP(2);
  }
  if (DYNROLE && t == 1) {
    for (u32 spin = 0; spin <= SPIN_CAP; ++spin) {
      MEMCLOB();
      const u32 v = __builtin_amdgcn_raw_buffer_load_b32(wsr, OFF_CNT, 0, AUX_SC1 | AUX_NT);
      if (__builtin_amdgcn_readfirstlane(v >> 16) == t16) break;
      __builtin_amdgcn_s_sleep(16);
    }
    lds_wait<8>(flags + 9, 8);
    u32 old = 0;
    if (lane == 0) old = __hip_atomic_fetch_add((GAS u32*)(p.ws + OFF_CNT), 1u, RLX_AGENT);
    if (lane == 0) *(LAS u32*)(L + L_INFO + 8) = old & 0xffu;
    lds_arrive(flags + 10, lane);
  }
  asm volatile("" :: "v"(pfv));
  int rk = bid < 160 ? bid + 96 : bid < 216 ? bid - 160 + (bid >= 214 ? 40 : 0) : bid - 216 + 54;
  if (DYNROLE) {
    lds_wait<8>(flags + 10, 1);
    rk = __builtin_amdgcn_readfirstlane(*(LAS u32*)(L + L_INFO + 8));
  }
  const int role = rk >= 96 ? 0 : rk < 54 ? 1 : rk < 94 ? 2 : 3;
  const int hp = (rk - 96) / 10, j_o = (rk - 96) - 10 * hp;
  const int u = rk;
  const bool ffn = role == 1;
  const int q4 = (lane & 15) >> 2, p4 = lane & 3;
  v4u bw[30];
  if (role == 0 || ffn) {
    lds_wait<8>(flags + 7, 8);
    STAMP(10);
    if (role == 0) {
      const __amdgpu_buffer_rsrc_t orr = mkrsrc(p.wo + (size_t)hp * 256 * DM + 64 * j_o, 256 * DM * 2 - 128 * j_o);
      const int vo = (64 * t + (lane >> 3)) * (DM * 2) + (lane & 7) * 16;
#pragma unroll
      for (int i = 0; i < 8; ++i) dma_nt(orr, L + L_W + (64 * t + 8 * i) * 128, vo, i * 8 * DM * 2);
    } else {
      const __amdgpu_buffer_rsrc_t w1r = mkrsrc(p.w1 + 32 * u, DM * FF * 2 - 64 * u), w3r = mkrsrc(p.w3 + 32 * u, DM * FF * 2 - 64 * u);
      const __amdgpu_buffer_rsrc_t w2r = mkrsrc(p.w2 + (size_t)32 * u * DM, 32 * DM * 2);
      const int vo13 = (160 * t + (lane >> 2)) * (FF * 2) + (lane & 3) * 16, vo2 = r * (DM * 2) + c * 16;
#pragma unroll
      for (int i = 0; i < 10; ++i) dma_nt<W13AUX>(w1r, L + L_W + (160 * t + 16 * i) * 64, vo13, i * 16 * FF * 2);
#pragma unroll
      for (int i = 0; i < 10; ++i) dma_nt<W13AUX>(w3r, L + L_W + 640 * 64 + (160 * t + 16 * i) * 64, vo13, i * 16 * FF * 2);
#pragma unroll
      for (int i = 0; i < 10; ++i) { const int n = 10 * t + i; dma_nt(w2r, L + L_W2 + n * 1024, vo2, (n / 5) * 4 * DM * 2 + (n % 5) * 256); }
    }
    wait_vm<0>();
    lds_arrive(flags + 2, lane);
    lds_wait<0>(flags + 2, 4);
    STAMP(11);
    if (role == 0) {
#pragma unroll
      for (int ks = 0; ks < 8; ++ks) bw[ks] = trfrag(L + L_W + (32 * ks + 8 * r + q4) * 128 + (16 * t + 4 * p4) * 2, 4 * 128);
    } else {
#pragma unroll
      for (int ks = 0; ks < 20; ++ks) bw[ks] = trfrag(L + L_W + (p4 >> 1) * (640 * 64) + (32 * ks + 8 * r + q4) * 64 + (8 * t + 4 * (p4 & 1)) * 2, 4 * 64);
#pragma unroll
      for (int T = 0; T < 10; ++T) { const int col = 160 * t + 16 * T + 4 * p4; bw[20 + T] = trfrag(L + L_W2 + ((2 * r) * 5 + (col >> 7)) * 1024 + q4 * 256 + (col & 127) * 2, 5 * 1024); }
    }
    STAMP(12);
  }
  if (role == 0) {
    v4u g0, g1;
    const int go = OFF_X1 + ((2 * hp + (lane >> 5)) * 8 + 2 * t) * 512 + (lane & 31) * 16;
    for (u32 spin = 0;; ++spin) {
      MEMCLOB();
      g0 = ld_poll(wsr, go); g1 = ld_poll(wsr, go + 512);
      if (__all(ok4(g0, tag) && ok4(g1, tag)) || spin > SPIN_CAP) break;
      if (PST) __builtin_amdgcn_s_sleep(PST);
    }
    STAMP(5);
    const f4 o = dec4(g0) + dec4(g1);
    *(LAS v2u*)(L + L_O + t * O_RS + lane * 8) = v2u{pack2(o.x, o.y), pack2(o.z, o.w)};
    __syncthreads();
    f4 d = {0.f, 0.f, 0.f, 0.f};
#pragma unroll
    for (int ks = 0; ks < 8; ++ks) d = mfma(*(LAS v4u*)(L + L_O + (c >> 2) * O_RS + ks * 64 + r * 16), bw[ks], d);
    const u32 y0 = enc1(d[0], tag);
    const v4u yv = {y0, dppu<QB1>(y0), dppu<QB2>(y0), dppu<QB3>(y0)};
    if ((c & 3) == 0) st_sc1(wsr, OFF_X2 + (((4 * j_o + t) * 4 + r) * 16 + hp) * 64 + c * 4, yv);
    STAMP(6);
  } else if (ffn) {
    v4u g[3];
    const int go = OFF_X3 + t * 2560 + lane * 16;
    const bool has2 = lane < 32;
    for (u32 spin = 0;; ++spin) {
      MEMCLOB();
      g[0] = ld_poll(wsr, go); g[1] = ld_poll(wsr, go + 1024); g[2] = ld_poll(wsr, go + (has2 ? 2048 : 0));
      if (__all(ok4(g[0], tag) && ok4(g[1], tag) && ok4(g[2], tag)) || spin > SPIN_CAP) break;
      if (PST) __builtin_amdgcn_s_sleep(PST);
    }
    STAMP(5);
    const f4 h0 = dec4(g[0]), h1 = dec4(g[1]), h2 = dec4(g[2]);
    const float pivot = first_lane(h0.x);
    const f4 d0 = h0 - pivot, d1 = h1 - pivot, d2 = has2 ? h2 - pivot : f4{0.f, 0.f, 0.f, 0.f};
    const f4 ds = (d0 + d1) + d2, dq = (d0 * d0 + d1 * d1) + d2 * d2;
    float mb, ri;
    ln_finish(pivot, (ds.x + ds.y) + (ds.z + ds.w), (dq.x + dq.y) + (dq.z + dq.w), mb, ri);
    LAS unsigned char* lp = L + L_LN2 + t * LN2_RS + lane * 8;
    *(LAS v2u*)lp = v2u{pack2(rb(h0.x - mb) * ri, rb(h0.y - mb) * ri), pack2(rb(h0.z - mb) * ri, rb(h0.w - mb) * ri)};
    *(LAS v2u*)(lp + 512) = v2u{pack2(rb(h1.x - mb) * ri, rb(h1.y - mb) * ri), pack2(rb(h1.z - mb) * ri, rb(h1.w - mb) * ri)};
    if (has2) *(LAS v2u*)(lp + 1024) = v2u{pack2(rb(h2.x - mb) * ri, rb(h2.y - mb) * ri), pack2(rb(h2.z - mb) * ri, rb(h2.w - mb) * ri)};
    __syncthreads();
    STAMP(13);
    f4 d = {0.f, 0.f, 0.f, 0.f};
#pragma unroll
    for (int ks = 0; ks < 20; ++ks) d = mfma(*(LAS v4u*)(L + L_LN2 + (c >> 2) * LN2_RS + ks * 64 + r * 16), bw[ks], d);
    const float gs = rb(d[0]), us = dppf<XOR8>(gs);
    const float si = rb(gs * __builtin_amdgcn_rcpf(1.0f + __builtin_amdgcn_exp2f(-gs * LOG2E)));
    if (c < 8) *(LAS u16*)(L + L_HH + r * 64 + (8 * t + c) * 2) = (u16)(pack2(si * us, 0.f) & 0xffffu);
    STAMP(14);
    __syncthreads();
    const v4u a = *(LAS v4u*)(L + L_HH + (c >> 2) * 64 + r * 16);
    float z[10];
#pragma unroll
    for (int T = 0; T < 10; ++T) z[T] = mfma(a, bw[20 + T], f4{0.f, 0.f, 0.f, 0.f})[0];
#pragma unroll
    for (int T = 0; T < 10; ++T) {
      const u32 y0 = enc1(z[T], tag);
      const v4u yv = {y0, dppu<QB1>(y0), dppu<QB2>(y0), dppu<QB3>(y0)};
      if ((c & 3) == 0) st_sc1(wsr, OFF_X4 + (((10 * t + T) * 4 + r) * NFU + u) * 64 + c * 4, yv);
    }
    STAMP(6);
  } else if (role == 2) {
    const int cs = rk - 54;
    const int pp = lane >> 2, q = lane & 3;
    const v2u xv = ((const GAS v2u*)p.x)[t * 160 + 4 * cs + q];
    f4 hv;
    {
      v4u g;
      const int go = OFF_X2 + ((cs * 4 + t) * 16 + pp) * 64 + q * 16;
      for (u32 spin = 0;; ++spin) {
        MEMCLOB();
        g = ld_poll(wsr, go);
        if (__all(ok4(g, tag)) || spin > SPIN_CAP) break;
        if (PST) __builtin_amdgcn_s_sleep(PST);
      }
      STAMP(5);
      const f4 y = dec4(g);
      hv.x = rb(blo(xv.x) + rb(sum16q(y.x))); hv.y = rb(bhi(xv.x) + rb(sum16q(y.y)));
      hv.z = rb(blo(xv.y) + rb(sum16q(y.z))); hv.w = rb(bhi(xv.y) + rb(sum16q(y.w)));
    }
    if (lane < 4) st_sc1(wsr, OFF_X3 + t * 2560 + (16 * cs + 4 * q) * 4, enc4(hv.x, hv.y, hv.z, hv.w, tag));
    STAMP(13);
    {
      const int go = OFF_X4 + ((cs * 4 + t) * NFU + pp) * 64 + q * 16;
      v4u g[4];
      const bool last = pp < NFU - 48;
      for (u32 spin = 0;; ++spin) {
        MEMCLOB();
        g[0] = ld_poll(wsr, go); g[1] = ld_poll(wsr, go + 1024); g[2] = ld_poll(wsr, go + 2048); g[3] = ld_poll(wsr, go + (last ? 3072 : 0));
        if (__all(ok4(g[0], tag) && ok4(g[1], tag) && ok4(g[2], tag) && ok4(g[3], tag)) || spin > SPIN_CAP) break;
        if (PST) __builtin_amdgcn_s_sleep(PST);
      }
      STAMP(14);
      f4 z = (dec4(g[0]) + dec4(g[1])) + dec4(g[2]);
      if (last) z += dec4(g[3]);
      const float o0 = hv.x + rb(sum16q(z.x)), o1 = hv.y + rb(sum16q(z.y)), o2 = hv.z + rb(sum16q(z.z)), o3 = hv.w + rb(sum16q(z.w));
      if (lane < 4) ((GAS v2u*)p.out)[t * 160 + 4 * cs + q] = v2u{pack2(o0, o1), pack2(o2, o3)};
    }
    if (cs == 0 && t == 0 && lane == 0) __hip_atomic_store((GAS u64*)p.ws, epoch + 1, RLX_AGENT);
    STAMP(6);
  }
  STAMP(7);
    return;
  }
  {
    const int w = wid;
#pragma unroll
    for (int n = W0; n < RD; ++n) { wait_vm<W0 - 1>(); ring[n] = ld_nt(kr, kvo, n * 8192); SB(); }
    u32 selA[4];
#pragma unroll
    for (int k = 0; k < 4; ++k) selA[k] = mksel(c & 7, k, c < 8);
    STAMP(8);
#if !(SONLY & 1)
    lds_wait<1>(flags + 3, 1);
#endif
    const v4u qv = *(LAS v4u*)(L + L_Q + c * 16);
    STAMP(1);
#define REFILL(n) { constexpr int nn_ = (n) + W1; if (nn_ >= RD && nn_ < 64) { if (nn_ < 32) ring[nn_ % RD] = ld_nt(kr, kvo, nn_ * 8192); else ring[nn_ % RD] = ld_nt(vr, kvo, (nn_ - 32) * 8192); } SB(); }
    float sc[2];
    float x[16];
#define QK_STEP(n) { const v4u kk = ring[(n) % RD]; x[(n) & 15] = dot2(kk.w, qv.w, dot2(kk.z, qv.z, dot2(kk.y, qv.y, dot2(kk.x, qv.x, 0.f)))); REFILL(n) }
#define QK4(n) QK_STEP(n) QK_STEP((n) + 1) QK_STEP((n) + 2) QK_STEP((n) + 3)
#define TR_STEP(HALF, CTRL, BIT) _Pragma("unroll") for (int hh = 0; hh < (HALF); ++hh) { \
      const float lo_ = x[hh], hi_ = x[hh + (HALF)]; const float t1_ = lo_ + dppf<CTRL>(lo_), t2_ = hi_ + dppf<CTRL>(hi_); x[hh] = (c & (BIT)) ? t2_ : t1_; }
#define TR_ALL() TR_STEP(8, XOR8, 8) TR_STEP(4, XOR7, 4) TR_STEP(2, XOR2, 2) TR_STEP(1, XOR1, 1)
    QK4(0) QK4(4) QK4(8) QK4(12)
    TR_ALL()
    sc[0] = rb(rb(x[0]) * 0.08838834764831845f);
    QK4(16) QK4(20) QK4(24) QK4(28)
    TR_ALL()
    sc[1] = rb(rb(x[0]) * 0.08838834764831845f);
    STAMP(2);
    { const float mw = wave_max(fmaxf(sc[0], sc[1])); if (lane == 0) mbuf[w] = mw; }
#if !(SONLY & 2)
    lds_arrive(flags + 4, lane);
    lds_wait<0>(flags + 4, 8);
#endif
    float M;
    { const f4 a = *(LAS f4*)mbuf, bb = *(LAS f4*)(mbuf + 4);
      M = fmaxf(fmaxf(fmaxf(a.x, a.y), fmaxf(a.z, a.w)), fmaxf(fmaxf(bb.x, bb.y), fmaxf(bb.z, bb.w))); }
    const float pe0 = __builtin_amdgcn_exp2f((sc[0] - M) * LOG2E), pe1 = __builtin_amdgcn_exp2f((sc[1] - M) * LOG2E);
    { const float lw = wave_sum(pe0 + pe1); if (lane == 0) lbuf[w] = lw; }
#if !(SONLY & 2)
    lds_arrive(flags + 5, lane);
    lds_wait<0>(flags + 5, 8);
#endif
    float Lsum;
    { const f4 a = *(LAS f4*)lbuf, bb = *(LAS f4*)(lbuf + 4);
      Lsum = ((a.x + a.y) + (a.z + a.w)) + ((bb.x + bb.y) + (bb.z + bb.w)); }
    const float linv = __builtin_amdgcn_rcpf(Lsum);
    u32 pb[2];
    pb[0] = pack2(pe0 * linv, 0.f); pb[1] = pack2(pe1 * linv, 0.f);
    f4 acc = {0.f, 0.f, 0.f, 0.f};
#define PV_STEP(i) { const v4u vv = ring[(32 + (i)) % RD]; \
      const u32 xx = (u32)__builtin_amdgcn_update_dpp(0, (int)pb[(i) >> 4], 0x150 + ((i) & 15), 0xf, 0xf, false); \
      v4u a; a[0] = __builtin_amdgcn_perm(xx, xx, selA[0]); a[1] = __builtin_amdgcn_perm(xx, xx, selA[1]); a[2] = __builtin_amdgcn_perm(xx, xx, selA[2]); a[3] = __builtin_amdgcn_perm(xx, xx, selA[3]); \
      acc = mfma(a, vv, acc); REFILL(32 + (i)) \
      if ((i) == WTRIG && lane == 0) __hip_atomic_fetch_add(flags + 7, 1u, RLX_WG); \
      if (DYNROLE && (i) == RTRIG && lane == 0) __hip_atomic_fetch_add(flags + 9, 1u, RLX_WG); }
#define PV4(i) PV_STEP(i) PV_STEP((i) + 1) PV_STEP((i) + 2) PV_STEP((i) + 3)
    PV4(0) PV4(4) PV4(8) PV4(12) PV4(16) PV4(20) PV4(24) PV4(28)
    if (lane < 32) *(LAS f4*)(L + L_RED + w * 512 + (8 * c + 4 * r) * 4) = acc;
    asm volatile("s_waitcnt lgkmcnt(0)" ::: "memory");
    u32 old = 0;
    if (lane == 0) old = __hip_atomic_fetch_add(flags + 6, 1u, RLX_WG);
    old = __builtin_amdgcn_readfirstlane(old);
    STAMP(3);
#if !(SONLY & (4 | 8))
    if (old == 7) {
      lds_wait<0>(flags + 8, 1);
      MEMCLOB();
      const f2 info = *(LAS f2*)(L + L_INFO);
      if (lane < 32) {
        f4 sum = {0.f, 0.f, 0.f, 0.f};
#pragma unroll
        for (int ww = 0; ww < 8; ++ww) sum += *(LAS f4*)(L + L_RED + ww * 512 + lane * 16);
        sum *= info.x;
        st_sc1(wsr, OFF_X1 + (h * 8 + bs) * 512 + lane * 16, enc4(sum.x, sum.y, sum.z, sum.w, __float_as_uint(info.y)));
      }
      STAMP(4);
    }
#endif
  }

}

extern "C" void kernel_launch(void* const* d_in, const int* in_sizes, int n_in, void* d_out, int out_size, void* d_ws, size_t ws_size, hipStream_t stream) {
  Params p;
  p.x = (const u16*)d_in[0]; p.K = (const u16*)d_in[1]; p.V = (const u16*)d_in[2]; p.wq = (const u16*)d_in[3]; p.wo = (const u16*)d_in[4];
  p.w1 = (const u16*)d_in[6]; p.w3 = (const u16*)d_in[7]; p.w2 = (const u16*)d_in[8];
  p.out = (u16*)d_out; p.ws = (unsigned char*)d_ws;
  hipLaunchKernelGGL(fused_block, dim3(256), dim3(NTHR), 0, stream, p);
}
```

```cpp
#include <hip/hip_runtime.h>
#include <stdint.h>

typedef unsigned short u16;
typedef unsigned u32;
typedef unsigned long long u64;
typedef u32 v4u __attribute__((ext_vector_type(4)));
typedef u32 v2u __attribute__((ext_vector_type(2)));
typedef float f4 __attribute__((ext_vector_type(4)));
typedef float f2 __attribute__((ext_vector_type(2)));
typedef short s4 __attribute__((ext_vector_type(4)));
typedef __bf16 bf2 __attribute__((ext_vector_type(2)));
typedef __bf16 bf8 __attribute__((ext_vector_type(8)));
#define GAS __attribute__((address_space(1)))
#define LAS __attribute__((address_space(3)))
#define RLX_AGENT __ATOMIC_RELAXED, __HIP_MEMORY_SCOPE_AGENT
#define RLX_WG __ATOMIC_RELAXED, __HIP_MEMORY_SCOPE_WORKGROUP

#define RD 20
#define W0 3
#define W1 5
#define WTRIG 24
#define RTRIG 16
#define DYNROLE 1
#define X4DLY 48
#define X4NT 0
#define OSLOPE 0
#define FDLY 12
#define ALLFRAG 0
#define WT W1
#define PF 0
#define PFAUX 2
#define QSLEEP 16
#define PST 0
#define CODEPF 1
#define CODE_BYTES 19200u
#define TAILNT 0
#define W13AUX 2
#define SONLY 0
#define STAMPS 0

namespace {
constexpr int DM = 640, NH = 32, DH = 128, SK = 8192, WIN = 2048, FF = 1728;
constexpr int NTHR = 768;
constexpr int AUX_NT = 2, AUX_SC1 = 16, RSRC_W3 = 0x00020000;
constexpr int OFF_CNT = 0x800;
constexpr int OFF_XL = 0x1000;
constexpr int OFF_XQ = 0x10000;
constexpr int OFF_X1 = 0x90000;
constexpr int OFF_X2 = 0xB0000;
constexpr int X2_BLK = 33 * 128;
constexpr int OFF_X3 = 0x108000;
constexpr int OFF_X4 = 0x110000;
constexpr int NFU = 54;
constexpr int X4_BLK = 55 * 128;
constexpr int WS_USED = 0x1A0000;
constexpr size_t OFF_STAMP = 8u << 20;
constexpr u32 SPIN_CAP = 1u << 16;
constexpr float LOG2E = 1.4426950408889634f;

struct Params {
  const u16 *x, *K, *V, *wq, *wo, *w1, *w3, *w2;
  u16* out;
  unsigned char* ws;
};

__device__ __forceinline__ float rb(float f) { return (float)(__bf16)f; }
__device__ __forceinline__ u32 pack2(float lo, float hi) { bf2 v = {(__bf16)lo, (__bf16)hi}; return __builtin_bit_cast(u32, v); }
__device__ __forceinline__ float blo(u32 v) { return __uint_as_float(v << 16); }
__device__ __forceinline__ float bhi(u32 v) { return __uint_as_float(v & 0xffff0000u); }
__device__ __forceinline__ float dot2(u32 a, u32 b, float c) {
  return __builtin_amdgcn_fdot2_f32_bf16(__builtin_bit_cast(bf2, a), __builtin_bit_cast(bf2, b), c, false);
}
__device__ __forceinline__ f4 mfma(v4u a, v4u b, f4 c) {
  return __builtin_amdgcn_mfma_f32_16x16x32_bf16(__builtin_bit_cast(bf8, a), __builtin_bit_cast(bf8, b), c, 0, 0, 0);
}
template <int CTRL> __device__ __forceinline__ u32 dppu(u32 x) { return (u32)__builtin_amdgcn_mov_dpp((int)x, CTRL, 0xf, 0xf, true); }
template <int CTRL> __device__ __forceinline__ float dppf(float x) {
  return __builtin_bit_cast(float, __builtin_amdgcn_mov_dpp(__builtin_bit_cast(int, x), CTRL, 0xf, 0xf, true));
}
constexpr int XOR1 = 0xB1, XOR2 = 0x4E, XOR7 = 0x141, XOR8 = 0x128, XOR15 = 0x140, ROR4 = 0x124;
constexpr int QB1 = 0x55, QB2 = 0xAA, QB3 = 0xFF;
__device__ __forceinline__ float xrow_sum(float x) {
  auto s = __builtin_amdgcn_permlane16_swap(__float_as_uint(x), __float_as_uint(x), false, false);
  x = __uint_as_float(s[0]) + __uint_as_float(s[1]);
  auto t = __builtin_amdgcn_permlane32_swap(__float_as_uint(x), __float_as_uint(x), false, false);
  return __uint_as_float(t[0]) + __uint_as_float(t[1]);
}
__device__ __forceinline__ float xhalf_sum(float x) {
  auto t = __builtin_amdgcn_permlane32_swap(__float_as_uint(x), __float_as_uint(x), false, false);
  return __uint_as_float(t[0]) + __uint_as_float(t[1]);
}
__device__ __forceinline__ float xrow_max(float x) {
  auto s = __builtin_amdgcn_permlane16_swap(__float_as_uint(x), __float_as_uint(x), false, false);
  x = fmaxf(__uint_as_float(s[0]), __uint_as_float(s[1]));
  auto t = __builtin_amdgcn_permlane32_swap(__float_as_uint(x), __float_as_uint(x), false, false);
  return fmaxf(__uint_as_float(t[0]), __uint_as_float(t[1]));
}
__device__ __forceinline__ float wave_sum(float x) {
  x += dppf<XOR1>(x); x += dppf<XOR2>(x); x += dppf<XOR7>(x); x += dppf<XOR15>(x);
  return xrow_sum(x);
}
__device__ __forceinline__ float wave_max(float x) {
  x = fmaxf(x, dppf<XOR1>(x)); x = fmaxf(x, dppf<XOR2>(x)); x = fmaxf(x, dppf<XOR7>(x)); x = fmaxf(x, dppf<XOR15>(x));
  return xrow_max(x);
}
__device__ __forceinline__ float sum8p(float x) { x += dppf<XOR8>(x); return xrow_sum(x); }
__device__ __forceinline__ v4u ld_nt(__amdgpu_buffer_rsrc_t r, int voff, int soff) { return __builtin_amdgcn_raw_buffer_load_b128(r, voff, soff, AUX_NT); }
__device__ __forceinline__ v4u ld_poll_nt(__amdgpu_buffer_rsrc_t r, int voff) { return __builtin_amdgcn_raw_buffer_load_b128(r, voff, 0, AUX_SC1 | AUX_NT); }
__device__ __forceinline__ v4u ld_poll(__amdgpu_buffer_rsrc_t r, int voff) { return __builtin_amdgcn_raw_buffer_load_b128(r, voff, 0, AUX_SC1 | TAILNT); }
__device__ __forceinline__ void st_sc1(__amdgpu_buffer_rsrc_t r, int voff, v4u v) { __builtin_amdgcn_raw_buffer_store_b128(v, r, voff, 0, AUX_SC1); }
template <int AUX = AUX_NT> __device__ __forceinline__ void dma_nt(__amdgpu_buffer_rsrc_t r, LAS unsigned char* dst, int voff, int soff) {
  __builtin_amdgcn_raw_ptr_buffer_load_lds(r, (LAS void*)dst, 16, voff, soff, 0, AUX);
}
__device__ __forceinline__ __amdgpu_buffer_rsrc_t mkrsrc(const void* p, int bytes) { return __builtin_amdgcn_make_buffer_rsrc((void*)p, 0, bytes, RSRC_W3); }
__device__ __forceinline__ u32 enc1(float a, u32 tag) { return ((__float_as_uint(a) + 0x200u) & ~0x3ffu) | tag; }
__device__ __forceinline__ v4u enc4(float a, float b, float c, float d, u32 tag) { return v4u{enc1(a, tag), enc1(b, tag), enc1(c, tag), enc1(d, tag)}; }
__device__ __forceinline__ bool ok4(v4u g, u32 tag) { return ((((g.x ^ tag) | (g.y ^ tag)) | ((g.z ^ tag) | (g.w ^ tag))) & 0x3ffu) == 0u; }
__device__ __forceinline__ float dec(u32 w) { return __uint_as_float(w & ~0x3ffu); }
__device__ __forceinline__ f4 dec4(v4u g) { return f4{dec(g.x), dec(g.y), dec(g.z), dec(g.w)}; }

__device__ __forceinline__ void ln_finish(float pivot, float s1, float s2, float& mb, float& ri) {
  const float m1 = wave_sum(s1) * (1.0f / DM), m2 = wave_sum(s2) * (1.0f / DM);
  const float var = fmaxf(m2 - m1 * m1, 0.f);
  mb = rb(pivot + m1);
  ri = __builtin_amdgcn_rcpf(rb(__builtin_amdgcn_sqrtf(rb(rb(var) + 1e-5f))));
}
__device__ __forceinline__ float first_lane(float x) { return __uint_as_float(__builtin_amdgcn_readfirstlane(__float_as_uint(x))); }
__device__ __forceinline__ u32 mksel(int j, int k, bool act) {
  const u32 s = (j & 1) ? 0x01000c0cu : 0x0c0c0100u;
  return (act && (j >> 1) == k) ? s : 0x0c0c0c0cu;
}
__device__ __forceinline__ v4u trfrag(LAS unsigned char* a0, int step4) {
  const v2u lo = __builtin_bit_cast(v2u, __builtin_amdgcn_ds_read_tr16_b64_v4i16((LAS s4*)a0));
  const v2u hi = __builtin_bit_cast(v2u, __builtin_amdgcn_ds_read_tr16_b64_v4i16((LAS s4*)(a0 + step4)));
  return v4u{lo.x, lo.y, hi.x, hi.y};
}
__device__ __forceinline__ void lds_arrive(LAS u32* cnt, int lane) {
  asm volatile("s_waitcnt lgkmcnt(0)" ::: "memory");
  if (lane == 0) __hip_atomic_fetch_add(cnt, 1u, RLX_WG);
}
template <int SL> __device__ __forceinline__ void lds_wait(LAS u32* cnt, u32 n) {
#pragma nounroll
  for (u32 spin = 0; spin < (1u << 22); ++spin) { if (__hip_atomic_load(cnt, RLX_WG) >= n) break; __builtin_amdgcn_s_sleep(SL); }
  asm volatile("" ::: "memory");
}
#define STAMP(n) do {} while (0)
#define PCNT(n, v) do {} while (0)
#define FLUSH_STAMPS() do {} while (0)
#define SB() __builtin_amdgcn_sched_barrier(0)
#define MEMCLOB() asm volatile("" ::: "memory")
template <int N> __device__ __forceinline__ void wait_vm() { asm volatile("s_waitcnt vmcnt(%0)" ::"n"(N) : "memory"); }

constexpr int window(int n) { return (n >= 32 - 2 * (WT - W1) && n < 32) ? W1 + (n - (32 - 2 * (WT - W1)) + 2) / 2 : W1; }
constexpr int issued(int n) {
  int v = RD;
  for (int k = 0; k <= n; ++k) { int w = k + 1 + window(k); if (w > k + 1 + RD - 1) w = k + RD; if (w > 64) w = 64; if (w > v) v = w; }
  return v;
}
constexpr int L_FLAG = 0;
constexpr int L_STAT = 64;
constexpr int L_INFO = 96;
constexpr int L_FAC = 192;
constexpr int L_M = 128;
constexpr int L_Q = 256;
constexpr int L_HH = 512;
constexpr int L_O = 1024;
constexpr int O_RS = 320;
constexpr int L_LN2 = 3328;
constexpr int LN2_RS = 1344;
constexpr int L_RED = 8704;
constexpr int L_W = L_RED + 8192;
constexpr int L_W2 = L_W + 2 * 640 * 64;
constexpr int L_BYTES = L_W2 + 32 * 1280;
}

extern "C" __global__ void __launch_bounds__(NTHR, 3) fused_block(Params p) {
  __shared__ __attribute__((aligned(16))) unsigned char smem[L_BYTES];
  LAS unsigned char* L = (LAS unsigned char*)smem;
  const int tid = threadIdx.x, lane = tid & 63, wid = __builtin_amdgcn_readfirstlane(tid >> 6);
  const int bid = blockIdx.x;
  const int r = lane >> 4, c = lane & 15;
  asm volatile("" :: "s"(p.x), "s"(p.K), "s"(p.V), "s"(p.wq), "s"(p.wo), "s"(p.w1), "s"(p.w3), "s"(p.w2), "s"(p.out), "s"(p.ws));
  STAMP(0);
  const int h = (bid & 7) + 8 * (bid >> 6), bs = (bid >> 3) & 7, b = bs >> 1, s = bs & 1;
  LAS u32* flags = (LAS u32*)(L + L_FLAG);
  LAS float* mbuf = (LAS float*)(L + L_M);
  LAS float* lbuf = mbuf + 8;
  const __amdgpu_buffer_rsrc_t wsr = mkrsrc(p.ws, WS_USED);
  const int t = wid & 3;
  const int bsel = (lane >> 3) & 1;

  const size_t kvoff = ((size_t)(b * NH + h) * SK + (SK - WIN) + s * 1024) * DH;
  const __amdgpu_buffer_rsrc_t kr = mkrsrc(p.K + kvoff, 1024 * DH * 2), vr = mkrsrc(p.V + kvoff, 1024 * DH * 2);
  const int kvo = wid * 1024 + lane * 16;
  v4u ring[RD];
  u32 xr[5];
  v4u wqv[5];
  u64 epoch = 0;
  if (wid < 8) {
#pragma unroll
    for (int n = 0; n < W0; ++n) ring[n] = ld_nt(kr, kvo, n * 8192);
  } else {
    const int row0 = 80 * bs + 20 * t + r;
#pragma unroll
    for (int k = 0; k < 5; ++k) xr[k] = ((const GAS u32*)p.x)[t * 320 + lane + 64 * k];
    const __amdgpu_buffer_rsrc_t qr = mkrsrc(p.wq + h * DH, DM * NH * DH * 2 - h * DH * 2);
#pragma unroll
    for (int i = 0; i < 5; ++i) wqv[i] = ld_nt(qr, row0 * (NH * DH * 2) + c * 16, i * 4 * (NH * DH * 2));
    epoch = __hip_atomic_load((GAS u64*)p.ws, RLX_AGENT);
    if (wid == 8 && lane < 16) flags[lane] = 0u;
  }
  STAMP(9);
  SB();
  __syncthreads();

  if (__builtin_expect(wid >= 8, 1)) {
  u32 pfv = 0;
  {
    u64 pc; u32 off;
    asm volatile("s_getpc_b64 %0\n.Lpf_here%=:\n\ts_mov_b32 %1, .Lpf_here%=-fused_block" : "=s"(pc), "=s"(off));
    if (bid < 8) {
      const u32 o = (u32)t * 8192u + lane * 128u;
      if (o < CODE_BYTES) pfv = *(const GAS u32*)(pc - off + o);
    }
  }
  u32 tag, t16;
  {
    LAS u32* xw = (LAS u32*)(L + L_LN2 + t * LN2_RS);
#pragma unroll
    for (int k = 0; k < 5; ++k) xw[lane + 64 * k] = xr[k];
    const float pivot = first_lane(blo(xr[0]));
    float s1 = 0.f, s2 = 0.f;
#pragma unroll
    for (int k = 0; k < 5; ++k) { const float d0 = blo(xr[k]) - pivot, d1 = bhi(xr[k]) - pivot; s1 += d0 + d1; s2 = fmaf(d0, d0, fmaf(d1, d1, s2)); }
    float mb, ri;
    ln_finish(pivot, s1, s2, mb, ri);
    if (lane == 0) *(LAS f2*)(L + L_STAT + t * 8) = f2{mb, ri};
    lds_arrive(flags + 0, lane);
    u32 sel[4];
#pragma unroll
    for (int k = 0; k < 4; ++k) sel[k] = mksel(lane & 7, k, true);
    lds_wait<0>(flags + 0, 4);
    f2 st[2];
    st[0] = *(LAS f2*)(L + L_STAT + bsel * 8); st[1] = *(LAS f2*)(L + L_STAT + (2 + bsel) * 8);
    u32 xe[5][2];
#pragma unroll
    for (int i = 0; i < 5; ++i)
#pragma unroll
      for (int P = 0; P < 2; ++P) xe[i][P] = *(LAS u16*)(L + L_LN2 + (2 * P + bsel) * LN2_RS + (80 * bs + 20 * t + r + 4 * i) * 2);
    f4 acc[2] = {{0.f, 0.f, 0.f, 0.f}, {0.f, 0.f, 0.f, 0.f}};
#pragma unroll
    for (int i = 0; i < 5; ++i)
#pragma unroll
      for (int P = 0; P < 2; ++P) {
        const u32 lv = pack2(rb(blo(xe[i][P]) - st[P].x) * st[P].y, 0.f);
        v4u a;
#pragma unroll
        for (int k = 0; k < 4; ++k) a[k] = __builtin_amdgcn_perm(lv, lv, sel[k]);
        acc[P] = mfma(a, wqv[i], acc[P]);
      }
#pragma unroll
    for (int P = 0; P < 2; ++P) *(LAS f4*)(L + L_RED + t * 2048 + P * 1024 + lane * 16) = acc[P];
    lds_arrive(flags + 1, lane);
    tag = __builtin_amdgcn_readfirstlane(((u32)epoch + 1u) & 0x3ffu);
    t16 = __builtin_amdgcn_readfirstlane(((u32)epoch + 1u) & 0xffffu);
    lds_wait<0>(flags + 1, 4);
    if (lane < 32) {
      f4 sum = {0.f, 0.f, 0.f, 0.f};
#pragma unroll
      for (int ww = 0; ww < 4; ++ww) sum += *(LAS f4*)(L + L_RED + ww * 2048 + (t >> 1) * 1024 + (16 * (2 * (t & 1) + (lane & 1)) + (lane >> 1)) * 16);
      st_sc1(wsr, OFF_XQ + ((h * 4 + t) * 8 + bs) * 512 + lane * 16, enc4(sum.x, sum.y, sum.z, sum.w, tag));
    }
  }
  STAMP(8);
  if (bid < 8 && t == 1 && lane == 0) __hip_atomic_store((GAS u32*)(p.ws + OFF_CNT + bid * 128), t16 << 16, RLX_AGENT);
  if (t == 0) {
    if (QSLEEP) __builtin_amdgcn_s_sleep(QSLEEP);
    v4u g[4];
    const int go = OFF_XQ + (h * 4 + b) * 4096 + lane * 16;
#pragma nounroll
    for (u32 spin = 0;; ++spin) {
      MEMCLOB();
      bool ok = true;
#pragma unroll
      for (int k = 0; k < 4; ++k) g[k] = ld_poll_nt(wsr, go + k * 1024);
#pragma unroll
      for (int k = 0; k < 4; ++k) ok &= ok4(g[k], tag);
      if (__all(ok) || spin > SPIN_CAP) break;
      __builtin_amdgcn_s_sleep(4);
    }
    const f4 sum = (dec4(g[0]) + dec4(g[1])) + (dec4(g[2]) + dec4(g[3]));
    const float q0 = xhalf_sum(sum.x), q1 = xhalf_sum(sum.y), q2 = xhalf_sum(sum.z), q3 = xhalf_sum(sum.w);
    if (lane < 32) *(LAS v2u*)(L + L_Q + lane * 8) = v2u{pack2(q0, q1), pack2(q2, q3)};
    lds_arrive(flags + 3, lane);
    STAMP(1);
    lds_wait<16>(flags + 5, 8);
    const float mw = mbuf[lane & 7], lw = lbuf[lane & 7];
    float M = fmaxf(mw, dppf<XOR1>(mw)); M = fmaxf(M, dppf<XOR2>(M)); M = fmaxf(M, dppf<XOR7>(M));
    const float ew = __builtin_amdgcn_exp2f((mw - M) * LOG2E);
    float Ls = lw * ew; Ls += dppf<XOR1>(Ls); Ls += dppf<XOR2>(Ls); Ls += dppf<XOR7>(Ls);
    Ls = first_lane(Ls);
    const v4u mine = enc4(M, Ls, M, Ls, tag);
    if (lane == 0) st_sc1(wsr, OFF_XL + (h * 8 + bs) * 128, mine);
    v4u gl;
#pragma nounroll
    for (u32 spin = 0;; ++spin) {
      MEMCLOB();
      gl = ld_poll_nt(wsr, OFF_XL + (h * 8 + (bs ^ 1)) * 128);
      if (__all(ok4(gl, tag)) || spin > SPIN_CAP) break;
      __builtin_amdgcn_s_sleep(16);
    }
    const float M0 = dec(mine.x), L0 = dec(mine.y), M1 = dec(gl.x), L1 = dec(gl.y), Mx = fmaxf(M0, M1);
    const float e0 = L0 * __builtin_amdgcn_exp2f((M0 - Mx) * LOG2E), e1 = L1 * __builtin_amdgcn_exp2f((M1 - Mx) * LOG2E);
    if (lane < 8) *(LAS float*)(L + L_FAC + lane * 4) = __builtin_amdgcn_exp2f((mw - Mx) * LOG2E) * __builtin_amdgcn_rcpf(e0 + e1);
    if (lane == 0) *(LAS u32*)(L + L_INFO) = tag;
    lds_arrive(flags + 8, lane);
    STAMP(2);
  }
  if (t == 1) {
#pragma nounroll
    for (u32 spin = 0; spin <= SPIN_CAP; ++spin) {
      MEMCLOB();
      const u32 v = __builtin_amdgcn_raw_buffer_load_b32(wsr, OFF_CNT + (bid & 7) * 128, 0, AUX_SC1 | AUX_NT);
      if (__builtin_amdgcn_readfirstlane(v >> 16) == t16) break;
      __builtin_amdgcn_s_sleep(16);
    }
    lds_wait<8>(flags + 9, 8);
    u32 old = 0;
    if (lane == 0) old = __hip_atomic_fetch_add((GAS u32*)(p.ws + OFF_CNT + (bid & 7) * 128), 1u, RLX_AGENT);
    if (lane == 0) *(LAS u32*)(L + L_INFO + 8) = old & 31u;
    lds_arrive(flags + 10, lane);
  }
  asm volatile("" :: "v"(pfv));
  lds_wait<8>(flags + 10, 1);
  const int rk = DYNROLE ? __builtin_amdgcn_readfirstlane(*(LAS u32*)(L + L_INFO + 8)) : (bid >> 3), grp = bid & 7;
  const int u = rk < 6 ? 2 * (grp + 8 * (rk >> 1)) + (rk & 1) : 48 + grp;
  const int cs = rk < 9 ? grp + 8 * (rk - 7) : 16 + grp;
  const int role = rk >= 12 ? 0 : rk < 7 ? (u < NFU ? 1 : 3) : (rk < 10 && cs < 20) ? 2 : 3;
  const int uo = grp * 20 + rk - 12, hh_o = uo / 5, j_o = uo - 5 * hh_o;
  const bool ffn = role == 1;
  const int q4 = (lane & 15) >> 2, p4 = lane & 3;
  v4u bw[30];
  const int sw2 = 2 * (q4 + 4 * (r & 1));
  auto down_frags = [&]() {
#pragma unroll
    for (int T = 0; T < 10; ++T) { const int col = 160 * t + 16 * T + 4 * p4; bw[20 + T] = trfrag(L + L_W2 + ((2 * r) * 5 + (col >> 7)) * 1024 + q4 * 256 + ((((col & 127) >> 3) ^ sw2) * 16) + (p4 & 1) * 8, 5 * 1024); }
  };
  lds_wait<8>(flags + 7, 8);
  u32 pfw = 0;
  if (PF && !ffn) {
    const int id = ((bid >> 3) * 4 + t) * 64 + lane;
    const GAS unsigned char* pa = nullptr;
    if (id < 5120) {
      const int pi = id / 1280, rem = id - 1280 * pi, mat = rem >= 640, row = rem - 640 * mat;
      pa = (const GAS unsigned char*)(mat ? p.w3 : p.w1) + row * (FF * 2) + (pi < 3 ? grp + 8 * pi : 24 + (grp >> 1)) * 128;
    } else if (id < 7360) {
      const int id2 = id - 5120, m = id2 / 320, rem = id2 - 320 * m, row = rem / 10, lc = rem - 10 * row;
      const int uu = m < 6 ? 2 * (grp + 8 * (m >> 1)) + (m & 1) : 48 + grp;
      if (uu < NFU) pa = (const GAS unsigned char*)p.w2 + (32 * uu + row) * (DM * 2) + lc * 128;
    }
    if (pa) pfw = PFAUX == 2 ? __builtin_nontemporal_load((const GAS u32*)pa) : *(const GAS u32*)pa;
  }
  if (role == 0 || ffn) {
    { const int nsl = ffn ? FDLY : ((31 - rk) * OSLOPE) >> 3;
#pragma nounroll
      for (int i = 0; i < nsl; ++i) __builtin_amdgcn_s_sleep(4); }
    STAMP(10);
    if (role == 0) {
      const __amdgpu_buffer_rsrc_t orr = mkrsrc(p.wo + (size_t)hh_o * DH * DM + 128 * j_o, DH * DM * 2 - 256 * j_o);
      const int vo0 = (32 * t + r) * (DM * 2) + (c ^ (2 * r)) * 16, vo1 = (32 * t + r) * (DM * 2) + (c ^ (2 * r + 8)) * 16;
#pragma unroll
      for (int i = 0; i < 8; ++i) dma_nt(orr, L + L_W + (32 * t + 4 * i) * 256, (i & 2) ? vo1 : vo0, i * 4 * DM * 2);
    } else {
      const __amdgpu_buffer_rsrc_t w1r = mkrsrc(p.w1 + 32 * u, DM * FF * 2 - 64 * u), w3r = mkrsrc(p.w3 + 32 * u, DM * FF * 2 - 64 * u);
      const __amdgpu_buffer_rsrc_t w2r = mkrsrc(p.w2 + (size_t)32 * u * DM, 32 * DM * 2);
      const int rw = lane >> 2, c4 = lane & 3, gp = (rw >> 3) & 1;
      const int vo1 = (160 * t + rw) * (FF * 2) + (c4 ^ gp) * 16, vo3 = (160 * t + rw) * (FF * 2) + (c4 ^ (2 + gp)) * 16;
      const int vo2 = r * (DM * 2) + (c ^ (2 * (r + 4 * (t & 1)))) * 16;
#pragma unroll
      for (int i = 0; i < 10; ++i) dma_nt<W13AUX>(w1r, L + L_W + (160 * t + 16 * i) * 64, vo1, i * 16 * FF * 2);
#pragma unroll
      for (int i = 0; i < 10; ++i) dma_nt<W13AUX>(w3r, L + L_W + 640 * 64 + (160 * t + 16 * i) * 64, vo3, i * 16 * FF * 2);
#pragma unroll
      for (int i = 0; i < 10; ++i) { const int n = 10 * t + i; dma_nt(w2r, L + L_W2 + n * 1024, vo2, (n / 5) * 4 * DM * 2 + (n % 5) * 256); }
    }
    wait_vm<0>();
    lds_arrive(flags + 2, lane);
    lds_wait<0>(flags + 2, 4);
    STAMP(11);
    if (role == 0) {
#pragma unroll
      for (int T = 0; T < 2; ++T)
#pragma unroll
        for (int ks = 0; ks < 4; ++ks) bw[T * 4 + ks] = trfrag(L + L_W + (32 * ks + 8 * r + q4) * 256 + (((4 * t + 2 * T + (p4 >> 1)) ^ sw2) * 16) + (p4 & 1) * 8, 4 * 256);
    } else {
#pragma unroll
      for (int ks = 0; ks < 20; ++ks) bw[ks] = trfrag(L + L_W + (p4 >> 1) * (640 * 64) + (32 * ks + 8 * r + q4) * 64 + ((t ^ (2 * (p4 >> 1) + (r & 1))) * 16) + (p4 & 1) * 8, 4 * 64);
      if (ALLFRAG) down_frags();
    }
    STAMP(12);
  }
  asm volatile("" :: "v"(pfw));
  const int cb = c & 3, cr = c >> 2;
  if (role == 0) {
    v4u g;
    const int go = OFF_X1 + (hh_o * 8 + 2 * t) * 512 + lane * 16;
#pragma nounroll
    for (u32 spin = 0;; ++spin) {
      MEMCLOB();
      g = ld_poll(wsr, go);
      if (__all(ok4(g, tag)) || spin > SPIN_CAP) break;
      if (PST) __builtin_amdgcn_s_sleep(PST);
    }
    STAMP(5);
    const f4 og = dec4(g);
    const float o0 = xhalf_sum(og.x), o1 = xhalf_sum(og.y), o2 = xhalf_sum(og.z), o3 = xhalf_sum(og.w);
    if (lane < 32) *(LAS v2u*)(L + L_O + t * O_RS + lane * 8) = v2u{pack2(o0, o1), pack2(o2, o3)};
    __syncthreads();
    f4 d0 = {0.f, 0.f, 0.f, 0.f}, d1 = {0.f, 0.f, 0.f, 0.f};
#pragma unroll
    for (int ks = 0; ks < 4; ++ks) {
      const v4u a = *(LAS v4u*)(L + L_O + cb * O_RS + ks * 64 + r * 16);
      d0 = mfma(bw[ks], a, d0); d1 = mfma(bw[4 + ks], a, d1);
    }
    const f4 y = (cr & 1) ? d1 : d0;
    if (cr < 2) st_sc1(wsr, OFF_X2 + ((4 * j_o + t) * 4 + cb) * X2_BLK + hh_o * 128 + cr * 64 + r * 16, enc4(y.x, y.y, y.z, y.w, tag));
    STAMP(6);
  } else if (ffn) {
    v4u g[3];
    const int go = OFF_X3 + t * 2560 + lane * 16;
    const bool has2 = lane < 32;
#pragma nounroll
    for (u32 spin = 0;; ++spin) {
      MEMCLOB();
      g[0] = ld_poll(wsr, go); g[1] = ld_poll(wsr, go + 1024); g[2] = ld_poll(wsr, go + (has2 ? 2048 : 0));
      if (__all(ok4(g[0], tag) && ok4(g[1], tag) && ok4(g[2], tag)) || spin > SPIN_CAP) break;
      if (PST) __builtin_amdgcn_s_sleep(PST);
    }
    STAMP(5);
    const float pivot = first_lane(dec(g[0].x));
    float s1 = 0.f, s2 = 0.f;
#pragma unroll
    for (int k = 0; k < 3; ++k)
#pragma unroll
      for (int e = 0; e < 4; ++e) { const float d = (k < 2 || has2) ? dec(g[k][e]) - pivot : 0.f; s1 += d; s2 = fmaf(d, d, s2); }
    float mb, ri;
    ln_finish(pivot, s1, s2, mb, ri);
    LAS unsigned char* lp = L + L_LN2 + t * LN2_RS + lane * 8;
#pragma unroll
    for (int k = 0; k < 3; ++k)
      if (k < 2 || has2) *(LAS v2u*)(lp + 512 * k) = v2u{pack2(rb(dec(g[k].x) - mb) * ri, rb(dec(g[k].y) - mb) * ri), pack2(rb(dec(g[k].z) - mb) * ri, rb(dec(g[k].w) - mb) * ri)};
    __syncthreads();
    STAMP(13);
    f4 da = {0.f, 0.f, 0.f, 0.f}, db = {0.f, 0.f, 0.f, 0.f};
#pragma unroll
    for (int ks = 0; ks < 20; ks += 2) {
      da = mfma(bw[ks], *(LAS v4u*)(L + L_LN2 + cb * LN2_RS + ks * 64 + r * 16), da);
      db = mfma(bw[ks + 1], *(LAS v4u*)(L + L_LN2 + cb * LN2_RS + ks * 64 + 64 + r * 16), db);
    }
    if (!ALLFRAG) down_frags();
    const f4 d = da + db;
    const float pre = rb(cr == 0 ? d.x : cr == 1 ? d.y : cr == 2 ? d.z : d.w);
    auto sw = __builtin_amdgcn_permlane32_swap(__float_as_uint(pre), __float_as_uint(pre), false, false);
    const float gs = __uint_as_float(sw[0]), us = __uint_as_float(sw[1]);
    const float si = rb(gs * __builtin_amdgcn_rcpf(1.0f + __builtin_amdgcn_exp2f(-gs * LOG2E)));
    if (lane < 32) *(LAS u16*)(L + L_HH + cb * 64 + (8 * t + 4 * r + cr) * 2) = (u16)(pack2(si * us, 0.f) & 0xffffu);
    STAMP(14);
    __syncthreads();
    const v4u a = *(LAS v4u*)(L + L_HH + cb * 64 + r * 16);
    const int zo = OFF_X4 + cb * X4_BLK + u * 128 + (cr & 1) * 64 + r * 16;
    const f4 zero = {0.f, 0.f, 0.f, 0.f};
#pragma unroll
    for (int T0 = 0; T0 < 8; T0 += 4) {
      const f4 z0 = mfma(bw[20 + T0], a, zero), z1 = mfma(bw[21 + T0], a, zero), z2 = mfma(bw[22 + T0], a, zero), z3 = mfma(bw[23 + T0], a, zero);
      const f4 lo = (cr & 1) ? z1 : z0, hi = (cr & 1) ? z3 : z2;
      const f4 y = (cr & 2) ? hi : lo;
      st_sc1(wsr, zo + (5 * t + (T0 >> 1) + (cr >> 1)) * 4 * X4_BLK, enc4(y.x, y.y, y.z, y.w, tag));
      SB();
    }
    { const f4 z8 = mfma(bw[28], a, zero), z9 = mfma(bw[29], a, zero);
      const f4 y = (cr & 1) ? z9 : z8;
      if (cr < 2) st_sc1(wsr, zo + (5 * t + 4) * 4 * X4_BLK, enc4(y.x, y.y, y.z, y.w, tag)); }
    STAMP(6);
  } else if (role == 2) {
    const int pp = lane >> 3, q = lane & 7;
    const v2u xv = ((const GAS v2u*)p.x)[t * 160 + 8 * cs + q];
    f4 hv;
    {
      v4u g[4];
      const int go = OFF_X2 + (cs * 4 + t) * X2_BLK + pp * 128 + q * 16;
  #pragma nounroll
    for (u32 spin = 0;; ++spin) {
        MEMCLOB();
#pragma unroll
        for (int m = 0; m < 4; ++m) g[m] = ld_poll(wsr, go + m * 1024);
        if (__all(ok4(g[0], tag) && ok4(g[1], tag) && ok4(g[2], tag) && ok4(g[3], tag)) || spin > SPIN_CAP) break;
        if (PST) __builtin_amdgcn_s_sleep(PST);
      }
      STAMP(5);
      const f4 y = (dec4(g[0]) + dec4(g[1])) + (dec4(g[2]) + dec4(g[3]));
      hv.x = rb(blo(xv.x) + rb(sum8p(y.x))); hv.y = rb(bhi(xv.x) + rb(sum8p(y.y)));
      hv.z = rb(blo(xv.y) + rb(sum8p(y.z))); hv.w = rb(bhi(xv.y) + rb(sum8p(y.w)));
    }
    if (lane < 8) st_sc1(wsr, OFF_X3 + t * 2560 + cs * 128 + q * 16, enc4(hv.x, hv.y, hv.z, hv.w, tag));
    STAMP(13);
    {
      if (X4DLY) __builtin_amdgcn_s_sleep(X4DLY);
      const int go = OFF_X4 + (cs * 4 + t) * X4_BLK + pp * 128 + q * 16;
      v4u g[7];
      const bool last = pp < NFU - 48;
  #pragma nounroll
    for (u32 spin = 0;; ++spin) {
        MEMCLOB();
#pragma unroll
        for (int m = 0; m < 6; ++m) g[m] = __builtin_amdgcn_raw_buffer_load_b128(wsr, go + m * 1024, 0, AUX_SC1 | X4NT);
        g[6] = __builtin_amdgcn_raw_buffer_load_b128(wsr, go + (last ? 6144 : 0), 0, AUX_SC1 | X4NT);
        bool ok = true;
#pragma unroll
        for (int m = 0; m < 7; ++m) ok &= ok4(g[m], tag);
        if (__all(ok) || spin > SPIN_CAP) break;
      }
      STAMP(14);
      f4 z = ((dec4(g[0]) + dec4(g[1])) + (dec4(g[2]) + dec4(g[3]))) + (dec4(g[4]) + dec4(g[5]));
      if (last) z += dec4(g[6]);
      const float o0 = hv.x + rb(sum8p(z.x)), o1 = hv.y + rb(sum8p(z.y)), o2 = hv.z + rb(sum8p(z.z)), o3 = hv.w + rb(sum8p(z.w));
      if (lane < 8) ((GAS v2u*)p.out)[t * 160 + 8 * cs + q] = v2u{pack2(o0, o1), pack2(o2, o3)};
    }
    if (cs == 0 && t == 0 && lane == 0) __hip_atomic_store((GAS u64*)p.ws, epoch + 1, RLX_AGENT);
    STAMP(6);
  }
  STAMP(7);
  FLUSH_STAMPS();
    return;
  }
  {
    const int w = wid;
#pragma unroll
    for (int n = W0; n < RD; ++n) { wait_vm<W0 - 1>(); ring[n] = ld_nt(kr, kvo, n * 8192); SB(); }
    u32 selA[4];
#pragma unroll
    for (int k = 0; k < 4; ++k) selA[k] = mksel(c & 7, k, c < 8);
    STAMP(8);
#if !(SONLY & 1)
    lds_wait<1>(flags + 3, 1);
#endif
    const v4u qv = *(LAS v4u*)(L + L_Q + c * 16);
    STAMP(1);
#define REFILL(n) { _Pragma("unroll") for (int m_ = issued((n) - 1); m_ < issued(n); ++m_) { if (m_ < 32) ring[m_ % RD] = ld_nt(kr, kvo, m_ * 8192); else ring[m_ % RD] = ld_nt(vr, kvo, (m_ - 32) * 8192); } SB(); }
    float sc[2];
    float x[16];
#define QK_STEP(n) { const v4u kk = ring[(n) % RD]; x[(n) & 15] = dot2(kk.w, qv.w, dot2(kk.z, qv.z, dot2(kk.y, qv.y, dot2(kk.x, qv.x, 0.f)))); REFILL(n) }
#define QK4(n) QK_STEP(n) QK_STEP((n) + 1) QK_STEP((n) + 2) QK_STEP((n) + 3)
#define TR_STEP(HALF, CTRL, BIT) _Pragma("unroll") for (int hh = 0; hh < (HALF); ++hh) { \
      const float lo_ = x[hh], hi_ = x[hh + (HALF)]; const float t1_ = lo_ + dppf<CTRL>(lo_), t2_ = hi_ + dppf<CTRL>(hi_); x[hh] = (c & (BIT)) ? t2_ : t1_; }
#define TR_ALL() TR_STEP(8, XOR8, 8) TR_STEP(4, XOR7, 4) TR_STEP(2, XOR2, 2) TR_STEP(1, XOR1, 1)
    QK4(0) QK4(4) QK4(8) QK4(12)
    TR_ALL()
    sc[0] = rb(rb(x[0]) * 0.08838834764831845f);
    QK4(16) QK4(20) QK4(24) QK4(28)
    TR_ALL()
    sc[1] = rb(rb(x[0]) * 0.08838834764831845f);
    STAMP(2);
    const float mw = wave_max(fmaxf(sc[0], sc[1]));
    const float pe0 = __builtin_amdgcn_exp2f((sc[0] - mw) * LOG2E), pe1 = __builtin_amdgcn_exp2f((sc[1] - mw) * LOG2E);
    { const float lw = wave_sum(pe0 + pe1); if (lane == 0) { mbuf[w] = mw; lbuf[w] = lw; } }
    lds_arrive(flags + 5, lane);
    u32 pb[2];
    pb[0] = pack2(pe0, 0.f); pb[1] = pack2(pe1, 0.f);
    f4 acc = {0.f, 0.f, 0.f, 0.f};
#define PV_STEP(i) { const v4u vv = ring[(32 + (i)) % RD]; \
      const u32 xx = (u32)__builtin_amdgcn_update_dpp(0, (int)pb[(i) >> 4], 0x150 + ((i) & 15), 0xf, 0xf, false); \
      v4u a; a[0] = __builtin_amdgcn_perm(xx, xx, selA[0]); a[1] = __builtin_amdgcn_perm(xx, xx, selA[1]); a[2] = __builtin_amdgcn_perm(xx, xx, selA[2]); a[3] = __builtin_amdgcn_perm(xx, xx, selA[3]); \
      acc = mfma(a, vv, acc); REFILL(32 + (i)) \
      if ((i) == WTRIG && lane == 0) __hip_atomic_fetch_add(flags + 7, 1u, RLX_WG); \
      if ((i) == RTRIG && lane == 0) __hip_atomic_fetch_add(flags + 9, 1u, RLX_WG); }
#define PV4(i) PV_STEP(i) PV_STEP((i) + 1) PV_STEP((i) + 2) PV_STEP((i) + 3)
    PV4(0) PV4(4) PV4(8) PV4(12) PV4(16) PV4(20) PV4(24)
    MEMCLOB();
    u32 iflag = __hip_atomic_load(flags + 8, RLX_WG);
    float fw = *(volatile LAS float*)(L + L_FAC + w * 4);
    u32 stag = *(volatile LAS u32*)(L + L_INFO);
    PV4(28)
#if !(SONLY & (4 | 8))
    if (__builtin_amdgcn_readfirstlane(iflag) == 0u) { lds_wait<0>(flags + 8, 1); fw = *(volatile LAS float*)(L + L_FAC + w * 4); stag = *(volatile LAS u32*)(L + L_INFO); }
#endif
    acc *= fw;
    if (lane < 32) *(LAS f4*)(L + L_RED + w * 512 + (8 * c + 4 * r) * 4) = acc;
    asm volatile("s_waitcnt lgkmcnt(0)" ::: "memory");
    u32 old = 0;
    if (lane == 0) old = __hip_atomic_fetch_add(flags + 6, 1u, RLX_WG);
    old = __builtin_amdgcn_readfirstlane(old);
    STAMP(3);
#if !(SONLY & (4 | 8))
    if (old == 7) {
      MEMCLOB();
      if (lane < 32) {
        f4 sum = {0.f, 0.f, 0.f, 0.f};
#pragma unroll
        for (int ww = 0; ww < 8; ++ww) sum += *(LAS f4*)(L + L_RED + ww * 512 + lane * 16);
        st_sc1(wsr, OFF_X1 + (h * 8 + bs) * 512 + lane * 16, enc4(sum.x, sum.y, sum.z, sum.w, stag));
      }
      STAMP(4);
    }
#endif
    FLUSH_STAMPS();
  }

}

extern "C" void kernel_launch(void* const* d_in, const int* in_sizes, int n_in, void* d_out, int out_size, void* d_ws, size_t ws_size, hipStream_t stream) {
  Params p;
  p.x = (const u16*)d_in[0]; p.K = (const u16*)d_in[1]; p.V = (const u16*)d_in[2]; p.wq = (const u16*)d_in[3]; p.wo = (const u16*)d_in[4];
  p.w1 = (const u16*)d_in[6]; p.w3 = (const u16*)d_in[7]; p.w2 = (const u16*)d_in[8];
  p.out = (u16*)d_out; p.ws = (unsigned char*)d_ws;
  hipLaunchKernelGGL(fused_block, dim3(256), dim3(NTHR), 0, stream, p);
}
```

```cpp
#include <hip/hip_runtime.h>
#include <stdint.h>

typedef unsigned short u16;
typedef unsigned u32;
typedef unsigned long long u64;
typedef u32 v4u __attribute__((ext_vector_type(4)));
typedef u32 v2u __attribute__((ext_vector_type(2)));
typedef float f4 __attribute__((ext_vector_type(4)));
typedef float f2 __attribute__((ext_vector_type(2)));
typedef short s4 __attribute__((ext_vector_type(4)));
typedef __bf16 bf2 __attribute__((ext_vector_type(2)));
typedef __bf16 bf8 __attribute__((ext_vector_type(8)));
#define GAS __attribute__((address_space(1)))
#define LAS __attribute__((address_space(3)))
#define RLX_AGENT __ATOMIC_RELAXED, __HIP_MEMORY_SCOPE_AGENT
#define RLX_WG __ATOMIC_RELAXED, __HIP_MEMORY_SCOPE_WORKGROUP

#define RD 24
#define W0 3
#define W1 4
#define WTRIG 22
#define RTRIG 16
#define DYNROLE 1
#define X4DLY 64
#define X4NT 0
#define OSLOPE 0
#define FDLY 0
#define WT W1
#define MAP 0
#define X3REP 8
#define X4WATCH 1
#define QSLEEP 16
#define PST 0
#define CODEPF 1
#define CODE_BYTES 18816u
#define TAILNT 0
#define W13AUX 2
#define SONLY 0
#define STAMPS 0

namespace {
constexpr int DM = 640, NH = 32, DH = 128, SK = 8192, WIN = 2048, FF = 1728;
constexpr int NTHR = 768;
constexpr int AUX_NT = 2, AUX_SC1 = 16, RSRC_W3 = 0x00020000;
constexpr int OFF_CNT = 0x800;
constexpr int OFF_XL = 0x1000;
constexpr int OFF_XQ = 0x10000;
constexpr int OFF_X1 = 0x90000;
constexpr int OFF_X2 = 0xB0000;
constexpr int X2_BLK = 33 * 128;
constexpr int OFF_X3 = 0x108000;
constexpr int X3_REP = 10240 + 128;
constexpr int OFF_X4 = 0x120000;
constexpr int NFU = 54;
constexpr int X4_BLK = 55 * 128;
constexpr int WS_USED = 0x1B0000;
constexpr size_t OFF_STAMP = 8u << 20;
constexpr u32 SPIN_CAP = 1u << 16;
constexpr float LOG2E = 1.4426950408889634f;

struct Params {
  const u16 *x, *K, *V, *wq, *wo, *w1, *w3, *w2;
  u16* out;
  unsigned char* ws;
};

__device__ __forceinline__ float rb(float f) { return (float)(__bf16)f; }
__device__ __forceinline__ u32 pack2(float lo, float hi) { bf2 v = {(__bf16)lo, (__bf16)hi}; return __builtin_bit_cast(u32, v); }
__device__ __forceinline__ float blo(u32 v) { return __uint_as_float(v << 16); }
__device__ __forceinline__ float bhi(u32 v) { return __uint_as_float(v & 0xffff0000u); }
__device__ __forceinline__ float dot2(u32 a, u32 b, float c) {
  return __builtin_amdgcn_fdot2_f32_bf16(__builtin_bit_cast(bf2, a), __builtin_bit_cast(bf2, b), c, false);
}
__device__ __forceinline__ f4 mfma(v4u a, v4u b, f4 c) {
  return __builtin_amdgcn_mfma_f32_16x16x32_bf16(__builtin_bit_cast(bf8, a), __builtin_bit_cast(bf8, b), c, 0, 0, 0);
}
template <int CTRL> __device__ __forceinline__ u32 dppu(u32 x) { return (u32)__builtin_amdgcn_mov_dpp((int)x, CTRL, 0xf, 0xf, true); }
template <int CTRL> __device__ __forceinline__ float dppf(float x) {
  return __builtin_bit_cast(float, __builtin_amdgcn_mov_dpp(__builtin_bit_cast(int, x), CTRL, 0xf, 0xf, true));
}
constexpr int XOR1 = 0xB1, XOR2 = 0x4E, XOR7 = 0x141, XOR8 = 0x128, XOR15 = 0x140, ROR4 = 0x124;
constexpr int QB1 = 0x55, QB2 = 0xAA, QB3 = 0xFF;
__device__ __forceinline__ float xrow_sum(float x) {
  auto s = __builtin_amdgcn_permlane16_swap(__float_as_uint(x), __float_as_uint(x), false, false);
  x = __uint_as_float(s[0]) + __uint_as_float(s[1]);
  auto t = __builtin_amdgcn_permlane32_swap(__float_as_uint(x), __float_as_uint(x), false, false);
  return __uint_as_float(t[0]) + __uint_as_float(t[1]);
}
__device__ __forceinline__ float xhalf_sum(float x) {
  auto t = __builtin_amdgcn_permlane32_swap(__float_as_uint(x), __float_as_uint(x), false, false);
  return __uint_as_float(t[0]) + __uint_as_float(t[1]);
}
__device__ __forceinline__ float xrow_max(float x) {
  auto s = __builtin_amdgcn_permlane16_swap(__float_as_uint(x), __float_as_uint(x), false, false);
  x = fmaxf(__uint_as_float(s[0]), __uint_as_float(s[1]));
  auto t = __builtin_amdgcn_permlane32_swap(__float_as_uint(x), __float_as_uint(x), false, false);
  return fmaxf(__uint_as_float(t[0]), __uint_as_float(t[1]));
}
__device__ __forceinline__ float wave_sum(float x) {
  x += dppf<XOR1>(x); x += dppf<XOR2>(x); x += dppf<XOR7>(x); x += dppf<XOR15>(x);
  return xrow_sum(x);
}
__device__ __forceinline__ float wave_max(float x) {
  x = fmaxf(x, dppf<XOR1>(x)); x = fmaxf(x, dppf<XOR2>(x)); x = fmaxf(x, dppf<XOR7>(x)); x = fmaxf(x, dppf<XOR15>(x));
  return xrow_max(x);
}
__device__ __forceinline__ float sum8p(float x) { x += dppf<XOR8>(x); return xrow_sum(x); }
__device__ __forceinline__ v4u ld_nt(__amdgpu_buffer_rsrc_t r, int voff, int soff) { return __builtin_amdgcn_raw_buffer_load_b128(r, voff, soff, AUX_NT); }
__device__ __forceinline__ v4u ld_poll_nt(__amdgpu_buffer_rsrc_t r, int voff) { return __builtin_amdgcn_raw_buffer_load_b128(r, voff, 0, AUX_SC1 | AUX_NT); }
__device__ __forceinline__ v4u ld_poll(__amdgpu_buffer_rsrc_t r, int voff) { return __builtin_amdgcn_raw_buffer_load_b128(r, voff, 0, AUX_SC1 | TAILNT); }
__device__ __forceinline__ void st_sc1(__amdgpu_buffer_rsrc_t r, int voff, v4u v) { __builtin_amdgcn_raw_buffer_store_b128(v, r, voff, 0, AUX_SC1); }
template <int AUX = AUX_NT> __device__ __forceinline__ void dma_nt(__amdgpu_buffer_rsrc_t r, LAS unsigned char* dst, int voff, int soff) {
  __builtin_amdgcn_raw_ptr_buffer_load_lds(r, (LAS void*)dst, 16, voff, soff, 0, AUX);
}
__device__ __forceinline__ void dma_asm(__amdgpu_buffer_rsrc_t r, int lds_off, int voff, int soff) {
  u32 keep;
  asm volatile("s_mov_b32 %0, m0\n\ts_mov_b32 m0, %1\n\ts_nop 0\n\tbuffer_load_dwordx4 %2, %3, %4 offen nt lds\n\ts_mov_b32 m0, %0"
               : "=&s"(keep) : "s"(lds_off), "v"(voff), "s"(r), "s"(soff) : "memory");
}
__device__ __forceinline__ __amdgpu_buffer_rsrc_t mkrsrc(const void* p, int bytes) { return __builtin_amdgcn_make_buffer_rsrc((void*)p, 0, bytes, RSRC_W3); }
__device__ __forceinline__ u32 enc1(float a, u32 tag) { return ((__float_as_uint(a) + 0x200u) & ~0x3ffu) | tag; }
__device__ __forceinline__ v4u enc4(float a, float b, float c, float d, u32 tag) { return v4u{enc1(a, tag), enc1(b, tag), enc1(c, tag), enc1(d, tag)}; }
__device__ __forceinline__ bool ok4(v4u g, u32 tag) { return ((((g.x ^ tag) | (g.y ^ tag)) | ((g.z ^ tag) | (g.w ^ tag))) & 0x3ffu) == 0u; }
__device__ __forceinline__ u32 bad4(v4u g, u32 tag) { return ((g.x ^ tag) | (g.y ^ tag)) | ((g.z ^ tag) | (g.w ^ tag)); }
__device__ __forceinline__ float dec(u32 w) { return __uint_as_float(w & ~0x3ffu); }
__device__ __forceinline__ f4 dec4(v4u g) { return f4{dec(g.x), dec(g.y), dec(g.z), dec(g.w)}; }

__device__ __forceinline__ void ln_finish(float pivot, float s1, float s2, float& mb, float& ri) {
  const float m1 = wave_sum(s1) * (1.0f / DM), m2 = wave_sum(s2) * (1.0f / DM);
  const float var = fmaxf(m2 - m1 * m1, 0.f);
  mb = rb(pivot + m1);
  ri = __builtin_amdgcn_rcpf(rb(__builtin_amdgcn_sqrtf(rb(rb(var) + 1e-5f))));
}
__device__ __forceinline__ float first_lane(float x) { return __uint_as_float(__builtin_amdgcn_readfirstlane(__float_as_uint(x))); }
__device__ __forceinline__ u32 mksel(int j, int k, bool act) {
  const u32 s = (j & 1) ? 0x01000c0cu : 0x0c0c0100u;
  return (act && (j >> 1) == k) ? s : 0x0c0c0c0cu;
}
__device__ __forceinline__ v4u trfrag(LAS unsigned char* a0, int step4) {
  const v2u lo = __builtin_bit_cast(v2u, __builtin_amdgcn_ds_read_tr16_b64_v4i16((LAS s4*)a0));
  const v2u hi = __builtin_bit_cast(v2u, __builtin_amdgcn_ds_read_tr16_b64_v4i16((LAS s4*)(a0 + step4)));
  return v4u{lo.x, lo.y, hi.x, hi.y};
}
__device__ __forceinline__ void lds_arrive(LAS u32* cnt, int lane) {
  asm volatile("s_waitcnt lgkmcnt(0)" ::: "memory");
  if (lane == 0) __hip_atomic_fetch_add(cnt, 1u, RLX_WG);
}
template <int SL> __device__ __forceinline__ void lds_wait(LAS u32* cnt, u32 n) {
#pragma nounroll
  for (u32 spin = 0; spin < (1u << 22); ++spin) { if (__hip_atomic_load(cnt, RLX_WG) >= n) break; __builtin_amdgcn_s_sleep(SL); }
  asm volatile("" ::: "memory");
}
#define STAMP(n) do {} while (0)
#define PCNT(n, v) do {} while (0)
#define FLUSH_STAMPS() do {} while (0)
#define SB() __builtin_amdgcn_sched_barrier(0)
#define MEMCLOB() asm volatile("" ::: "memory")
template <int N> __device__ __forceinline__ void wait_vm() { asm volatile("s_waitcnt vmcnt(%0)" ::"n"(N) : "memory"); }

constexpr int window(int n) { return (n >= 32 - 2 * (WT - W1) && n < 32) ? W1 + (n - (32 - 2 * (WT - W1)) + 2) / 2 : W1; }
constexpr int issued(int n) {
  int v = RD;
  for (int k = 0; k <= n; ++k) { int w = k + 1 + window(k); if (w > k + 1 + RD - 1) w = k + RD; if (w > 64) w = 64; if (w > v) v = w; }
  return v;
}
__device__ __forceinline__ int ffn_unit(int rk, int grp) { return rk < 6 ? 2 * (grp + 8 * (rk >> 1)) + (rk & 1) : rk == 6 ? 48 + grp : NFU; }
constexpr int L_FLAG = 0;
constexpr int L_STAT = 64;
constexpr int L_INFO = 96;
constexpr int L_FAC = 192;
constexpr int L_M = 128;
constexpr int L_Q = 256;
constexpr int L_HH = 512;
constexpr int L_O = 1024;
constexpr int O_RS = 320;
constexpr int L_LN2 = 3328;
constexpr int LN2_RS = 1344;
constexpr int L_RED = 8704;
constexpr int L_W = L_RED + 8192;
constexpr int L_W2 = L_W + 2 * 640 * 64;
constexpr int L_BYTES = L_W2 + 32 * 1280;
}

extern "C" __global__ void __launch_bounds__(NTHR, 3) fused_block(Params p) {
  __shared__ __attribute__((aligned(16))) unsigned char smem[L_BYTES];
  LAS unsigned char* L = (LAS unsigned char*)smem;
  const int tid = threadIdx.x, lane = tid & 63, wid = __builtin_amdgcn_readfirstlane(tid >> 6);
  const int bid = blockIdx.x;
  const int r = lane >> 4, c = lane & 15;
  asm volatile("" :: "s"(p.x), "s"(p.K), "s"(p.V), "s"(p.wq), "s"(p.wo), "s"(p.w1), "s"(p.w3), "s"(p.w2), "s"(p.out), "s"(p.ws));
  STAMP(0);
  const int h = (bid & 7) + 8 * (bid >> 6), bs = (bid >> 3) & 7, b = bs >> 1, s = bs & 1;
  LAS u32* flags = (LAS u32*)(L + L_FLAG);
  LAS float* mbuf = (LAS float*)(L + L_M);
  LAS float* lbuf = mbuf + 8;
  const __amdgpu_buffer_rsrc_t wsr = mkrsrc(p.ws, WS_USED);
  const int t = wid & 3;
  const int bsel = (lane >> 3) & 1;

  const size_t kvoff = ((size_t)(b * NH + h) * SK + (SK - WIN) + s * 1024) * DH;
  const __amdgpu_buffer_rsrc_t kr = mkrsrc(p.K + kvoff, 1024 * DH * 2), vr = mkrsrc(p.V + kvoff, 1024 * DH * 2);
  const int kvo = wid * 1024 + lane * 16;
  v4u ring[RD];
  u32 xr[5];
  v4u wqv[5];
  u64 epoch = 0;
  if (wid < 8) {
#pragma unroll
    for (int n = 0; n < W0; ++n) ring[n] = ld_nt(kr, kvo, n * 8192);
  } else {
    const int row0 = 80 * bs + 20 * t + r;
#pragma unroll
    for (int k = 0; k < 5; ++k) xr[k] = ((const GAS u32*)p.x)[t * 320 + lane + 64 * k];
    const __amdgpu_buffer_rsrc_t qr = mkrsrc(p.wq + h * DH, DM * NH * DH * 2 - h * DH * 2);
#pragma unroll
    for (int i = 0; i < 5; ++i) wqv[i] = ld_nt(qr, row0 * (NH * DH * 2) + c * 16, i * 4 * (NH * DH * 2));
    epoch = __hip_atomic_load((GAS u64*)p.ws, RLX_AGENT);
    if (wid == 8 && lane < 16) flags[lane] = 0u;
  }
  STAMP(9);
  SB();
  __syncthreads();

  if (__builtin_expect(wid >= 8, 1)) {
  u32 pfv = 0;
  {
    u64 pc; u32 off;
    asm volatile("s_getpc_b64 %0\n.Lpf_here%=:\n\ts_mov_b32 %1, .Lpf_here%=-fused_block" : "=s"(pc), "=s"(off));
    if (bid < 8) {
      const u32 o = (u32)t * 8192u + lane * 128u;
      if (o < CODE_BYTES) pfv = *(const GAS u32*)(pc - off + o);
    }
  }
  u32 tag, t16;
  {
    LAS u32* xw = (LAS u32*)(L + L_LN2 + t * LN2_RS);
#pragma unroll
    for (int k = 0; k < 5; ++k) xw[lane + 64 * k] = xr[k];
    const float pivot = first_lane(blo(xr[0]));
    float s1 = 0.f, s2 = 0.f;
#pragma unroll
    for (int k = 0; k < 5; ++k) { const float d0 = blo(xr[k]) - pivot, d1 = bhi(xr[k]) - pivot; s1 += d0 + d1; s2 = fmaf(d0, d0, fmaf(d1, d1, s2)); }
    float mb, ri;
    ln_finish(pivot, s1, s2, mb, ri);
    if (lane == 0) *(LAS f2*)(L + L_STAT + t * 8) = f2{mb, ri};
    lds_arrive(flags + 0, lane);
    u32 sel[4];
#pragma unroll
    for (int k = 0; k < 4; ++k) sel[k] = mksel(lane & 7, k, true);
    lds_wait<0>(flags + 0, 4);
    f2 st[2];
    st[0] = *(LAS f2*)(L + L_STAT + bsel * 8); st[1] = *(LAS f2*)(L + L_STAT + (2 + bsel) * 8);
    u32 xe[5][2];
#pragma unroll
    for (int i = 0; i < 5; ++i)
#pragma unroll
      for (int P = 0; P < 2; ++P) xe[i][P] = *(LAS u16*)(L + L_LN2 + (2 * P + bsel) * LN2_RS + (80 * bs + 20 * t + r + 4 * i) * 2);
    f4 acc[2] = {{0.f, 0.f, 0.f, 0.f}, {0.f, 0.f, 0.f, 0.f}};
#pragma unroll
    for (int i = 0; i < 5; ++i)
#pragma unroll
      for (int P = 0; P < 2; ++P) {
        const u32 lv = pack2(rb(blo(xe[i][P]) - st[P].x) * st[P].y, 0.f);
        v4u a;
#pragma unroll
        for (int k = 0; k < 4; ++k) a[k] = __builtin_amdgcn_perm(lv, lv, sel[k]);
        acc[P] = mfma(a, wqv[i], acc[P]);
      }
#pragma unroll
    for (int P = 0; P < 2; ++P) *(LAS f4*)(L + L_RED + t * 2048 + P * 1024 + lane * 16) = acc[P];
    lds_arrive(flags + 1, lane);
    tag = __builtin_amdgcn_readfirstlane(((u32)epoch + 1u) & 0x3ffu);
    t16 = __builtin_amdgcn_readfirstlane(((u32)epoch + 1u) & 0xffffu);
    lds_wait<0>(flags + 1, 4);
    if (lane < 32) {
      f4 sum = {0.f, 0.f, 0.f, 0.f};
#pragma unroll
      for (int ww = 0; ww < 4; ++ww) sum += *(LAS f4*)(L + L_RED + ww * 2048 + (t >> 1) * 1024 + (16 * (2 * (t & 1) + (lane & 1)) + (lane >> 1)) * 16);
      st_sc1(wsr, OFF_XQ + ((h * 4 + t) * 8 + bs) * 512 + lane * 16, enc4(sum.x, sum.y, sum.z, sum.w, tag));
    }
  }
  STAMP(8);
  if (bid < 8 && t == 1 && lane == 0) __hip_atomic_store((GAS u32*)(p.ws + OFF_CNT + bid * 128), t16 << 16, RLX_AGENT);
  if (t == 0) {
    if (QSLEEP) __builtin_amdgcn_s_sleep(QSLEEP);
    v4u g[4];
    const int go = OFF_XQ + (h * 4 + b) * 4096 + lane * 16;
#pragma nounroll
    for (u32 spin = 0;; ++spin) {
      MEMCLOB();
      u32 bad = 0;
#pragma unroll
      for (int k = 0; k < 4; ++k) g[k] = ld_poll_nt(wsr, go + k * 1024);
#pragma unroll
      for (int k = 0; k < 4; ++k) bad |= bad4(g[k], tag);
      if (__all((bad & 0x3ffu) == 0u) || spin > SPIN_CAP) break;
      __builtin_amdgcn_s_sleep(4);
    }
    const f4 sum = (dec4(g[0]) + dec4(g[1])) + (dec4(g[2]) + dec4(g[3]));
    const float q0 = xhalf_sum(sum.x), q1 = xhalf_sum(sum.y), q2 = xhalf_sum(sum.z), q3 = xhalf_sum(sum.w);
    if (lane < 32) *(LAS v2u*)(L + L_Q + lane * 8) = v2u{pack2(q0, q1), pack2(q2, q3)};
    lds_arrive(flags + 3, lane);
    STAMP(1);
    lds_wait<16>(flags + 5, 8);
    const float mw = mbuf[lane & 7], lw = lbuf[lane & 7];
    float M = fmaxf(mw, dppf<XOR1>(mw)); M = fmaxf(M, dppf<XOR2>(M)); M = fmaxf(M, dppf<XOR7>(M));
    const float ew = __builtin_amdgcn_exp2f((mw - M) * LOG2E);
    float Ls = lw * ew; Ls += dppf<XOR1>(Ls); Ls += dppf<XOR2>(Ls); Ls += dppf<XOR7>(Ls);
    Ls = first_lane(Ls);
    const v4u mine = enc4(M, Ls, M, Ls, tag);
    if (lane == 0) st_sc1(wsr, OFF_XL + (h * 8 + bs) * 128, mine);
    v4u gl;
#pragma nounroll
    for (u32 spin = 0;; ++spin) {
      MEMCLOB();
      gl = ld_poll_nt(wsr, OFF_XL + (h * 8 + (bs ^ 1)) * 128);
      if (__all(ok4(gl, tag)) || spin > SPIN_CAP) break;
      __builtin_amdgcn_s_sleep(16);
    }
    const float M0 = dec(mine.x), L0 = dec(mine.y), M1 = dec(gl.x), L1 = dec(gl.y), Mx = fmaxf(M0, M1);
    const float e0 = L0 * __builtin_amdgcn_exp2f((M0 - Mx) * LOG2E), e1 = L1 * __builtin_amdgcn_exp2f((M1 - Mx) * LOG2E);
    if (lane < 8) *(LAS float*)(L + L_FAC + lane * 4) = __builtin_amdgcn_exp2f((mw - Mx) * LOG2E) * __builtin_amdgcn_rcpf(e0 + e1);
    if (lane == 0) *(LAS u32*)(L + L_INFO) = tag;
    lds_arrive(flags + 8, lane);
    STAMP(2);
  }
  if (t == 1) {
#pragma nounroll
    for (u32 spin = 0; spin <= SPIN_CAP; ++spin) {
      MEMCLOB();
      const u32 v = __builtin_amdgcn_raw_buffer_load_b32(wsr, OFF_CNT + (bid & 7) * 128, 0, AUX_SC1 | AUX_NT);
      if (__builtin_amdgcn_readfirstlane(v >> 16) == t16) break;
      __builtin_amdgcn_s_sleep(16);
    }
    lds_wait<8>(flags + 9, 8);
    u32 old = 0;
    if (lane == 0) old = __hip_atomic_fetch_add((GAS u32*)(p.ws + OFF_CNT + (bid & 7) * 128), 1u, RLX_AGENT);
    if (lane == 0) *(LAS u32*)(L + L_INFO + 8) = old & 31u;
    lds_arrive(flags + 10, lane);
  }
  asm volatile("" :: "v"(pfv));
  lds_wait<8>(flags + 10, 1);
  const int rk = DYNROLE ? __builtin_amdgcn_readfirstlane(*(LAS u32*)(L + L_INFO + 8)) : (bid >> 3), grp = bid & 7;
  const int u = ffn_unit(rk, grp);
  const int role = rk >= 12 ? 0 : (rk < 7 && u < NFU) ? 1 : 3;
  const int task = grp * 10 + rk - 12, cs = task >> 2, sb = task & 3;
  const bool owner = rk >= 12 && rk < 22 && t == 0;
  const int uo = grp * 20 + rk - 12, hh_o = uo / 5, j_o = uo - 5 * hh_o;
  const bool ffn = role == 1;
  const int q4 = (lane & 15) >> 2, p4 = lane & 3;
  v4u bw[20];
  const int sw2 = 2 * (q4 + 4 * (r & 1));
  lds_wait<8>(flags + 7, 8);
  if (role == 0 || ffn) {
    { const int nsl = ffn ? FDLY : ((31 - rk) * OSLOPE) >> 3;
#pragma nounroll
      for (int i = 0; i < nsl; ++i) __builtin_amdgcn_s_sleep(4); }
    STAMP(10);
    if (role == 0) {
      const __amdgpu_buffer_rsrc_t orr = mkrsrc(p.wo + (size_t)hh_o * DH * DM + 128 * j_o, DH * DM * 2 - 256 * j_o);
      const int vo0 = (32 * t + r) * (DM * 2) + (c ^ (2 * r)) * 16, vo1 = (32 * t + r) * (DM * 2) + (c ^ (2 * r + 8)) * 16;
#pragma unroll
      for (int i = 0; i < 8; ++i) dma_nt(orr, L + L_W + (32 * t + 4 * i) * 256, (i & 2) ? vo1 : vo0, i * 4 * DM * 2);
    } else {
      const __amdgpu_buffer_rsrc_t w1r = mkrsrc(p.w1 + 32 * u, DM * FF * 2 - 64 * u), w3r = mkrsrc(p.w3 + 32 * u, DM * FF * 2 - 64 * u);
      const int rw = lane >> 2, c4 = lane & 3, gp = (rw >> 3) & 1;
      const int vo1 = (160 * t + rw) * (FF * 2) + (c4 ^ gp) * 16, vo3 = (160 * t + rw) * (FF * 2) + (c4 ^ (2 + gp)) * 16;
#pragma unroll
      for (int i = 0; i < 10; ++i) dma_nt<W13AUX>(w1r, L + L_W + (160 * t + 16 * i) * 64, vo1, i * 16 * FF * 2);
#pragma unroll
      for (int i = 0; i < 10; ++i) dma_nt<W13AUX>(w3r, L + L_W + 640 * 64 + (160 * t + 16 * i) * 64, vo3, i * 16 * FF * 2);
    }
    wait_vm<0>();
    lds_arrive(flags + 2, lane);
    lds_wait<0>(flags + 2, 4);
    STAMP(11);
    if (role == 0) {
#pragma unroll
      for (int T = 0; T < 2; ++T)
#pragma unroll
        for (int ks = 0; ks < 4; ++ks) bw[T * 4 + ks] = trfrag(L + L_W + (32 * ks + 8 * r + q4) * 256 + (((4 * t + 2 * T + (p4 >> 1)) ^ sw2) * 16) + (p4 & 1) * 8, 4 * 256);
    } else {
#pragma unroll
      for (int ks = 0; ks < 20; ++ks) bw[ks] = trfrag(L + L_W + (p4 >> 1) * (640 * 64) + (32 * ks + 8 * r + q4) * 64 + ((t ^ (2 * (p4 >> 1) + (r & 1))) * 16) + (p4 & 1) * 8, 4 * 64);
    }
    STAMP(12);
  }
  const int cb = c & 3, cr = c >> 2;
  if (role == 0) {
    v4u g;
    const int go = OFF_X1 + (hh_o * 8 + 2 * t) * 512 + lane * 16;
#pragma nounroll
    for (u32 spin = 0;; ++spin) {
      MEMCLOB();
      g = ld_poll(wsr, go);
      if (__all(ok4(g, tag)) || spin > SPIN_CAP) break;
      if (PST) __builtin_amdgcn_s_sleep(PST);
    }
    STAMP(5);
    const f4 og = dec4(g);
    const float o0 = xhalf_sum(og.x), o1 = xhalf_sum(og.y), o2 = xhalf_sum(og.z), o3 = xhalf_sum(og.w);
    if (lane < 32) *(LAS v2u*)(L + L_O + t * O_RS + lane * 8) = v2u{pack2(o0, o1), pack2(o2, o3)};
    __syncthreads();
    f4 d0 = {0.f, 0.f, 0.f, 0.f}, d1 = {0.f, 0.f, 0.f, 0.f};
#pragma unroll
    for (int ks = 0; ks < 4; ++ks) {
      const v4u a = *(LAS v4u*)(L + L_O + cb * O_RS + ks * 64 + r * 16);
      d0 = mfma(bw[ks], a, d0); d1 = mfma(bw[4 + ks], a, d1);
    }
    const f4 y = (cr & 1) ? d1 : d0;
    if (cr < 2) st_sc1(wsr, OFF_X2 + ((4 * j_o + t) * 4 + cb) * X2_BLK + hh_o * 128 + cr * 64 + r * 16, enc4(y.x, y.y, y.z, y.w, tag));
    STAMP(6);
  } else if (ffn) {
    v4u g[3];
    const int go = OFF_X3 + (X3REP > 1 ? grp * X3_REP : 0) + t * 2560 + lane * 16;
    const bool has2 = lane < 32;
#pragma nounroll
    for (u32 spin = 0;; ++spin) {
      MEMCLOB();
      g[0] = ld_poll(wsr, go); g[1] = ld_poll(wsr, go + 1024); g[2] = ld_poll(wsr, go + (has2 ? 2048 : 0));
      if (__all(((bad4(g[0], tag) | bad4(g[1], tag) | bad4(g[2], tag)) & 0x3ffu) == 0u) || spin > SPIN_CAP) break;
      if (PST) __builtin_amdgcn_s_sleep(PST);
    }
    STAMP(5);
    const float pivot = first_lane(dec(g[0].x));
    float s1 = 0.f, s2 = 0.f;
#pragma unroll
    for (int k = 0; k < 3; ++k)
#pragma unroll
      for (int e = 0; e < 4; ++e) { const float d = (k < 2 || has2) ? dec(g[k][e]) - pivot : 0.f; s1 += d; s2 = fmaf(d, d, s2); }
    float mb, ri;
    ln_finish(pivot, s1, s2, mb, ri);
    LAS unsigned char* lp = L + L_LN2 + t * LN2_RS + lane * 8;
#pragma unroll
    for (int k = 0; k < 3; ++k)
      if (k < 2 || has2) *(LAS v2u*)(lp + 512 * k) = v2u{pack2(rb(dec(g[k].x) - mb) * ri, rb(dec(g[k].y) - mb) * ri), pack2(rb(dec(g[k].z) - mb) * ri, rb(dec(g[k].w) - mb) * ri)};
    __syncthreads();
    STAMP(13);
    f4 da = {0.f, 0.f, 0.f, 0.f}, db = {0.f, 0.f, 0.f, 0.f};
#pragma unroll
    for (int ks = 0; ks < 20; ks += 2) {
      da = mfma(bw[ks], *(LAS v4u*)(L + L_LN2 + cb * LN2_RS + ks * 64 + r * 16), da);
      db = mfma(bw[ks + 1], *(LAS v4u*)(L + L_LN2 + cb * LN2_RS + ks * 64 + 64 + r * 16), db);
    }
    const f4 d = da + db;
    const float pre = rb(cr == 0 ? d.x : cr == 1 ? d.y : cr == 2 ? d.z : d.w);
    auto sw = __builtin_amdgcn_permlane32_swap(__float_as_uint(pre), __float_as_uint(pre), false, false);
    const float gs = __uint_as_float(sw[0]), us = __uint_as_float(sw[1]);
    const float si = rb(gs * __builtin_amdgcn_rcpf(1.0f + __builtin_amdgcn_exp2f(-gs * LOG2E)));
    if (lane < 32) *(LAS u16*)(L + L_HH + cb * 64 + (8 * t + 4 * r + cr) * 2) = (u16)(pack2(si * us, 0.f) & 0xffffu);
    STAMP(14);
    __syncthreads();
    STAMP(6);
  }
  if (owner) {
    const int pp = lane >> 3, q = lane & 7;
    const v2u xv = *(LAS v2u*)(L + L_LN2 + sb * LN2_RS + (32 * cs + 4 * q) * 2);
    f4 hv;
    {
      v4u g[4];
      const int go = OFF_X2 + (cs * 4 + sb) * X2_BLK + pp * 128 + q * 16;
  #pragma nounroll
    for (u32 spin = 0;; ++spin) {
        MEMCLOB();
#pragma unroll
        for (int m = 0; m < 4; ++m) g[m] = ld_poll(wsr, go + m * 1024);
        if (__all((((bad4(g[0], tag) | bad4(g[1], tag)) | (bad4(g[2], tag) | bad4(g[3], tag))) & 0x3ffu) == 0u) || spin > SPIN_CAP) break;
        if (PST) __builtin_amdgcn_s_sleep(PST);
      }
      STAMP(3);
      const f4 y = (dec4(g[0]) + dec4(g[1])) + (dec4(g[2]) + dec4(g[3]));
      hv.x = rb(blo(xv.x) + rb(sum8p(y.x))); hv.y = rb(bhi(xv.x) + rb(sum8p(y.y)));
      hv.z = rb(blo(xv.y) + rb(sum8p(y.z))); hv.w = rb(bhi(xv.y) + rb(sum8p(y.w)));
    }
    if (pp < X3REP) st_sc1(wsr, OFF_X3 + pp * X3_REP + sb * 2560 + cs * 128 + q * 16, enc4(hv.x, hv.y, hv.z, hv.w, tag));
    STAMP(4);
    {
      { const int gh = OFF_X3 + (X3REP > 1 ? grp * X3_REP : 0) + sb * 2560 + lane * 16;
#pragma nounroll
        for (u32 spin = 0;; ++spin) {
          MEMCLOB();
          const v4u a0 = ld_poll(wsr, gh), a1 = ld_poll(wsr, gh + 1024), a2 = ld_poll(wsr, gh + (lane < 32 ? 2048 : 0));
          if (__all(((bad4(a0, tag) | bad4(a1, tag) | bad4(a2, tag)) & 0x3ffu) == 0u) || spin > SPIN_CAP) break;
        } }
      if (X4DLY) __builtin_amdgcn_s_sleep(X4DLY);
      const int go = OFF_X4 + (cs * 4 + sb) * X4_BLK + pp * 128 + q * 16;
      v4u g[7];
      const bool last = pp < NFU - 48;
  #pragma nounroll
    for (u32 spin = 0;; ++spin) {
        MEMCLOB();
#pragma unroll
        for (int m = 0; m < 6; ++m) g[m] = __builtin_amdgcn_raw_buffer_load_b128(wsr, go + m * 1024, 0, AUX_SC1 | X4NT);
        g[6] = __builtin_amdgcn_raw_buffer_load_b128(wsr, go + (last ? 6144 : 0), 0, AUX_SC1 | X4NT);
        u32 bad = 0;
#pragma unroll
        for (int m = 0; m < 7; ++m) bad |= bad4(g[m], tag);
        if (__all((bad & 0x3ffu) == 0u) || spin > SPIN_CAP) break;
      }
      STAMP(13);
      f4 z = ((dec4(g[0]) + dec4(g[1])) + (dec4(g[2]) + dec4(g[3]))) + (dec4(g[4]) + dec4(g[5]));
      if (last) z += dec4(g[6]);
      const float o0 = hv.x + rb(sum8p(z.x)), o1 = hv.y + rb(sum8p(z.y)), o2 = hv.z + rb(sum8p(z.z)), o3 = hv.w + rb(sum8p(z.w));
      if (lane < 8) ((GAS v2u*)p.out)[sb * 160 + 8 * cs + q] = v2u{pack2(o0, o1), pack2(o2, o3)};
    }
    if (task == 0 && lane == 0) __hip_atomic_store((GAS u64*)p.ws, epoch + 1, RLX_AGENT);
    STAMP(14);
  }
  STAMP(7);
  FLUSH_STAMPS();
    return;
  }
  {
    const int w = wid;
#pragma unroll
    for (int n = W0; n < RD; ++n) { wait_vm<W0 - 1>(); ring[n] = ld_nt(kr, kvo, n * 8192); SB(); }
    u32 selA[4];
#pragma unroll
    for (int k = 0; k < 4; ++k) selA[k] = mksel(c & 7, k, c < 8);
    STAMP(8);
#if !(SONLY & 1)
    lds_wait<1>(flags + 3, 1);
#endif
    const v4u qv = *(LAS v4u*)(L + L_Q + c * 16);
    STAMP(1);
#define REFILL(n) { _Pragma("unroll") for (int m_ = issued((n) - 1); m_ < issued(n); ++m_) { if (m_ < 32) ring[m_ % RD] = ld_nt(kr, kvo, m_ * 8192); else ring[m_ % RD] = ld_nt(vr, kvo, (m_ - 32) * 8192); } SB(); }
    float sc[2];
    float x[16];
#define QK_STEP(n) { const v4u kk = ring[(n) % RD]; x[(n) & 15] = dot2(kk.w, qv.w, dot2(kk.z, qv.z, dot2(kk.y, qv.y, dot2(kk.x, qv.x, 0.f)))); REFILL(n) }
#define QK4(n) QK_STEP(n) QK_STEP((n) + 1) QK_STEP((n) + 2) QK_STEP((n) + 3)
#define TR_STEP(HALF, CTRL, BIT) _Pragma("unroll") for (int hh = 0; hh < (HALF); ++hh) { \
      const float lo_ = x[hh], hi_ = x[hh + (HALF)]; const float t1_ = lo_ + dppf<CTRL>(lo_), t2_ = hi_ + dppf<CTRL>(hi_); x[hh] = (c & (BIT)) ? t2_ : t1_; }
#define TR_ALL() TR_STEP(8, XOR8, 8) TR_STEP(4, XOR7, 4) TR_STEP(2, XOR2, 2) TR_STEP(1, XOR1, 1)
    QK4(0) QK4(4) QK4(8) QK4(12)
    TR_ALL()
    sc[0] = rb(rb(x[0]) * 0.08838834764831845f);
    QK4(16) QK4(20) QK4(24) QK4(28)
    TR_ALL()
    sc[1] = rb(rb(x[0]) * 0.08838834764831845f);
    STAMP(2);
    const float mw = wave_max(fmaxf(sc[0], sc[1]));
    const float pe0 = __builtin_amdgcn_exp2f((sc[0] - mw) * LOG2E), pe1 = __builtin_amdgcn_exp2f((sc[1] - mw) * LOG2E);
    { const float lw = wave_sum(pe0 + pe1); if (lane == 0) { mbuf[w] = mw; lbuf[w] = lw; } }
    lds_arrive(flags + 5, lane);
    u32 pb[2];
    pb[0] = pack2(pe0, 0.f); pb[1] = pack2(pe1, 0.f);
    f4 acc = {0.f, 0.f, 0.f, 0.f};
#define PV_STEP(i) { const v4u vv = ring[(32 + (i)) % RD]; \
      const u32 xx = (u32)__builtin_amdgcn_update_dpp(0, (int)pb[(i) >> 4], 0x150 + ((i) & 15), 0xf, 0xf, false); \
      v4u a; a[0] = __builtin_amdgcn_perm(xx, xx, selA[0]); a[1] = __builtin_amdgcn_perm(xx, xx, selA[1]); a[2] = __builtin_amdgcn_perm(xx, xx, selA[2]); a[3] = __builtin_amdgcn_perm(xx, xx, selA[3]); \
      acc = mfma(a, vv, acc); REFILL(32 + (i)) \
      if ((i) == WTRIG && lane == 0) __hip_atomic_fetch_add(flags + 7, 1u, RLX_WG); \
      if ((i) == RTRIG && lane == 0) __hip_atomic_fetch_add(flags + 9, 1u, RLX_WG); }
#define PV4(i) PV_STEP(i) PV_STEP((i) + 1) PV_STEP((i) + 2) PV_STEP((i) + 3)
    PV4(0) PV4(4) PV4(8) PV4(12) PV4(16) PV4(20) PV4(24)
    MEMCLOB();
    u32 iflag = __hip_atomic_load(flags + 8, RLX_WG);
    float fw = *(volatile LAS float*)(L + L_FAC + w * 4);
    u32 stag = *(volatile LAS u32*)(L + L_INFO);
    PV4(28)
#if !(SONLY & (4 | 8))
    if (__builtin_amdgcn_readfirstlane(iflag) == 0u) { lds_wait<0>(flags + 8, 1); fw = *(volatile LAS float*)(L + L_FAC + w * 4); stag = *(volatile LAS u32*)(L + L_INFO); }
#endif
    acc *= fw;
    if (lane < 32) *(LAS f4*)(L + L_RED + w * 512 + (8 * c + 4 * r) * 4) = acc;
    asm volatile("s_waitcnt lgkmcnt(0)" ::: "memory");
    u32 old = 0;
    if (lane == 0) old = __hip_atomic_fetch_add(flags + 6, 1u, RLX_WG);
    old = __builtin_amdgcn_readfirstlane(old);
    STAMP(3);
#if !(SONLY & (4 | 8))
    if (old == 7) {
      MEMCLOB();
      if (lane < 32) {
        f4 sum = {0.f, 0.f, 0.f, 0.f};
#pragma unroll
        for (int ww = 0; ww < 8; ++ww) sum += *(LAS f4*)(L + L_RED + ww * 512 + lane * 16);
        st_sc1(wsr, OFF_X1 + (h * 8 + bs) * 512 + lane * 16, enc4(sum.x, sum.y, sum.z, sum.w, stag));
      }
      STAMP(4);
    }
#endif
#if !(SONLY & (4 | 8))
    if (w < 4) {
      lds_wait<8>(flags + 10, 1);
      const int rk = DYNROLE ? __builtin_amdgcn_readfirstlane(*(LAS u32*)(L + L_INFO + 8)) : (bid >> 3);
      const int u = ffn_unit(rk, bid & 7);
      if (u < NFU) {
        const int t = w, q4 = c >> 2, p4 = c & 3, cb = c & 3, cr = c >> 2;
        const __amdgpu_buffer_rsrc_t w2r = mkrsrc(p.w2 + (size_t)32 * u * DM, 32 * DM * 2);
        const int vo2 = r * (DM * 2) + (c ^ (2 * (r + 4 * (t & 1)))) * 16;
#pragma unroll
        for (int i = 0; i < 10; ++i) dma_nt(w2r, L + L_W2 + (10 * t + i) * 1024, vo2, t * (8 * DM * 2) + (i / 5) * 4 * DM * 2 + (i % 5) * 256);
        wait_vm<0>();
        lds_arrive(flags + 11, lane);
        lds_wait<1>(flags + 11, 4);
        const int sw2 = 2 * (q4 + 4 * (r & 1));
        v4u bd[10];
#pragma unroll
        for (int T = 0; T < 10; ++T) { const int col = 160 * t + 16 * T + 4 * p4; bd[T] = trfrag(L + L_W2 + ((2 * r) * 5 + (col >> 7)) * 1024 + q4 * 256 + ((((col & 127) >> 3) ^ sw2) * 16) + (p4 & 1) * 8, 5 * 1024); }
        asm volatile("" :: "v"(bd[0]), "v"(bd[1]), "v"(bd[2]), "v"(bd[3]), "v"(bd[4]), "v"(bd[5]), "v"(bd[6]), "v"(bd[7]), "v"(bd[8]), "v"(bd[9]));
        STAMP(10);
        __syncthreads();
        __syncthreads();
        STAMP(11);
        const v4u a = *(LAS v4u*)(L + L_HH + cb * 64 + r * 16);
        const int zo = OFF_X4 + cb * X4_BLK + u * 128 + (cr & 1) * 64 + r * 16;
        const f4 zero = {0.f, 0.f, 0.f, 0.f};
        const f4 z0 = mfma(bd[0], a, zero), z1 = mfma(bd[1], a, zero), z2 = mfma(bd[2], a, zero), z3 = mfma(bd[3], a, zero), z4 = mfma(bd[4], a, zero);
        const f4 z5 = mfma(bd[5], a, zero), z6 = mfma(bd[6], a, zero), z7 = mfma(bd[7], a, zero), z8 = mfma(bd[8], a, zero), z9 = mfma(bd[9], a, zero);
        const bool c1 = cr & 1, c2 = cr & 2;
        auto pick = [&](const f4& a0, const f4& a1, const f4& a2, const f4& a3) {
          f4 y;
          y.x = c2 ? (c1 ? a3.x : a2.x) : (c1 ? a1.x : a0.x); y.y = c2 ? (c1 ? a3.y : a2.y) : (c1 ? a1.y : a0.y);
          y.z = c2 ? (c1 ? a3.z : a2.z) : (c1 ? a1.z : a0.z); y.w = c2 ? (c1 ? a3.w : a2.w) : (c1 ? a1.w : a0.w);
          return enc4(y.x, y.y, y.z, y.w, stag);
        };
        st_sc1(wsr, zo + (5 * t + (cr >> 1)) * 4 * X4_BLK, pick(z0, z1, z2, z3));
        st_sc1(wsr, zo + (5 * t + 2 + (cr >> 1)) * 4 * X4_BLK, pick(z4, z5, z6, z7));
        { const v4u y = pick(z8, z9, z8, z9); if (cr < 2) st_sc1(wsr, zo + (5 * t + 4) * 4 * X4_BLK, y); }
        STAMP(6);
      }
    }
#endif
    FLUSH_STAMPS();
  }
}

extern "C" void kernel_launch(void* const* d_in, const int* in_sizes, int n_in, void* d_out, int out_size, void* d_ws, size_t ws_size, hipStream_t stream) {
  Params p;
  p.x = (const u16*)d_in[0]; p.K = (const u16*)d_in[1]; p.V = (const u16*)d_in[2]; p.wq = (const u16*)d_in[3]; p.wo = (const u16*)d_in[4];
  p.w1 = (const u16*)d_in[6]; p.w3 = (const u16*)d_in[7]; p.w2 = (const u16*)d_in[8];
  p.out = (u16*)d_out; p.ws = (unsigned char*)d_ws;
  hipLaunchKernelGGL(fused_block, dim3(256), dim3(NTHR), 0, stream, p);
}
```

```cpp
#include <hip/hip_runtime.h>
#include <stdint.h>

typedef unsigned short u16;
typedef unsigned u32;
typedef unsigned long long u64;
typedef u32 v4u __attribute__((ext_vector_type(4)));
typedef u32 v2u __attribute__((ext_vector_type(2)));
typedef float f4 __attribute__((ext_vector_type(4)));
typedef float f2 __attribute__((ext_vector_type(2)));
typedef short s4 __attribute__((ext_vector_type(4)));
typedef __bf16 bf2 __attribute__((ext_vector_type(2)));
typedef __bf16 bf8 __attribute__((ext_vector_type(8)));
#define GAS __attribute__((address_space(1)))
#define LAS __attribute__((address_space(3)))
#define RLX_AGENT __ATOMIC_RELAXED, __HIP_MEMORY_SCOPE_AGENT
#define RLX_WG __ATOMIC_RELAXED, __HIP_MEMORY_SCOPE_WORKGROUP

#define RD 24
#define W0 3
#define W1 4
#define WTRIG 22
#define RTRIG 16
#define DYNROLE 1
#define X4DLY 40
#define X4NT 0
#define OSLOPE 0
#define FDLY 0
#define WT W1
#define MAP 0
#define X3REP 8
#define X4GAP 0
#define X4NT2 2
#define QPS 4
#define QSLEEP 16
#define PST 0
#define CODEPF 1
#define CODE_BYTES 18944u
#define TAILNT 0
#define W13AUX 2
#define SONLY 0
#define STAMPS 0

namespace {
constexpr int DM = 640, NH = 32, DH = 128, SK = 8192, WIN = 2048, FF = 1728;
constexpr int NTHR = 768;
constexpr int AUX_NT = 2, AUX_SC1 = 16, RSRC_W3 = 0x00020000;
constexpr int OFF_CNT = 0x800;
constexpr int OFF_XL = 0x1000;
constexpr int OFF_XQ = 0x10000;
constexpr int OFF_X1 = 0x90000;
constexpr int OFF_X2 = 0xB0000;
constexpr int X2_BLK = 33 * 128;
constexpr int OFF_X3 = 0x108000;
constexpr int X3_REP = 10240 + 128;
constexpr int OFF_X4 = 0x120000;
constexpr int NFU = 54;
constexpr int X4_BLK = 55 * 128;
constexpr int WS_USED = 0x1B0000;
constexpr size_t OFF_STAMP = 8u << 20;
constexpr u32 SPIN_CAP = 1u << 16;
constexpr float LOG2E = 1.4426950408889634f;

struct Params {
  const u16 *x, *K, *V, *wq, *wo, *w1, *w3, *w2;
  u16* out;
  unsigned char* ws;
};

__device__ __forceinline__ float rb(float f) { return (float)(__bf16)f; }
__device__ __forceinline__ u32 pack2(float lo, float hi) { bf2 v = {(__bf16)lo, (__bf16)hi}; return __builtin_bit_cast(u32, v); }
__device__ __forceinline__ float blo(u32 v) { return __uint_as_float(v << 16); }
__device__ __forceinline__ float bhi(u32 v) { return __uint_as_float(v & 0xffff0000u); }
__device__ __forceinline__ float dot2(u32 a, u32 b, float c) {
  return __builtin_amdgcn_fdot2_f32_bf16(__builtin_bit_cast(bf2, a), __builtin_bit_cast(bf2, b), c, false);
}
__device__ __forceinline__ f4 mfma(v4u a, v4u b, f4 c) {
  return __builtin_amdgcn_mfma_f32_16x16x32_bf16(__builtin_bit_cast(bf8, a), __builtin_bit_cast(bf8, b), c, 0, 0, 0);
}
template <int CTRL> __device__ __forceinline__ u32 dppu(u32 x) { return (u32)__builtin_amdgcn_mov_dpp((int)x, CTRL, 0xf, 0xf, true); }
template <int CTRL> __device__ __forceinline__ float dppf(float x) {
  return __builtin_bit_cast(float, __builtin_amdgcn_mov_dpp(__builtin_bit_cast(int, x), CTRL, 0xf, 0xf, true));
}
constexpr int XOR1 = 0xB1, XOR2 = 0x4E, XOR7 = 0x141, XOR8 = 0x128, XOR15 = 0x140, ROR4 = 0x124;
constexpr int QB1 = 0x55, QB2 = 0xAA, QB3 = 0xFF;
__device__ __forceinline__ float xrow_sum(float x) {
  auto s = __builtin_amdgcn_permlane16_swap(__float_as_uint(x), __float_as_uint(x), false, false);
  x = __uint_as_float(s[0]) + __uint_as_float(s[1]);
  auto t = __builtin_amdgcn_permlane32_swap(__float_as_uint(x), __float_as_uint(x), false, false);
  return __uint_as_float(t[0]) + __uint_as_float(t[1]);
}
__device__ __forceinline__ float xhalf_sum(float x) {
  auto t = __builtin_amdgcn_permlane32_swap(__float_as_uint(x), __float_as_uint(x), false, false);
  return __uint_as_float(t[0]) + __uint_as_float(t[1]);
}
__device__ __forceinline__ float xrow_max(float x) {
  auto s = __builtin_amdgcn_permlane16_swap(__float_as_uint(x), __float_as_uint(x), false, false);
  x = fmaxf(__uint_as_float(s[0]), __uint_as_float(s[1]));
  auto t = __builtin_amdgcn_permlane32_swap(__float_as_uint(x), __float_as_uint(x), false, false);
  return fmaxf(__uint_as_float(t[0]), __uint_as_float(t[1]));
}
__device__ __forceinline__ float wave_sum(float x) {
  x += dppf<XOR1>(x); x += dppf<XOR2>(x); x += dppf<XOR7>(x); x += dppf<XOR15>(x);
  return xrow_sum(x);
}
__device__ __forceinline__ float wave_max(float x) {
  x = fmaxf(x, dppf<XOR1>(x)); x = fmaxf(x, dppf<XOR2>(x)); x = fmaxf(x, dppf<XOR7>(x)); x = fmaxf(x, dppf<XOR15>(x));
  return xrow_max(x);
}
__device__ __forceinline__ float sum8p(float x) { x += dppf<XOR8>(x); return xrow_sum(x); }
__device__ __forceinline__ v4u ld_nt(__amdgpu_buffer_rsrc_t r, int voff, int soff) { return __builtin_amdgcn_raw_buffer_load_b128(r, voff, soff, AUX_NT); }
__device__ __forceinline__ v4u ld_poll_nt(__amdgpu_buffer_rsrc_t r, int voff) { return __builtin_amdgcn_raw_buffer_load_b128(r, voff, 0, AUX_SC1 | AUX_NT); }
__device__ __forceinline__ v4u ld_poll(__amdgpu_buffer_rsrc_t r, int voff) { return __builtin_amdgcn_raw_buffer_load_b128(r, voff, 0, AUX_SC1 | TAILNT); }
__device__ __forceinline__ void st_sc1(__amdgpu_buffer_rsrc_t r, int voff, v4u v) { __builtin_amdgcn_raw_buffer_store_b128(v, r, voff, 0, AUX_SC1); }
template <int AUX = AUX_NT> __device__ __forceinline__ void dma_nt(__amdgpu_buffer_rsrc_t r, LAS unsigned char* dst, int voff, int soff) {
  __builtin_amdgcn_raw_ptr_buffer_load_lds(r, (LAS void*)dst, 16, voff, soff, 0, AUX);
}
__device__ __forceinline__ void dma_asm(__amdgpu_buffer_rsrc_t r, int lds_off, int voff, int soff) {
  u32 keep;
  asm volatile("s_mov_b32 %0, m0\n\ts_mov_b32 m0, %1\n\ts_nop 0\n\tbuffer_load_dwordx4 %2, %3, %4 offen nt lds\n\ts_mov_b32 m0, %0"
               : "=&s"(keep) : "s"(lds_off), "v"(voff), "s"(r), "s"(soff) : "memory");
}
__device__ __forceinline__ __amdgpu_buffer_rsrc_t mkrsrc(const void* p, int bytes) { return __builtin_amdgcn_make_buffer_rsrc((void*)p, 0, bytes, RSRC_W3); }
__device__ __forceinline__ u32 enc1(float a, u32 tag) { return ((__float_as_uint(a) + 0x200u) & ~0x3ffu) | tag; }
__device__ __forceinline__ v4u enc4(float a, float b, float c, float d, u32 tag) { return v4u{enc1(a, tag), enc1(b, tag), enc1(c, tag), enc1(d, tag)}; }
__device__ __forceinline__ bool ok4(v4u g, u32 tag) { return ((((g.x ^ tag) | (g.y ^ tag)) | ((g.z ^ tag) | (g.w ^ tag))) & 0x3ffu) == 0u; }
__device__ __forceinline__ u32 bad4(v4u g, u32 tag) { return ((g.x ^ tag) | (g.y ^ tag)) | ((g.z ^ tag) | (g.w ^ tag)); }
__device__ __forceinline__ float dec(u32 w) { return __uint_as_float(w & ~0x3ffu); }
__device__ __forceinline__ f4 dec4(v4u g) { return f4{dec(g.x), dec(g.y), dec(g.z), dec(g.w)}; }

__device__ __forceinline__ void ln_finish(float pivot, float s1, float s2, float& mb, float& ri) {
  const float m1 = wave_sum(s1) * (1.0f / DM), m2 = wave_sum(s2) * (1.0f / DM);
  const float var = fmaxf(m2 - m1 * m1, 0.f);
  mb = rb(pivot + m1);
  ri = __builtin_amdgcn_rcpf(rb(__builtin_amdgcn_sqrtf(rb(rb(var) + 1e-5f))));
}
__device__ __forceinline__ float first_lane(float x) { return __uint_as_float(__builtin_amdgcn_readfirstlane(__float_as_uint(x))); }
__device__ __forceinline__ u32 mksel(int j, int k, bool act) {
  const u32 s = (j & 1) ? 0x01000c0cu : 0x0c0c0100u;
  return (act && (j >> 1) == k) ? s : 0x0c0c0c0cu;
}
__device__ __forceinline__ v4u trfrag(LAS unsigned char* a0, int step4) {
  const v2u lo = __builtin_bit_cast(v2u, __builtin_amdgcn_ds_read_tr16_b64_v4i16((LAS s4*)a0));
  const v2u hi = __builtin_bit_cast(v2u, __builtin_amdgcn_ds_read_tr16_b64_v4i16((LAS s4*)(a0 + step4)));
  return v4u{lo.x, lo.y, hi.x, hi.y};
}
__device__ __forceinline__ void lds_arrive(LAS u32* cnt, int lane) {
  asm volatile("s_waitcnt lgkmcnt(0)" ::: "memory");
  if (lane == 0) __hip_atomic_fetch_add(cnt, 1u, RLX_WG);
}
template <int SL> __device__ __forceinline__ void lds_wait(LAS u32* cnt, u32 n) {
#pragma nounroll
  for (u32 spin = 0; spin < (1u << 22); ++spin) { if (__hip_atomic_load(cnt, RLX_WG) >= n) break; __builtin_amdgcn_s_sleep(SL); }
  asm volatile("" ::: "memory");
}
#define STAMP(n) do {} while (0)
#define PCNT(n, v) do {} while (0)
#define FLUSH_STAMPS() do {} while (0)
#define SB() __builtin_amdgcn_sched_barrier(0)
#define MEMCLOB() asm volatile("" ::: "memory")
template <int N> __device__ __forceinline__ void wait_vm() { asm volatile("s_waitcnt vmcnt(%0)" ::"n"(N) : "memory"); }

constexpr int window(int n) { return (n >= 32 - 2 * (WT - W1) && n < 32) ? W1 + (n - (32 - 2 * (WT - W1)) + 2) / 2 : W1; }
constexpr int issued(int n) {
  int v = RD;
  for (int k = 0; k <= n; ++k) { int w = k + 1 + window(k); if (w > k + 1 + RD - 1) w = k + RD; if (w > 64) w = 64; if (w > v) v = w; }
  return v;
}
__device__ __forceinline__ int ffn_unit(int rk, int grp) { return rk < 6 ? 2 * (grp + 8 * (rk >> 1)) + (rk & 1) : rk == 6 ? 48 + grp : NFU; }
constexpr int L_FLAG = 0;
constexpr int L_STAT = 64;
constexpr int L_INFO = 96;
constexpr int L_FAC = 192;
constexpr int L_M = 128;
constexpr int L_Q = 256;
constexpr int L_HH = 512;
constexpr int L_O = 1024;
constexpr int O_RS = 320;
constexpr int L_LN2 = 3328;
constexpr int LN2_RS = 1344;
constexpr int L_RED = 8704;
constexpr int L_W = L_RED + 8192;
constexpr int L_W2 = L_W + 2 * 640 * 64;
constexpr int L_BYTES = L_W2 + 32 * 1280;
}

extern "C" __global__ void __launch_bounds__(NTHR, 3) fused_block(Params p) {
  __shared__ __attribute__((aligned(16))) unsigned char smem[L_BYTES];
  LAS unsigned char* L = (LAS unsigned char*)smem;
  const int tid = threadIdx.x, lane = tid & 63, wid = __builtin_amdgcn_readfirstlane(tid >> 6);
  const int bid = blockIdx.x;
  const int r = lane >> 4, c = lane & 15;
  asm volatile("" :: "s"(p.x), "s"(p.K), "s"(p.V), "s"(p.wq), "s"(p.wo), "s"(p.w1), "s"(p.w3), "s"(p.w2), "s"(p.out), "s"(p.ws));
  STAMP(0);
  const int h = (bid & 7) + 8 * (bid >> 6), bs = (bid >> 3) & 7, b = bs >> 1, s = bs & 1;
  LAS u32* flags = (LAS u32*)(L + L_FLAG);
  LAS float* mbuf = (LAS float*)(L + L_M);
  LAS float* lbuf = mbuf + 8;
  const __amdgpu_buffer_rsrc_t wsr = mkrsrc(p.ws, WS_USED);
  const int t = wid & 3;
  const int bsel = (lane >> 3) & 1;

  const size_t kvoff = ((size_t)(b * NH + h) * SK + (SK - WIN) + s * 1024) * DH;
  const __amdgpu_buffer_rsrc_t kr = mkrsrc(p.K + kvoff, 1024 * DH * 2), vr = mkrsrc(p.V + kvoff, 1024 * DH * 2);
  const int kvo = wid * 1024 + lane * 16;
  v4u ring[RD];
  u32 xr[5];
  v4u wqv[5];
  u64 epoch = 0;
  if (wid < 8) {
#pragma unroll
    for (int n = 0; n < W0; ++n) ring[n] = ld_nt(kr, kvo, n * 8192);
  } else {
    const int row0 = 80 * bs + 20 * t + r;
#pragma unroll
    for (int k = 0; k < 5; ++k) xr[k] = ((const GAS u32*)p.x)[t * 320 + lane + 64 * k];
    const __amdgpu_buffer_rsrc_t qr = mkrsrc(p.wq + h * DH, DM * NH * DH * 2 - h * DH * 2);
#pragma unroll
    for (int i = 0; i < 5; ++i) wqv[i] = ld_nt(qr, row0 * (NH * DH * 2) + c * 16, i * 4 * (NH * DH * 2));
    epoch = __hip_atomic_load((GAS u64*)p.ws, RLX_AGENT);
    if (wid == 8 && lane < 16) flags[lane] = 0u;
  }
  STAMP(9);
  SB();
  __syncthreads();

  if (__builtin_expect(wid >= 8, 1)) {
  u32 pfv = 0;
  {
    u64 pc; u32 off;
    asm volatile("s_getpc_b64 %0\n.Lpf_here%=:\n\ts_mov_b32 %1, .Lpf_here%=-fused_block" : "=s"(pc), "=s"(off));
    if (bid < 8) {
      const u32 o = (u32)t * 8192u + lane * 128u;
      if (o < CODE_BYTES) pfv = *(const GAS u32*)(pc - off + o);
    }
  }
  u32 tag, t16;
  {
    LAS u32* xw = (LAS u32*)(L + L_LN2 + t * LN2_RS);
#pragma unroll
    for (int k = 0; k < 5; ++k) xw[lane + 64 * k] = xr[k];
    const float pivot = first_lane(blo(xr[0]));
    float s1 = 0.f, s2 = 0.f;
#pragma unroll
    for (int k = 0; k < 5; ++k) { const float d0 = blo(xr[k]) - pivot, d1 = bhi(xr[k]) - pivot; s1 += d0 + d1; s2 = fmaf(d0, d0, fmaf(d1, d1, s2)); }
    float mb, ri;
    ln_finish(pivot, s1, s2, mb, ri);
    if (lane == 0) *(LAS f2*)(L + L_STAT + t * 8) = f2{mb, ri};
    lds_arrive(flags + 0, lane);
    u32 sel[4];
#pragma unroll
    for (int k = 0; k < 4; ++k) sel[k] = mksel(lane & 7, k, true);
    lds_wait<0>(flags + 0, 4);
    f2 st[2];
    st[0] = *(LAS f2*)(L + L_STAT + bsel * 8); st[1] = *(LAS f2*)(L + L_STAT + (2 + bsel) * 8);
    u32 xe[5][2];
#pragma unroll
    for (int i = 0; i < 5; ++i)
#pragma unroll
      for (int P = 0; P < 2; ++P) xe[i][P] = *(LAS u16*)(L + L_LN2 + (2 * P + bsel) * LN2_RS + (80 * bs + 20 * t + r + 4 * i) * 2);
    f4 acc[2] = {{0.f, 0.f, 0.f, 0.f}, {0.f, 0.f, 0.f, 0.f}};
#pragma unroll
    for (int i = 0; i < 5; ++i)
#pragma unroll
      for (int P = 0; P < 2; ++P) {
        const u32 lv = pack2(rb(blo(xe[i][P]) - st[P].x) * st[P].y, 0.f);
        v4u a;
#pragma unroll
        for (int k = 0; k < 4; ++k) a[k] = __builtin_amdgcn_perm(lv, lv, sel[k]);
        acc[P] = mfma(a, wqv[i], acc[P]);
      }
#pragma unroll
    for (int P = 0; P < 2; ++P) *(LAS f4*)(L + L_RED + t * 2048 + P * 1024 + lane * 16) = acc[P];
    lds_arrive(flags + 1, lane);
    tag = __builtin_amdgcn_readfirstlane(((u32)epoch + 1u) & 0x3ffu);
    t16 = __builtin_amdgcn_readfirstlane(((u32)epoch + 1u) & 0xffffu);
    lds_wait<0>(flags + 1, 4);
    if (lane < 32) {
      f4 sum = {0.f, 0.f, 0.f, 0.f};
#pragma unroll
      for (int ww = 0; ww < 4; ++ww) sum += *(LAS f4*)(L + L_RED + ww * 2048 + (t >> 1) * 1024 + (16 * (2 * (t & 1) + (lane & 1)) + (lane >> 1)) * 16);
      st_sc1(wsr, OFF_XQ + ((h * 4 + t) * 8 + bs) * 512 + lane * 16, enc4(sum.x, sum.y, sum.z, sum.w, tag));
    }
  }
  STAMP(8);
  if (bid < 8 && t == 1 && lane == 0) __hip_atomic_store((GAS u32*)(p.ws + OFF_CNT + bid * 128), t16 << 16, RLX_AGENT);
  if (t == 0) {
    if (QSLEEP) __builtin_amdgcn_s_sleep(QSLEEP);
    v4u g[4];
    const int go = OFF_XQ + (h * 4 + b) * 4096 + lane * 16;
#pragma nounroll
    for (u32 spin = 0;; ++spin) {
      MEMCLOB();
      u32 bad = 0;
#pragma unroll
      for (int k = 0; k < 4; ++k) g[k] = ld_poll_nt(wsr, go + k * 1024);
#pragma unroll
      for (int k = 0; k < 4; ++k) bad |= bad4(g[k], tag);
      if (__all((bad & 0x3ffu) == 0u) || spin > SPIN_CAP) break;
      __builtin_amdgcn_s_sleep(QPS);
    }
    const f4 sum = (dec4(g[0]) + dec4(g[1])) + (dec4(g[2]) + dec4(g[3]));
    const float q0 = xhalf_sum(sum.x), q1 = xhalf_sum(sum.y), q2 = xhalf_sum(sum.z), q3 = xhalf_sum(sum.w);
    if (lane < 32) *(LAS v2u*)(L + L_Q + lane * 8) = v2u{pack2(q0, q1), pack2(q2, q3)};
    lds_arrive(flags + 3, lane);
    STAMP(1);
    lds_wait<16>(flags + 5, 8);
    const float mw = mbuf[lane & 7], lw = lbuf[lane & 7];
    float M = fmaxf(mw, dppf<XOR1>(mw)); M = fmaxf(M, dppf<XOR2>(M)); M = fmaxf(M, dppf<XOR7>(M));
    const float ew = __builtin_amdgcn_exp2f((mw - M) * LOG2E);
    float Ls = lw * ew; Ls += dppf<XOR1>(Ls); Ls += dppf<XOR2>(Ls); Ls += dppf<XOR7>(Ls);
    Ls = first_lane(Ls);
    const v4u mine = enc4(M, Ls, M, Ls, tag);
    if (lane == 0) st_sc1(wsr, OFF_XL + (h * 8 + bs) * 128, mine);
    v4u gl;
#pragma nounroll
    for (u32 spin = 0;; ++spin) {
      MEMCLOB();
      gl = ld_poll_nt(wsr, OFF_XL + (h * 8 + (bs ^ 1)) * 128);
      if (__all(ok4(gl, tag)) || spin > SPIN_CAP) break;
      __builtin_amdgcn_s_sleep(16);
    }
    const float M0 = dec(mine.x), L0 = dec(mine.y), M1 = dec(gl.x), L1 = dec(gl.y), Mx = fmaxf(M0, M1);
    const float e0 = L0 * __builtin_amdgcn_exp2f((M0 - Mx) * LOG2E), e1 = L1 * __builtin_amdgcn_exp2f((M1 - Mx) * LOG2E);
    if (lane < 8) *(LAS float*)(L + L_FAC + lane * 4) = __builtin_amdgcn_exp2f((mw - Mx) * LOG2E) * __builtin_amdgcn_rcpf(e0 + e1);
    if (lane == 0) *(LAS u32*)(L + L_INFO) = tag;
    lds_arrive(flags + 8, lane);
    STAMP(2);
  }
  if (t == 1) {
#pragma nounroll
    for (u32 spin = 0; spin <= SPIN_CAP; ++spin) {
      MEMCLOB();
      const u32 v = __builtin_amdgcn_raw_buffer_load_b32(wsr, OFF_CNT + (bid & 7) * 128, 0, AUX_SC1 | AUX_NT);
      if (__builtin_amdgcn_readfirstlane(v >> 16) == t16) break;
      __builtin_amdgcn_s_sleep(16);
    }
    lds_wait<8>(flags + 9, 8);
    u32 old = 0;
    if (lane == 0) old = __hip_atomic_fetch_add((GAS u32*)(p.ws + OFF_CNT + (bid & 7) * 128), 1u, RLX_AGENT);
    if (lane == 0) *(LAS u32*)(L + L_INFO + 8) = old & 31u;
    lds_arrive(flags + 10, lane);
  }
  asm volatile("" :: "v"(pfv));
  lds_wait<8>(flags + 10, 1);
  const int rk = DYNROLE ? __builtin_amdgcn_readfirstlane(*(LAS u32*)(L + L_INFO + 8)) : (bid >> 3), grp = bid & 7;
  const int u = ffn_unit(rk, grp);
  const int role = rk >= 12 ? 0 : (rk < 7 && u < NFU) ? 1 : 3;
  const int task = grp * 10 + rk - 12, cs = task >> 2, sb = task & 3;
  const bool owner = rk >= 12 && rk < 22 && t == 0;
  const int uo = grp * 20 + rk - 12, hh_o = uo / 5, j_o = uo - 5 * hh_o;
  const bool ffn = role == 1;
  const int q4 = (lane & 15) >> 2, p4 = lane & 3;
  v4u bw[20];
  const int sw2 = 2 * (q4 + 4 * (r & 1));
  lds_wait<8>(flags + 7, 8);
  if (role == 0 || ffn) {
    { const int nsl = ffn ? FDLY : ((31 - rk) * OSLOPE) >> 3;
#pragma nounroll
      for (int i = 0; i < nsl; ++i) __builtin_amdgcn_s_sleep(4); }
    STAMP(10);
    if (role == 0) {
      const __amdgpu_buffer_rsrc_t orr = mkrsrc(p.wo + (size_t)hh_o * DH * DM + 128 * j_o, DH * DM * 2 - 256 * j_o);
      const int vo0 = (32 * t + r) * (DM * 2) + (c ^ (2 * r)) * 16, vo1 = (32 * t + r) * (DM * 2) + (c ^ (2 * r + 8)) * 16;
#pragma unroll
      for (int i = 0; i < 8; ++i) dma_nt(orr, L + L_W + (32 * t + 4 * i) * 256, (i & 2) ? vo1 : vo0, i * 4 * DM * 2);
    } else {
      const __amdgpu_buffer_rsrc_t w1r = mkrsrc(p.w1 + 32 * u, DM * FF * 2 - 64 * u), w3r = mkrsrc(p.w3 + 32 * u, DM * FF * 2 - 64 * u);
      const int rw = lane >> 2, c4 = lane & 3, gp = (rw >> 3) & 1;
      const int vo1 = (160 * t + rw) * (FF * 2) + (c4 ^ gp) * 16, vo3 = (160 * t + rw) * (FF * 2) + (c4 ^ (2 + gp)) * 16;
#pragma unroll
      for (int i = 0; i < 10; ++i) dma_nt<W13AUX>(w1r, L + L_W + (160 * t + 16 * i) * 64, vo1, i * 16 * FF * 2);
#pragma unroll
      for (int i = 0; i < 10; ++i) dma_nt<W13AUX>(w3r, L + L_W + 640 * 64 + (160 * t + 16 * i) * 64, vo3, i * 16 * FF * 2);
    }
    if (ffn) {
      wait_vm<0>();
      lds_arrive(flags + 2, lane);
      lds_wait<0>(flags + 2, 4);
      STAMP(11);
#pragma unroll
      for (int ks = 0; ks < 20; ++ks) bw[ks] = trfrag(L + L_W + (p4 >> 1) * (640 * 64) + (32 * ks + 8 * r + q4) * 64 + ((t ^ (2 * (p4 >> 1) + (r & 1))) * 16) + (p4 & 1) * 8, 4 * 64);
      STAMP(12);
    }
  }
  const int cb = c & 3, cr = c >> 2;
  if (role == 0) {
    v4u g;
    const int go = OFF_X1 + (hh_o * 8 + 2 * t) * 512 + lane * 16;
#pragma nounroll
    for (u32 spin = 0;; ++spin) {
      MEMCLOB();
      g = ld_poll(wsr, go);
      if (__all(ok4(g, tag)) || spin > SPIN_CAP) break;
      if (PST) __builtin_amdgcn_s_sleep(PST);
    }
    STAMP(5);
    const f4 og = dec4(g);
    const float o0 = xhalf_sum(og.x), o1 = xhalf_sum(og.y), o2 = xhalf_sum(og.z), o3 = xhalf_sum(og.w);
    if (lane < 32) *(LAS v2u*)(L + L_O + t * O_RS + lane * 8) = v2u{pack2(o0, o1), pack2(o2, o3)};
    wait_vm<0>();
    __syncthreads();
    f4 d0 = {0.f, 0.f, 0.f, 0.f}, d1 = {0.f, 0.f, 0.f, 0.f};
#pragma unroll
    for (int ks = 0; ks < 4; ++ks) {
      const v4u a = *(LAS v4u*)(L + L_O + cb * O_RS + ks * 64 + r * 16);
      d0 = mfma(trfrag(L + L_W + (32 * ks + 8 * r + q4) * 256 + (((4 * t + (p4 >> 1)) ^ sw2) * 16) + (p4 & 1) * 8, 4 * 256), a, d0);
      d1 = mfma(trfrag(L + L_W + (32 * ks + 8 * r + q4) * 256 + (((4 * t + 2 + (p4 >> 1)) ^ sw2) * 16) + (p4 & 1) * 8, 4 * 256), a, d1);
    }
    const f4 y = (cr & 1) ? d1 : d0;
    if (cr < 2) st_sc1(wsr, OFF_X2 + ((4 * j_o + t) * 4 + cb) * X2_BLK + hh_o * 128 + cr * 64 + r * 16, enc4(y.x, y.y, y.z, y.w, tag));
    STAMP(6);
  } else if (ffn) {
    v4u g[3];
    const int go = OFF_X3 + (X3REP > 1 ? grp * X3_REP : 0) + t * 2560 + lane * 16;
    const bool has2 = lane < 32;
#pragma nounroll
    for (u32 spin = 0;; ++spin) {
      MEMCLOB();
      g[0] = ld_poll(wsr, go); g[1] = ld_poll(wsr, go + 1024); g[2] = ld_poll(wsr, go + (has2 ? 2048 : 0));
      if (__all(((bad4(g[0], tag) | bad4(g[1], tag) | bad4(g[2], tag)) & 0x3ffu) == 0u) || spin > SPIN_CAP) break;
      if (PST) __builtin_amdgcn_s_sleep(PST);
    }
    STAMP(5);
    const float pivot = first_lane(dec(g[0].x));
    float s1 = 0.f, s2 = 0.f;
#pragma unroll
    for (int k = 0; k < 3; ++k)
#pragma unroll
      for (int e = 0; e < 4; ++e) { const float d = (k < 2 || has2) ? dec(g[k][e]) - pivot : 0.f; s1 += d; s2 = fmaf(d, d, s2); }
    float mb, ri;
    ln_finish(pivot, s1, s2, mb, ri);
    LAS unsigned char* lp = L + L_LN2 + t * LN2_RS + lane * 8;
#pragma unroll
    for (int k = 0; k < 3; ++k)
      if (k < 2 || has2) *(LAS v2u*)(lp + 512 * k) = v2u{pack2(rb(dec(g[k].x) - mb) * ri, rb(dec(g[k].y) - mb) * ri), pack2(rb(dec(g[k].z) - mb) * ri, rb(dec(g[k].w) - mb) * ri)};
    __syncthreads();
    STAMP(13);
    f4 da = {0.f, 0.f, 0.f, 0.f}, db = {0.f, 0.f, 0.f, 0.f};
#pragma unroll
    for (int ks = 0; ks < 20; ks += 2) {
      da = mfma(bw[ks], *(LAS v4u*)(L + L_LN2 + cb * LN2_RS + ks * 64 + r * 16), da);
      db = mfma(bw[ks + 1], *(LAS v4u*)(L + L_LN2 + cb * LN2_RS + ks * 64 + 64 + r * 16), db);
    }
    const f4 d = da + db;
    const float pre = rb(cr == 0 ? d.x : cr == 1 ? d.y : cr == 2 ? d.z : d.w);
    auto sw = __builtin_amdgcn_permlane32_swap(__float_as_uint(pre), __float_as_uint(pre), false, false);
    const float gs = __uint_as_float(sw[0]), us = __uint_as_float(sw[1]);
    const float si = rb(gs * __builtin_amdgcn_rcpf(1.0f + __builtin_amdgcn_exp2f(-gs * LOG2E)));
    if (lane < 32) *(LAS u16*)(L + L_HH + cb * 64 + (8 * t + 4 * r + cr) * 2) = (u16)(pack2(si * us, 0.f) & 0xffffu);
    STAMP(14);
    __syncthreads();
    STAMP(6);
  }
  if (owner) {
    const int pp = lane >> 3, q = lane & 7;
    const v2u xv = *(LAS v2u*)(L + L_LN2 + sb * LN2_RS + (32 * cs + 4 * q) * 2);
    f4 hv;
    {
      v4u g[4];
      const int go = OFF_X2 + (cs * 4 + sb) * X2_BLK + pp * 128 + q * 16;
  #pragma nounroll
    for (u32 spin = 0;; ++spin) {
        MEMCLOB();
#pragma unroll
        for (int m = 0; m < 4; ++m) g[m] = ld_poll(wsr, go + m * 1024);
        if (__all((((bad4(g[0], tag) | bad4(g[1], tag)) | (bad4(g[2], tag) | bad4(g[3], tag))) & 0x3ffu) == 0u) || spin > SPIN_CAP) break;
        if (PST) __builtin_amdgcn_s_sleep(PST);
      }
      STAMP(3);
      const f4 y = (dec4(g[0]) + dec4(g[1])) + (dec4(g[2]) + dec4(g[3]));
      hv.x = rb(blo(xv.x) + rb(sum8p(y.x))); hv.y = rb(bhi(xv.x) + rb(sum8p(y.y)));
      hv.z = rb(blo(xv.y) + rb(sum8p(y.z))); hv.w = rb(bhi(xv.y) + rb(sum8p(y.w)));
    }
    if (pp < X3REP) st_sc1(wsr, OFF_X3 + pp * X3_REP + sb * 2560 + cs * 128 + q * 16, enc4(hv.x, hv.y, hv.z, hv.w, tag));
    STAMP(4);
    {
      if (X4DLY) __builtin_amdgcn_s_sleep(X4DLY);
      const int go = OFF_X4 + (cs * 4 + sb) * X4_BLK + pp * 128 + q * 16;
      v4u g[7];
      const bool last = pp < NFU - 48;
#pragma nounroll
      for (u32 spin = 0;; ++spin) {
        MEMCLOB();
        g[0] = __builtin_amdgcn_raw_buffer_load_b128(wsr, go, 0, AUX_SC1 | X4NT);
        if (__all((bad4(g[0], tag) & 0x3ffu) == 0u) || spin > SPIN_CAP) break;
      }
      if (X4GAP) __builtin_amdgcn_s_sleep(X4GAP);
#pragma nounroll
      for (u32 spin = 0;; ++spin) {
        MEMCLOB();
#pragma unroll
        for (int m = 1; m < 6; ++m) g[m] = __builtin_amdgcn_raw_buffer_load_b128(wsr, go + m * 1024, 0, AUX_SC1 | X4NT2);
        g[6] = __builtin_amdgcn_raw_buffer_load_b128(wsr, go + (last ? 6144 : 1024), 0, AUX_SC1 | X4NT2);
        u32 bad = 0;
#pragma unroll
        for (int m = 1; m < 7; ++m) bad |= bad4(g[m], tag);
        if (__all((bad & 0x3ffu) == 0u) || spin > SPIN_CAP) break;
      }
      STAMP(13);
      f4 z = ((dec4(g[0]) + dec4(g[1])) + (dec4(g[2]) + dec4(g[3]))) + (dec4(g[4]) + dec4(g[5]));
      if (last) z += dec4(g[6]);
      const float o0 = hv.x + rb(sum8p(z.x)), o1 = hv.y + rb(sum8p(z.y)), o2 = hv.z + rb(sum8p(z.z)), o3 = hv.w + rb(sum8p(z.w));
      if (lane < 8) ((GAS v2u*)p.out)[sb * 160 + 8 * cs + q] = v2u{pack2(o0, o1), pack2(o2, o3)};
    }
    if (task == 0 && lane == 0) __hip_atomic_store((GAS u64*)p.ws, epoch + 1, RLX_AGENT);
    STAMP(14);
  }
  STAMP(7);
  FLUSH_STAMPS();
    return;
  }
  {
    const int w = wid;
#pragma unroll
    for (int n = W0; n < RD; ++n) { wait_vm<W0 - 1>(); ring[n] = ld_nt(kr, kvo, n * 8192); SB(); }
    u32 selA[4];
#pragma unroll
    for (int k = 0; k < 4; ++k) selA[k] = mksel(c & 7, k, c < 8);
    STAMP(8);
#if !(SONLY & 1)
    lds_wait<1>(flags + 3, 1);
#endif
    const v4u qv = *(LAS v4u*)(L + L_Q + c * 16);
    STAMP(1);
#define REFILL(n) { _Pragma("unroll") for (int m_ = issued((n) - 1); m_ < issued(n); ++m_) { if (m_ < 32) ring[m_ % RD] = ld_nt(kr, kvo, m_ * 8192); else ring[m_ % RD] = ld_nt(vr, kvo, (m_ - 32) * 8192); } SB(); }
    float sc[2];
    float x[16];
#define QK_STEP(n) { const v4u kk = ring[(n) % RD]; x[(n) & 15] = dot2(kk.w, qv.w, dot2(kk.z, qv.z, dot2(kk.y, qv.y, dot2(kk.x, qv.x, 0.f)))); REFILL(n) }
#define QK4(n) QK_STEP(n) QK_STEP((n) + 1) QK_STEP((n) + 2) QK_STEP((n) + 3)
#define TR_STEP(HALF, CTRL, BIT) _Pragma("unroll") for (int hh = 0; hh < (HALF); ++hh) { \
      const float lo_ = x[hh], hi_ = x[hh + (HALF)]; const float t1_ = lo_ + dppf<CTRL>(lo_), t2_ = hi_ + dppf<CTRL>(hi_); x[hh] = (c & (BIT)) ? t2_ : t1_; }
#define TR_ALL() TR_STEP(8, XOR8, 8) TR_STEP(4, XOR7, 4) TR_STEP(2, XOR2, 2) TR_STEP(1, XOR1, 1)
    QK4(0) QK4(4) QK4(8) QK4(12)
    TR_ALL()
    sc[0] = rb(rb(x[0]) * 0.08838834764831845f);
    QK4(16) QK4(20) QK4(24) QK4(28)
    TR_ALL()
    sc[1] = rb(rb(x[0]) * 0.08838834764831845f);
    STAMP(2);
    const float mw = wave_max(fmaxf(sc[0], sc[1]));
    const float pe0 = __builtin_amdgcn_exp2f((sc[0] - mw) * LOG2E), pe1 = __builtin_amdgcn_exp2f((sc[1] - mw) * LOG2E);
    { const float lw = wave_sum(pe0 + pe1); if (lane == 0) { mbuf[w] = mw; lbuf[w] = lw; } }
    lds_arrive(flags + 5, lane);
    u32 pb[2];
    pb[0] = pack2(pe0, 0.f); pb[1] = pack2(pe1, 0.f);
    f4 acc = {0.f, 0.f, 0.f, 0.f};
#define PV_STEP(i) { const v4u vv = ring[(32 + (i)) % RD]; \
      const u32 xx = (u32)__builtin_amdgcn_update_dpp(0, (int)pb[(i) >> 4], 0x150 + ((i) & 15), 0xf, 0xf, false); \
      v4u a; a[0] = __builtin_amdgcn_perm(xx, xx, selA[0]); a[1] = __builtin_amdgcn_perm(xx, xx, selA[1]); a[2] = __builtin_amdgcn_perm(xx, xx, selA[2]); a[3] = __builtin_amdgcn_perm(xx, xx, selA[3]); \
      acc = mfma(a, vv, acc); REFILL(32 + (i)) \
      if ((i) == WTRIG && lane == 0) __hip_atomic_fetch_add(flags + 7, 1u, RLX_WG); \
      if ((i) == RTRIG && lane == 0) __hip_atomic_fetch_add(flags + 9, 1u, RLX_WG); }
#define PV4(i) PV_STEP(i) PV_STEP((i) + 1) PV_STEP((i) + 2) PV_STEP((i) + 3)
    PV4(0) PV4(4) PV4(8) PV4(12) PV4(16) PV4(20) PV4(24)
    MEMCLOB();
    u32 iflag = __hip_atomic_load(flags + 8, RLX_WG);
    float fw = *(volatile LAS float*)(L + L_FAC + w * 4);
    u32 stag = *(volatile LAS u32*)(L + L_INFO);
    PV4(28)
#if !(SONLY & (4 | 8))
    if (__builtin_amdgcn_readfirstlane(iflag) == 0u) { lds_wait<0>(flags + 8, 1); fw = *(volatile LAS float*)(L + L_FAC + w * 4); stag = *(volatile LAS u32*)(L + L_INFO); }
#endif
    acc *= fw;
    if (lane < 32) *(LAS f4*)(L + L_RED + w * 512 + (8 * c + 4 * r) * 4) = acc;
    asm volatile("s_waitcnt lgkmcnt(0)" ::: "memory");
    u32 old = 0;
    if (lane == 0) old = __hip_atomic_fetch_add(flags + 6, 1u, RLX_WG);
    old = __builtin_amdgcn_readfirstlane(old);
    STAMP(3);
#if !(SONLY & (4 | 8))
    if (old == 7) {
      MEMCLOB();
      if (lane < 32) {
        f4 sum = {0.f, 0.f, 0.f, 0.f};
#pragma unroll
        for (int ww = 0; ww < 8; ++ww) sum += *(LAS f4*)(L + L_RED + ww * 512 + lane * 16);
        st_sc1(wsr, OFF_X1 + (h * 8 + bs) * 512 + lane * 16, enc4(sum.x, sum.y, sum.z, sum.w, stag));
      }
      STAMP(4);
    }
#endif
#if !(SONLY & (4 | 8))
    if (w < 4) {
      lds_wait<8>(flags + 10, 1);
      const int rk = DYNROLE ? __builtin_amdgcn_readfirstlane(*(LAS u32*)(L + L_INFO + 8)) : (bid >> 3);
      const int u = ffn_unit(rk, bid & 7);
      if (u < NFU) {
        const int t = w, q4 = c >> 2, p4 = c & 3, cb = c & 3, cr = c >> 2;
        const __amdgpu_buffer_rsrc_t w2r = mkrsrc(p.w2 + (size_t)32 * u * DM, 32 * DM * 2);
        const int vo2 = r * (DM * 2) + (c ^ (2 * (r + 4 * (t & 1)))) * 16;
#pragma unroll
        for (int i = 0; i < 10; ++i) dma_nt(w2r, L + L_W2 + (10 * t + i) * 1024, vo2, t * (8 * DM * 2) + (i / 5) * 4 * DM * 2 + (i % 5) * 256);
        wait_vm<0>();
        lds_arrive(flags + 11, lane);
        lds_wait<1>(flags + 11, 4);
        const int sw2 = 2 * (q4 + 4 * (r & 1));
        v4u bd[10];
#pragma unroll
        for (int T = 0; T < 10; ++T) { const int col = 160 * t + 16 * T + 4 * p4; bd[T] = trfrag(L + L_W2 + ((2 * r) * 5 + (col >> 7)) * 1024 + q4 * 256 + ((((col & 127) >> 3) ^ sw2) * 16) + (p4 & 1) * 8, 5 * 1024); }
        asm volatile("" :: "v"(bd[0]), "v"(bd[1]), "v"(bd[2]), "v"(bd[3]), "v"(bd[4]), "v"(bd[5]), "v"(bd[6]), "v"(bd[7]), "v"(bd[8]), "v"(bd[9]));
        STAMP(10);
        __syncthreads();
        __syncthreads();
        STAMP(11);
        const v4u a = *(LAS v4u*)(L + L_HH + cb * 64 + r * 16);
        const int zo = OFF_X4 + cb * X4_BLK + u * 128 + (cr & 1) * 64 + r * 16;
        const f4 zero = {0.f, 0.f, 0.f, 0.f};
        v4u am[4];
#pragma unroll
        for (int k = 0; k < 4; ++k) am[k] = cr == k ? a : v4u{0u, 0u, 0u, 0u};
        f4 z0 = mfma(bd[0], am[0], zero), z1 = mfma(bd[4], am[0], zero), z2 = mfma(bd[8], am[0], zero);
        z0 = mfma(bd[1], am[1], z0); z1 = mfma(bd[5], am[1], z1); z2 = mfma(bd[9], am[1], z2);
        z0 = mfma(bd[2], am[2], z0); z1 = mfma(bd[6], am[2], z1);
        z0 = mfma(bd[3], am[3], z0); z1 = mfma(bd[7], am[3], z1);
        if (cr < 2) st_sc1(wsr, zo + (5 * t + 4) * 4 * X4_BLK, enc4(z2.x, z2.y, z2.z, z2.w, stag));
        st_sc1(wsr, zo + (5 * t + (cr >> 1)) * 4 * X4_BLK, enc4(z0.x, z0.y, z0.z, z0.w, stag));
        st_sc1(wsr, zo + (5 * t + 2 + (cr >> 1)) * 4 * X4_BLK, enc4(z1.x, z1.y, z1.z, z1.w, stag));
        STAMP(6);
      }
    }
#endif
    FLUSH_STAMPS();
  }
}

extern "C" void kernel_launch(void* const* d_in, const int* in_sizes, int n_in, void* d_out, int out_size, void* d_ws, size_t ws_size, hipStream_t stream) {
  Params p;
  p.x = (const u16*)d_in[0]; p.K = (const u16*)d_in[1]; p.V = (const u16*)d_in[2]; p.wq = (const u16*)d_in[3]; p.wo = (const u16*)d_in[4];
  p.w1 = (const u16*)d_in[6]; p.w3 = (const u16*)d_in[7]; p.w2 = (const u16*)d_in[8];
  p.out = (u16*)d_out; p.ws = (unsigned char*)d_ws;
  hipLaunchKernelGGL(fused_block, dim3(256), dim3(NTHR), 0, stream, p);
}
```

```cpp
#include <hip/hip_runtime.h>
#include <stdint.h>

typedef unsigned short u16;
typedef unsigned u32;
typedef unsigned long long u64;
typedef u32 v4u __attribute__((ext_vector_type(4)));
typedef u32 v2u __attribute__((ext_vector_type(2)));
typedef float f4 __attribute__((ext_vector_type(4)));
typedef float f2 __attribute__((ext_vector_type(2)));
typedef short s4 __attribute__((ext_vector_type(4)));
typedef __bf16 bf2 __attribute__((ext_vector_type(2)));
typedef __bf16 bf8 __attribute__((ext_vector_type(8)));
#define GAS __attribute__((address_space(1)))
#define LAS __attribute__((address_space(3)))
#define RLX_AGENT __ATOMIC_RELAXED, __HIP_MEMORY_SCOPE_AGENT
#define RLX_WG __ATOMIC_RELAXED, __HIP_MEMORY_SCOPE_WORKGROUP

#define RD 24
#define W0 3
#define W1 4
#define WTRIG 22
#define RTRIG 16
#define DYNROLE 1
#define X4DLY 40
#define X4NT 0
#define OSLOPE 0
#define FDLY 0
#define WT W1
#define MAP 0
#define X3REP 8
#define X4GAP 0
#define X4NT2 2
#define QPS 4
#define TIMED 1
#define TSCALE 150
#define FLEAD 120
#define OLEAD 90
#define QSLEEP 16
#define PST 0
#define CODEPF 1
#define CODE_BYTES 19200u
#define TAILNT 0
#define W13AUX 2
#define SONLY 0
#define STAMPS 0

namespace {
constexpr int DM = 640, NH = 32, DH = 128, SK = 8192, WIN = 2048, FF = 1728;
constexpr int NTHR = 768;
constexpr int AUX_NT = 2, AUX_SC1 = 16, RSRC_W3 = 0x00020000;
constexpr int OFF_CNT = 0x800;
constexpr int OFF_XL = 0x1000;
constexpr int OFF_XQ = 0x10000;
constexpr int OFF_X1 = 0x90000;
constexpr int OFF_X2 = 0xB0000;
constexpr int X2_BLK = 33 * 128;
constexpr int OFF_X3 = 0x108000;
constexpr int X3_REP = 10240 + 128;
constexpr int OFF_X4 = 0x120000;
constexpr int NFU = 54;
constexpr int X4_BLK = 55 * 128;
constexpr int WS_USED = 0x1B0000;
constexpr size_t OFF_STAMP = 8u << 20;
constexpr u32 SPIN_CAP = 1u << 16;
constexpr float LOG2E = 1.4426950408889634f;

struct Params {
  const u16 *x, *K, *V, *wq, *wo, *w1, *w3, *w2;
  u16* out;
  unsigned char* ws;
};

__device__ __forceinline__ float rb(float f) { return (float)(__bf16)f; }
__device__ __forceinline__ u32 pack2(float lo, float hi) { bf2 v = {(__bf16)lo, (__bf16)hi}; return __builtin_bit_cast(u32, v); }
__device__ __forceinline__ float blo(u32 v) { return __uint_as_float(v << 16); }
__device__ __forceinline__ float bhi(u32 v) { return __uint_as_float(v & 0xffff0000u); }
__device__ __forceinline__ float dot2(u32 a, u32 b, float c) {
  return __builtin_amdgcn_fdot2_f32_bf16(__builtin_bit_cast(bf2, a), __builtin_bit_cast(bf2, b), c, false);
}
__device__ __forceinline__ f4 mfma(v4u a, v4u b, f4 c) {
  return __builtin_amdgcn_mfma_f32_16x16x32_bf16(__builtin_bit_cast(bf8, a), __builtin_bit_cast(bf8, b), c, 0, 0, 0);
}
template <int CTRL> __device__ __forceinline__ u32 dppu(u32 x) { return (u32)__builtin_amdgcn_mov_dpp((int)x, CTRL, 0xf, 0xf, true); }
template <int CTRL> __device__ __forceinline__ float dppf(float x) {
  return __builtin_bit_cast(float, __builtin_amdgcn_mov_dpp(__builtin_bit_cast(int, x), CTRL, 0xf, 0xf, true));
}
constexpr int XOR1 = 0xB1, XOR2 = 0x4E, XOR7 = 0x141, XOR8 = 0x128, XOR15 = 0x140, ROR4 = 0x124;
constexpr int QB1 = 0x55, QB2 = 0xAA, QB3 = 0xFF;
__device__ __forceinline__ float xrow_sum(float x) {
  auto s = __builtin_amdgcn_permlane16_swap(__float_as_uint(x), __float_as_uint(x), false, false);
  x = __uint_as_float(s[0]) + __uint_as_float(s[1]);
  auto t = __builtin_amdgcn_permlane32_swap(__float_as_uint(x), __float_as_uint(x), false, false);
  return __uint_as_float(t[0]) + __uint_as_float(t[1]);
}
__device__ __forceinline__ float xhalf_sum(float x) {
  auto t = __builtin_amdgcn_permlane32_swap(__float_as_uint(x), __float_as_uint(x), false, false);
  return __uint_as_float(t[0]) + __uint_as_float(t[1]);
}
__device__ __forceinline__ float xrow_max(float x) {
  auto s = __builtin_amdgcn_permlane16_swap(__float_as_uint(x), __float_as_uint(x), false, false);
  x = fmaxf(__uint_as_float(s[0]), __uint_as_float(s[1]));
  auto t = __builtin_amdgcn_permlane32_swap(__float_as_uint(x), __float_as_uint(x), false, false);
  return fmaxf(__uint_as_float(t[0]), __uint_as_float(t[1]));
}
__device__ __forceinline__ float wave_sum(float x) {
  x += dppf<XOR1>(x); x += dppf<XOR2>(x); x += dppf<XOR7>(x); x += dppf<XOR15>(x);
  return xrow_sum(x);
}
__device__ __forceinline__ float wave_max(float x) {
  x = fmaxf(x, dppf<XOR1>(x)); x = fmaxf(x, dppf<XOR2>(x)); x = fmaxf(x, dppf<XOR7>(x)); x = fmaxf(x, dppf<XOR15>(x));
  return xrow_max(x);
}
__device__ __forceinline__ float sum8p(float x) { x += dppf<XOR8>(x); return xrow_sum(x); }
__device__ __forceinline__ v4u ld_nt(__amdgpu_buffer_rsrc_t r, int voff, int soff) { return __builtin_amdgcn_raw_buffer_load_b128(r, voff, soff, AUX_NT); }
__device__ __forceinline__ v4u ld_poll_nt(__amdgpu_buffer_rsrc_t r, int voff) { return __builtin_amdgcn_raw_buffer_load_b128(r, voff, 0, AUX_SC1 | AUX_NT); }
__device__ __forceinline__ v4u ld_poll(__amdgpu_buffer_rsrc_t r, int voff) { return __builtin_amdgcn_raw_buffer_load_b128(r, voff, 0, AUX_SC1 | TAILNT); }
__device__ __forceinline__ void st_sc1(__amdgpu_buffer_rsrc_t r, int voff, v4u v) { __builtin_amdgcn_raw_buffer_store_b128(v, r, voff, 0, AUX_SC1); }
template <int AUX = AUX_NT> __device__ __forceinline__ void dma_nt(__amdgpu_buffer_rsrc_t r, LAS unsigned char* dst, int voff, int soff) {
  __builtin_amdgcn_raw_ptr_buffer_load_lds(r, (LAS void*)dst, 16, voff, soff, 0, AUX);
}
__device__ __forceinline__ void dma_asm(__amdgpu_buffer_rsrc_t r, int lds_off, int voff, int soff) {
  u32 keep;
  asm volatile("s_mov_b32 %0, m0\n\ts_mov_b32 m0, %1\n\ts_nop 0\n\tbuffer_load_dwordx4 %2, %3, %4 offen nt lds\n\ts_mov_b32 m0, %0"
               : "=&s"(keep) : "s"(lds_off), "v"(voff), "s"(r), "s"(soff) : "memory");
}
__device__ __forceinline__ __amdgpu_buffer_rsrc_t mkrsrc(const void* p, int bytes) { return __builtin_amdgcn_make_buffer_rsrc((void*)p, 0, bytes, RSRC_W3); }
__device__ __forceinline__ u32 enc1(float a, u32 tag) { return ((__float_as_uint(a) + 0x200u) & ~0x3ffu) | tag; }
__device__ __forceinline__ v4u enc4(float a, float b, float c, float d, u32 tag) { return v4u{enc1(a, tag), enc1(b, tag), enc1(c, tag), enc1(d, tag)}; }
__device__ __forceinline__ bool ok4(v4u g, u32 tag) { return ((((g.x ^ tag) | (g.y ^ tag)) | ((g.z ^ tag) | (g.w ^ tag))) & 0x3ffu) == 0u; }
__device__ __forceinline__ u32 bad4(v4u g, u32 tag) { return ((g.x ^ tag) | (g.y ^ tag)) | ((g.z ^ tag) | (g.w ^ tag)); }
__device__ __forceinline__ float dec(u32 w) { return __uint_as_float(w & ~0x3ffu); }
__device__ __forceinline__ f4 dec4(v4u g) { return f4{dec(g.x), dec(g.y), dec(g.z), dec(g.w)}; }

__device__ __forceinline__ void ln_finish(float pivot, float s1, float s2, float& mb, float& ri) {
  const float m1 = wave_sum(s1) * (1.0f / DM), m2 = wave_sum(s2) * (1.0f / DM);
  const float var = fmaxf(m2 - m1 * m1, 0.f);
  mb = rb(pivot + m1);
  ri = __builtin_amdgcn_rcpf(rb(__builtin_amdgcn_sqrtf(rb(rb(var) + 1e-5f))));
}
__device__ __forceinline__ float first_lane(float x) { return __uint_as_float(__builtin_amdgcn_readfirstlane(__float_as_uint(x))); }
__device__ __forceinline__ u32 mksel(int j, int k, bool act) {
  const u32 s = (j & 1) ? 0x01000c0cu : 0x0c0c0100u;
  return (act && (j >> 1) == k) ? s : 0x0c0c0c0cu;
}
__device__ __forceinline__ v4u trfrag(LAS unsigned char* a0, int step4) {
  const v2u lo = __builtin_bit_cast(v2u, __builtin_amdgcn_ds_read_tr16_b64_v4i16((LAS s4*)a0));
  const v2u hi = __builtin_bit_cast(v2u, __builtin_amdgcn_ds_read_tr16_b64_v4i16((LAS s4*)(a0 + step4)));
  return v4u{lo.x, lo.y, hi.x, hi.y};
}
__device__ __forceinline__ void lds_arrive(LAS u32* cnt, int lane) {
  asm volatile("s_waitcnt lgkmcnt(0)" ::: "memory");
  if (lane == 0) __hip_atomic_fetch_add(cnt, 1u, RLX_WG);
}
template <int SL> __device__ __forceinline__ void lds_wait(LAS u32* cnt, u32 n) {
#pragma nounroll
  for (u32 spin = 0; spin < (1u << 22); ++spin) { if (__hip_atomic_load(cnt, RLX_WG) >= n) break; __builtin_amdgcn_s_sleep(SL); }
  asm volatile("" ::: "memory");
}
#define STAMP(n) do {} while (0)
#define PCNT(n, v) do {} while (0)
#define FLUSH_STAMPS() do {} while (0)
#define SB() __builtin_amdgcn_sched_barrier(0)
#define MEMCLOB() asm volatile("" ::: "memory")
template <int N> __device__ __forceinline__ void wait_vm() { asm volatile("s_waitcnt vmcnt(%0)" ::"n"(N) : "memory"); }

constexpr int window(int n) { return (n >= 32 - 2 * (WT - W1) && n < 32) ? W1 + (n - (32 - 2 * (WT - W1)) + 2) / 2 : W1; }
constexpr int issued(int n) {
  int v = RD;
  for (int k = 0; k <= n; ++k) { int w = k + 1 + window(k); if (w > k + 1 + RD - 1) w = k + RD; if (w > 64) w = 64; if (w > v) v = w; }
  return v;
}
__device__ __forceinline__ int ffn_unit(int rk, int grp) { return rk < 6 ? 2 * (grp + 8 * (rk >> 1)) + (rk & 1) : rk == 6 ? 48 + grp : NFU; }
constexpr int L_FLAG = 0;
constexpr int L_STAT = 64;
constexpr int L_INFO = 96;
constexpr int L_FAC = 192;
constexpr int L_M = 128;
constexpr int L_Q = 256;
constexpr int L_HH = 512;
constexpr int L_O = 1024;
constexpr int O_RS = 320;
constexpr int L_LN2 = 3328;
constexpr int LN2_RS = 1344;
constexpr int L_RED = 8704;
constexpr int L_W = L_RED + 8192;
constexpr int L_W2 = L_W + 2 * 640 * 64;
constexpr int L_BYTES = L_W2 + 32 * 1280;
}

extern "C" __global__ void __launch_bounds__(NTHR, 3) fused_block(Params p) {
  __shared__ __attribute__((aligned(16))) unsigned char smem[L_BYTES];
  LAS unsigned char* L = (LAS unsigned char*)smem;
  const int tid = threadIdx.x, lane = tid & 63, wid = __builtin_amdgcn_readfirstlane(tid >> 6);
  const int bid = blockIdx.x;
  const int r = lane >> 4, c = lane & 15;
  asm volatile("" :: "s"(p.x), "s"(p.K), "s"(p.V), "s"(p.wq), "s"(p.wo), "s"(p.w1), "s"(p.w3), "s"(p.w2), "s"(p.out), "s"(p.ws));
  STAMP(0);
  const int h = (bid & 7) + 8 * (bid >> 6), bs = (bid >> 3) & 7, b = bs >> 1, s = bs & 1;
  LAS u32* flags = (LAS u32*)(L + L_FLAG);
  LAS float* mbuf = (LAS float*)(L + L_M);
  LAS float* lbuf = mbuf + 8;
  const __amdgpu_buffer_rsrc_t wsr = mkrsrc(p.ws, WS_USED);
  const int t = wid & 3;
  const int bsel = (lane >> 3) & 1;

  const size_t kvoff = ((size_t)(b * NH + h) * SK + (SK - WIN) + s * 1024) * DH;
  const __amdgpu_buffer_rsrc_t kr = mkrsrc(p.K + kvoff, 1024 * DH * 2), vr = mkrsrc(p.V + kvoff, 1024 * DH * 2);
  const int kvo = wid * 1024 + lane * 16;
  v4u ring[RD];
  u32 xr[5];
  v4u wqv[5];
  u64 epoch = 0;
  if (wid < 8) {
#pragma unroll
    for (int n = 0; n < W0; ++n) ring[n] = ld_nt(kr, kvo, n * 8192);
  } else {
    const int row0 = 80 * bs + 20 * t + r;
#pragma unroll
    for (int k = 0; k < 5; ++k) xr[k] = ((const GAS u32*)p.x)[t * 320 + lane + 64 * k];
    const __amdgpu_buffer_rsrc_t qr = mkrsrc(p.wq + h * DH, DM * NH * DH * 2 - h * DH * 2);
#pragma unroll
    for (int i = 0; i < 5; ++i) wqv[i] = ld_nt(qr, row0 * (NH * DH * 2) + c * 16, i * 4 * (NH * DH * 2));
    epoch = __hip_atomic_load((GAS u64*)p.ws, RLX_AGENT);
    if (wid == 8 && lane < 16) flags[lane] = 0u;
  }
  STAMP(9);
  SB();
  __syncthreads();

  if (__builtin_expect(wid >= 8, 1)) {
  const u32 tstart = (u32)__builtin_amdgcn_s_memrealtime();
  u32 pfv = 0;
  {
    u64 pc; u32 off;
    asm volatile("s_getpc_b64 %0\n.Lpf_here%=:\n\ts_mov_b32 %1, .Lpf_here%=-fused_block" : "=s"(pc), "=s"(off));
    if (bid < 8) {
      const u32 o = (u32)t * 8192u + lane * 128u;
      if (o < CODE_BYTES) pfv = *(const GAS u32*)(pc - off + o);
    }
  }
  u32 tag, t16;
  {
    LAS u32* xw = (LAS u32*)(L + L_LN2 + t * LN2_RS);
#pragma unroll
    for (int k = 0; k < 5; ++k) xw[lane + 64 * k] = xr[k];
    const float pivot = first_lane(blo(xr[0]));
    float s1 = 0.f, s2 = 0.f;
#pragma unroll
    for (int k = 0; k < 5; ++k) { const float d0 = blo(xr[k]) - pivot, d1 = bhi(xr[k]) - pivot; s1 += d0 + d1; s2 = fmaf(d0, d0, fmaf(d1, d1, s2)); }
    float mb, ri;
    ln_finish(pivot, s1, s2, mb, ri);
    if (lane == 0) *(LAS f2*)(L + L_STAT + t * 8) = f2{mb, ri};
    lds_arrive(flags + 0, lane);
    u32 sel[4];
#pragma unroll
    for (int k = 0; k < 4; ++k) sel[k] = mksel(lane & 7, k, true);
    lds_wait<0>(flags + 0, 4);
    f2 st[2];
    st[0] = *(LAS f2*)(L + L_STAT + bsel * 8); st[1] = *(LAS f2*)(L + L_STAT + (2 + bsel) * 8);
    u32 xe[5][2];
#pragma unroll
    for (int i = 0; i < 5; ++i)
#pragma unroll
      for (int P = 0; P < 2; ++P) xe[i][P] = *(LAS u16*)(L + L_LN2 + (2 * P + bsel) * LN2_RS + (80 * bs + 20 * t + r + 4 * i) * 2);
    f4 acc[2] = {{0.f, 0.f, 0.f, 0.f}, {0.f, 0.f, 0.f, 0.f}};
#pragma unroll
    for (int i = 0; i < 5; ++i)
#pragma unroll
      for (int P = 0; P < 2; ++P) {
        const u32 lv = pack2(rb(blo(xe[i][P]) - st[P].x) * st[P].y, 0.f);
        v4u a;
#pragma unroll
        for (int k = 0; k < 4; ++k) a[k] = __builtin_amdgcn_perm(lv, lv, sel[k]);
        acc[P] = mfma(a, wqv[i], acc[P]);
      }
#pragma unroll
    for (int P = 0; P < 2; ++P) *(LAS f4*)(L + L_RED + t * 2048 + P * 1024 + lane * 16) = acc[P];
    lds_arrive(flags + 1, lane);
    tag = __builtin_amdgcn_readfirstlane(((u32)epoch + 1u) & 0x3ffu);
    t16 = __builtin_amdgcn_readfirstlane(((u32)epoch + 1u) & 0xffffu);
    lds_wait<0>(flags + 1, 4);
    if (lane < 32) {
      f4 sum = {0.f, 0.f, 0.f, 0.f};
#pragma unroll
      for (int ww = 0; ww < 4; ++ww) sum += *(LAS f4*)(L + L_RED + ww * 2048 + (t >> 1) * 1024 + (16 * (2 * (t & 1) + (lane & 1)) + (lane >> 1)) * 16);
      st_sc1(wsr, OFF_XQ + ((h * 4 + t) * 8 + bs) * 512 + lane * 16, enc4(sum.x, sum.y, sum.z, sum.w, tag));
    }
  }
  STAMP(8);
  if (bid < 8 && t == 1 && lane == 0) __hip_atomic_store((GAS u32*)(p.ws + OFF_CNT + bid * 128), t16 << 16, RLX_AGENT);
  if (t == 0) {
    if (QSLEEP) __builtin_amdgcn_s_sleep(QSLEEP);
    v4u g[4];
    const int go = OFF_XQ + (h * 4 + b) * 4096 + lane * 16;
#pragma nounroll
    for (u32 spin = 0;; ++spin) {
      MEMCLOB();
      u32 bad = 0;
#pragma unroll
      for (int k = 0; k < 4; ++k) g[k] = ld_poll_nt(wsr, go + k * 1024);
#pragma unroll
      for (int k = 0; k < 4; ++k) bad |= bad4(g[k], tag);
      if (__all((bad & 0x3ffu) == 0u) || spin > SPIN_CAP) break;
      __builtin_amdgcn_s_sleep(QPS);
    }
    const f4 sum = (dec4(g[0]) + dec4(g[1])) + (dec4(g[2]) + dec4(g[3]));
    const float q0 = xhalf_sum(sum.x), q1 = xhalf_sum(sum.y), q2 = xhalf_sum(sum.z), q3 = xhalf_sum(sum.w);
    if (lane < 32) *(LAS v2u*)(L + L_Q + lane * 8) = v2u{pack2(q0, q1), pack2(q2, q3)};
    lds_arrive(flags + 3, lane);
    STAMP(1);
    lds_wait<8>(flags + 5, 4);
    const u32 now = (u32)__builtin_amdgcn_s_memrealtime();
    lds_wait<16>(flags + 5, 8);
    const float mw = mbuf[lane & 7], lw = lbuf[lane & 7];
    float M = fmaxf(mw, dppf<XOR1>(mw)); M = fmaxf(M, dppf<XOR2>(M)); M = fmaxf(M, dppf<XOR7>(M));
    const float ew = __builtin_amdgcn_exp2f((mw - M) * LOG2E);
    float Ls = lw * ew; Ls += dppf<XOR1>(Ls); Ls += dppf<XOR2>(Ls); Ls += dppf<XOR7>(Ls);
    Ls = first_lane(Ls);
    const v4u mine = v4u{enc1(M, tag), enc1(Ls, tag), (now << 10) | tag, (now << 10) | tag};
    if (lane == 0) st_sc1(wsr, OFF_XL + (h * 8 + bs) * 128, mine);
    v4u gl;
#pragma nounroll
    for (u32 spin = 0;; ++spin) {
      MEMCLOB();
      gl = ld_poll_nt(wsr, OFF_XL + (h * 8 + (bs ^ 1)) * 128);
      if (__all(ok4(gl, tag)) || spin > SPIN_CAP) break;
      __builtin_amdgcn_s_sleep(16);
    }
    const float M0 = dec(mine.x), L0 = dec(mine.y), M1 = dec(gl.x), L1 = dec(gl.y), Mx = fmaxf(M0, M1);
    const float e0 = L0 * __builtin_amdgcn_exp2f((M0 - Mx) * LOG2E), e1 = L1 * __builtin_amdgcn_exp2f((M1 - Mx) * LOG2E);
    if (lane < 8) *(LAS float*)(L + L_FAC + lane * 4) = __builtin_amdgcn_exp2f((mw - Mx) * LOG2E) * __builtin_amdgcn_rcpf(e0 + e1);
    if (lane == 0) *(LAS u32*)(L + L_INFO) = tag;
    lds_arrive(flags + 8, lane);
    STAMP(2);
  }
  if (t == 1) {
#pragma nounroll
    for (u32 spin = 0; spin <= SPIN_CAP; ++spin) {
      MEMCLOB();
      const u32 v = __builtin_amdgcn_raw_buffer_load_b32(wsr, OFF_CNT + (bid & 7) * 128, 0, AUX_SC1 | AUX_NT);
      if (__builtin_amdgcn_readfirstlane(v >> 16) == t16) break;
      __builtin_amdgcn_s_sleep(16);
    }
    lds_wait<8>(flags + 9, 8);
    u32 old = 0;
    if (lane == 0) old = __hip_atomic_fetch_add((GAS u32*)(p.ws + OFF_CNT + (bid & 7) * 128), 1u, RLX_AGENT);
    if (lane == 0) *(LAS u32*)(L + L_INFO + 8) = old & 31u;
    lds_arrive(flags + 10, lane);
  }
  asm volatile("" :: "v"(pfv));
  lds_wait<8>(flags + 10, 1);
  const int rk = DYNROLE ? __builtin_amdgcn_readfirstlane(*(LAS u32*)(L + L_INFO + 8)) : (bid >> 3), grp = bid & 7;
  const int u = ffn_unit(rk, grp);
  const int role = rk >= 12 ? 0 : (rk < 7 && u < NFU) ? 1 : 3;
  const int task = grp * 10 + rk - 12, cs = task >> 2, sb = task & 3;
  const bool owner = rk >= 12 && rk < 22 && t == 0;
  const int uo = grp * 20 + rk - 12, hh_o = uo / 5, j_o = uo - 5 * hh_o;
  const bool ffn = role == 1;
  const int q4 = (lane & 15) >> 2, p4 = lane & 3;
  v4u bw[20];
  const int sw2 = 2 * (q4 + 4 * (r & 1));
  lds_wait<8>(flags + 7, 8);
  if (TIMED && (role == 0 || ffn)) {
    u32 tk = tstart;
    {
      const v4u gx = __builtin_amdgcn_raw_buffer_load_b128(wsr, OFF_XL + ((lane & 31) * 8 + (lane & 7)) * 128, 0, AUX_SC1);
      if (ok4(gx, tag)) tk = gx.z >> 10; }
    u32 el = (tk - tstart) & 0x3fffffu;
    el = max(el, dppu<XOR1>(el)); el = max(el, dppu<XOR2>(el)); el = max(el, dppu<XOR7>(el)); el = max(el, dppu<XOR15>(el));
    { auto sw = __builtin_amdgcn_permlane16_swap(el, el, false, false); el = max(sw[0], sw[1]); }
    el = __builtin_amdgcn_readfirstlane(el);
    if (el > 200u && el < 4000u) {
      const u32 target = el + ((el * TSCALE) >> 8) - (ffn ? FLEAD : OLEAD);
#pragma nounroll
      for (u32 spin = 0; spin < 4096; ++spin) { if ((((u32)__builtin_amdgcn_s_memrealtime() - tstart) & 0x3fffffu) >= target) break; __builtin_amdgcn_s_sleep(2); }
    }
  }
  if (role == 0 || ffn) {
    { const int nsl = ffn ? FDLY : ((31 - rk) * OSLOPE) >> 3;
#pragma nounroll
      for (int i = 0; i < nsl; ++i) __builtin_amdgcn_s_sleep(4); }
    STAMP(10);
    if (role == 0) {
      const __amdgpu_buffer_rsrc_t orr = mkrsrc(p.wo + (size_t)hh_o * DH * DM + 128 * j_o, DH * DM * 2 - 256 * j_o);
      const int vo0 = (32 * t + r) * (DM * 2) + (c ^ (2 * r)) * 16, vo1 = (32 * t + r) * (DM * 2) + (c ^ (2 * r + 8)) * 16;
#pragma unroll
      for (int i = 0; i < 8; ++i) dma_nt(orr, L + L_W + (32 * t + 4 * i) * 256, (i & 2) ? vo1 : vo0, i * 4 * DM * 2);
    } else {
      const __amdgpu_buffer_rsrc_t w1r = mkrsrc(p.w1 + 32 * u, DM * FF * 2 - 64 * u), w3r = mkrsrc(p.w3 + 32 * u, DM * FF * 2 - 64 * u);
      const int rw = lane >> 2, c4 = lane & 3, gp = (rw >> 3) & 1;
      const int vo1 = (160 * t + rw) * (FF * 2) + (c4 ^ gp) * 16, vo3 = (160 * t + rw) * (FF * 2) + (c4 ^ (2 + gp)) * 16;
#pragma unroll
      for (int i = 0; i < 10; ++i) dma_nt<W13AUX>(w1r, L + L_W + (160 * t + 16 * i) * 64, vo1, i * 16 * FF * 2);
#pragma unroll
      for (int i = 0; i < 10; ++i) dma_nt<W13AUX>(w3r, L + L_W + 640 * 64 + (160 * t + 16 * i) * 64, vo3, i * 16 * FF * 2);
    }
    if (ffn) {
      wait_vm<0>();
      lds_arrive(flags + 2, lane);
      lds_wait<0>(flags + 2, 4);
      STAMP(11);
#pragma unroll
      for (int ks = 0; ks < 20; ++ks) bw[ks] = trfrag(L + L_W + (p4 >> 1) * (640 * 64) + (32 * ks + 8 * r + q4) * 64 + ((t ^ (2 * (p4 >> 1) + (r & 1))) * 16) + (p4 & 1) * 8, 4 * 64);
      STAMP(12);
    }
  }
  const int cb = c & 3, cr = c >> 2;
  if (role == 0) {
    v4u g;
    const int go = OFF_X1 + (hh_o * 8 + 2 * t) * 512 + lane * 16;
#pragma nounroll
    for (u32 spin = 0;; ++spin) {
      MEMCLOB();
      g = ld_poll(wsr, go);
      if (__all(ok4(g, tag)) || spin > SPIN_CAP) break;
      if (PST) __builtin_amdgcn_s_sleep(PST);
    }
    STAMP(5);
    const f4 og = dec4(g);
    const float o0 = xhalf_sum(og.x), o1 = xhalf_sum(og.y), o2 = xhalf_sum(og.z), o3 = xhalf_sum(og.w);
    if (lane < 32) *(LAS v2u*)(L + L_O + t * O_RS + lane * 8) = v2u{pack2(o0, o1), pack2(o2, o3)};
    wait_vm<0>();
    __syncthreads();
    f4 d0 = {0.f, 0.f, 0.f, 0.f}, d1 = {0.f, 0.f, 0.f, 0.f};
#pragma unroll
    for (int ks = 0; ks < 4; ++ks) {
      const v4u a = *(LAS v4u*)(L + L_O + cb * O_RS + ks * 64 + r * 16);
      d0 = mfma(trfrag(L + L_W + (32 * ks + 8 * r + q4) * 256 + (((4 * t + (p4 >> 1)) ^ sw2) * 16) + (p4 & 1) * 8, 4 * 256), a, d0);
      d1 = mfma(trfrag(L + L_W + (32 * ks + 8 * r + q4) * 256 + (((4 * t + 2 + (p4 >> 1)) ^ sw2) * 16) + (p4 & 1) * 8, 4 * 256), a, d1);
    }
    const f4 y = (cr & 1) ? d1 : d0;
    if (cr < 2) st_sc1(wsr, OFF_X2 + ((4 * j_o + t) * 4 + cb) * X2_BLK + hh_o * 128 + cr * 64 + r * 16, enc4(y.x, y.y, y.z, y.w, tag));
    STAMP(6);
  } else if (ffn) {
    v4u g[3];
    const int go = OFF_X3 + (X3REP > 1 ? grp * X3_REP : 0) + t * 2560 + lane * 16;
    const bool has2 = lane < 32;
#pragma nounroll
    for (u32 spin = 0;; ++spin) {
      MEMCLOB();
      g[0] = ld_poll(wsr, go); g[1] = ld_poll(wsr, go + 1024); g[2] = ld_poll(wsr, go + (has2 ? 2048 : 0));
      if (__all(((bad4(g[0], tag) | bad4(g[1], tag) | bad4(g[2], tag)) & 0x3ffu) == 0u) || spin > SPIN_CAP) break;
      if (PST) __builtin_amdgcn_s_sleep(PST);
    }
    STAMP(5);
    const float pivot = first_lane(dec(g[0].x));
    float s1 = 0.f, s2 = 0.f;
#pragma unroll
    for (int k = 0; k < 3; ++k)
#pragma unroll
      for (int e = 0; e < 4; ++e) { const float d = (k < 2 || has2) ? dec(g[k][e]) - pivot : 0.f; s1 += d; s2 = fmaf(d, d, s2); }
    float mb, ri;
    ln_finish(pivot, s1, s2, mb, ri);
    LAS unsigned char* lp = L + L_LN2 + t * LN2_RS + lane * 8;
#pragma unroll
    for (int k = 0; k < 3; ++k)
      if (k < 2 || has2) *(LAS v2u*)(lp + 512 * k) = v2u{pack2(rb(dec(g[k].x) - mb) * ri, rb(dec(g[k].y) - mb) * ri), pack2(rb(dec(g[k].z) - mb) * ri, rb(dec(g[k].w) - mb) * ri)};
    __syncthreads();
    STAMP(13);
    f4 da = {0.f, 0.f, 0.f, 0.f}, db = {0.f, 0.f, 0.f, 0.f};
#pragma unroll
    for (int ks = 0; ks < 20; ks += 2) {
      da = mfma(bw[ks], *(LAS v4u*)(L + L_LN2 + cb * LN2_RS + ks * 64 + r * 16), da);
      db = mfma(bw[ks + 1], *(LAS v4u*)(L + L_LN2 + cb * LN2_RS + ks * 64 + 64 + r * 16), db);
    }
    const f4 d = da + db;
    const float pre = rb(cr == 0 ? d.x : cr == 1 ? d.y : cr == 2 ? d.z : d.w);
    auto sw = __builtin_amdgcn_permlane32_swap(__float_as_uint(pre), __float_as_uint(pre), false, false);
    const float gs = __uint_as_float(sw[0]), us = __uint_as_float(sw[1]);
    const float si = rb(gs * __builtin_amdgcn_rcpf(1.0f + __builtin_amdgcn_exp2f(-gs * LOG2E)));
    if (lane < 32) *(LAS u16*)(L + L_HH + cb * 64 + (8 * t + 4 * r + cr) * 2) = (u16)(pack2(si * us, 0.f) & 0xffffu);
    STAMP(14);
    __syncthreads();
    STAMP(6);
  }
  if (owner) {
    const int pp = lane >> 3, q = lane & 7;
    const v2u xv = *(LAS v2u*)(L + L_LN2 + sb * LN2_RS + (32 * cs + 4 * q) * 2);
    f4 hv;
    {
      v4u g[4];
      const int go = OFF_X2 + (cs * 4 + sb) * X2_BLK + pp * 128 + q * 16;
  #pragma nounroll
    for (u32 spin = 0;; ++spin) {
        MEMCLOB();
#pragma unroll
        for (int m = 0; m < 4; ++m) g[m] = ld_poll(wsr, go + m * 1024);
        if (__all((((bad4(g[0], tag) | bad4(g[1], tag)) | (bad4(g[2], tag) | bad4(g[3], tag))) & 0x3ffu) == 0u) || spin > SPIN_CAP) break;
        if (PST) __builtin_amdgcn_s_sleep(PST);
      }
      STAMP(3);
      const f4 y = (dec4(g[0]) + dec4(g[1])) + (dec4(g[2]) + dec4(g[3]));
      hv.x = rb(blo(xv.x) + rb(sum8p(y.x))); hv.y = rb(bhi(xv.x) + rb(sum8p(y.y)));
      hv.z = rb(blo(xv.y) + rb(sum8p(y.z))); hv.w = rb(bhi(xv.y) + rb(sum8p(y.w)));
    }
    if (pp < X3REP) st_sc1(wsr, OFF_X3 + pp * X3_REP + sb * 2560 + cs * 128 + q * 16, enc4(hv.x, hv.y, hv.z, hv.w, tag));
    STAMP(4);
    {
      if (X4DLY) __builtin_amdgcn_s_sleep(X4DLY);
      const int go = OFF_X4 + (cs * 4 + sb) * X4_BLK + pp * 128 + q * 16;
      v4u g[7];
      const bool last = pp < NFU - 48;
#pragma nounroll
      for (u32 spin = 0;; ++spin) {
        MEMCLOB();
        g[0] = __builtin_amdgcn_raw_buffer_load_b128(wsr, go, 0, AUX_SC1 | X4NT);
        if (__all((bad4(g[0], tag) & 0x3ffu) == 0u) || spin > SPIN_CAP) break;
      }
      if (X4GAP) __builtin_amdgcn_s_sleep(X4GAP);
#pragma nounroll
      for (u32 spin = 0;; ++spin) {
        MEMCLOB();
#pragma unroll
        for (int m = 1; m < 6; ++m) g[m] = __builtin_amdgcn_raw_buffer_load_b128(wsr, go + m * 1024, 0, AUX_SC1 | X4NT2);
        g[6] = __builtin_amdgcn_raw_buffer_load_b128(wsr, go + (last ? 6144 : 1024), 0, AUX_SC1 | X4NT2);
        u32 bad = 0;
#pragma unroll
        for (int m = 1; m < 7; ++m) bad |= bad4(g[m], tag);
        if (__all((bad & 0x3ffu) == 0u) || spin > SPIN_CAP) break;
      }
      STAMP(13);
      f4 z = ((dec4(g[0]) + dec4(g[1])) + (dec4(g[2]) + dec4(g[3]))) + (dec4(g[4]) + dec4(g[5]));
      if (last) z += dec4(g[6]);
      const float o0 = hv.x + rb(sum8p(z.x)), o1 = hv.y + rb(sum8p(z.y)), o2 = hv.z + rb(sum8p(z.z)), o3 = hv.w + rb(sum8p(z.w));
      if (lane < 8) ((GAS v2u*)p.out)[sb * 160 + 8 * cs + q] = v2u{pack2(o0, o1), pack2(o2, o3)};
    }
    if (task == 0 && lane == 0) __hip_atomic_store((GAS u64*)p.ws, epoch + 1, RLX_AGENT);
    STAMP(14);
  }
  STAMP(7);
  FLUSH_STAMPS();
    return;
  }
  {
    const int w = wid;
#pragma unroll
    for (int n = W0; n < RD; ++n) { wait_vm<W0 - 1>(); ring[n] = ld_nt(kr, kvo, n * 8192); SB(); }
    u32 selA[4];
#pragma unroll
    for (int k = 0; k < 4; ++k) selA[k] = mksel(c & 7, k, c < 8);
    STAMP(8);
#if !(SONLY & 1)
    lds_wait<1>(flags + 3, 1);
#endif
    const v4u qv = *(LAS v4u*)(L + L_Q + c * 16);
    STAMP(1);
#define REFILL(n) { _Pragma("unroll") for (int m_ = issued((n) - 1); m_ < issued(n); ++m_) { if (m_ < 32) ring[m_ % RD] = ld_nt(kr, kvo, m_ * 8192); else ring[m_ % RD] = ld_nt(vr, kvo, (m_ - 32) * 8192); } SB(); }
    float sc[2];
    float x[16];
#define QK_STEP(n) { const v4u kk = ring[(n) % RD]; x[(n) & 15] = dot2(kk.w, qv.w, dot2(kk.z, qv.z, dot2(kk.y, qv.y, dot2(kk.x, qv.x, 0.f)))); REFILL(n) }
#define QK4(n) QK_STEP(n) QK_STEP((n) + 1) QK_STEP((n) + 2) QK_STEP((n) + 3)
#define TR_STEP(HALF, CTRL, BIT) _Pragma("unroll") for (int hh = 0; hh < (HALF); ++hh) { \
      const float lo_ = x[hh], hi_ = x[hh + (HALF)]; const float t1_ = lo_ + dppf<CTRL>(lo_), t2_ = hi_ + dppf<CTRL>(hi_); x[hh] = (c & (BIT)) ? t2_ : t1_; }
#define TR_ALL() TR_STEP(8, XOR8, 8) TR_STEP(4, XOR7, 4) TR_STEP(2, XOR2, 2) TR_STEP(1, XOR1, 1)
    QK4(0) QK4(4) QK4(8) QK4(12)
    TR_ALL()
    sc[0] = rb(rb(x[0]) * 0.08838834764831845f);
    QK4(16) QK4(20) QK4(24) QK4(28)
    TR_ALL()
    sc[1] = rb(rb(x[0]) * 0.08838834764831845f);
    STAMP(2);
    const float mw = wave_max(fmaxf(sc[0], sc[1]));
    const float pe0 = __builtin_amdgcn_exp2f((sc[0] - mw) * LOG2E), pe1 = __builtin_amdgcn_exp2f((sc[1] - mw) * LOG2E);
    { const float lw = wave_sum(pe0 + pe1); if (lane == 0) { mbuf[w] = mw; lbuf[w] = lw; } }
    lds_arrive(flags + 5, lane);
    u32 pb[2];
    pb[0] = pack2(pe0, 0.f); pb[1] = pack2(pe1, 0.f);
    f4 acc = {0.f, 0.f, 0.f, 0.f};
#define PV_STEP(i) { const v4u vv = ring[(32 + (i)) % RD]; \
      const u32 xx = (u32)__builtin_amdgcn_update_dpp(0, (int)pb[(i) >> 4], 0x150 + ((i) & 15), 0xf, 0xf, false); \
      v4u a; a[0] = __builtin_amdgcn_perm(xx, xx, selA[0]); a[1] = __builtin_amdgcn_perm(xx, xx, selA[1]); a[2] = __builtin_amdgcn_perm(xx, xx, selA[2]); a[3] = __builtin_amdgcn_perm(xx, xx, selA[3]); \
      acc = mfma(a, vv, acc); REFILL(32 + (i)) \
      if ((i) == WTRIG && lane == 0) __hip_atomic_fetch_add(flags + 7, 1u, RLX_WG); \
      if ((i) == RTRIG && lane == 0) __hip_atomic_fetch_add(flags + 9, 1u, RLX_WG); }
#define PV4(i) PV_STEP(i) PV_STEP((i) + 1) PV_STEP((i) + 2) PV_STEP((i) + 3)
    PV4(0) PV4(4) PV4(8) PV4(12) PV4(16) PV4(20) PV4(24)
    MEMCLOB();
    u32 iflag = __hip_atomic_load(flags + 8, RLX_WG);
    float fw = *(volatile LAS float*)(L + L_FAC + w * 4);
    u32 stag = *(volatile LAS u32*)(L + L_INFO);
    PV4(28)
#if !(SONLY & (4 | 8))
    if (__builtin_amdgcn_readfirstlane(iflag) == 0u) { lds_wait<0>(flags + 8, 1); fw = *(volatile LAS float*)(L + L_FAC + w * 4); stag = *(volatile LAS u32*)(L + L_INFO); }
#endif
    acc *= fw;
    if (lane < 32) *(LAS f4*)(L + L_RED + w * 512 + (8 * c + 4 * r) * 4) = acc;
    asm volatile("s_waitcnt lgkmcnt(0)" ::: "memory");
    u32 old = 0;
    if (lane == 0) old = __hip_atomic_fetch_add(flags + 6, 1u, RLX_WG);
    old = __builtin_amdgcn_readfirstlane(old);
    STAMP(3);
#if !(SONLY & (4 | 8))
    if (old == 7) {
      MEMCLOB();
      if (lane < 32) {
        f4 sum = {0.f, 0.f, 0.f, 0.f};
#pragma unroll
        for (int ww = 0; ww < 8; ++ww) sum += *(LAS f4*)(L + L_RED + ww * 512 + lane * 16);
        st_sc1(wsr, OFF_X1 + (h * 8 + bs) * 512 + lane * 16, enc4(sum.x, sum.y, sum.z, sum.w, stag));
      }
      STAMP(4);
    }
#endif
#if !(SONLY & (4 | 8))
    if (w < 4) {
      lds_wait<8>(flags + 10, 1);
      const int rk = DYNROLE ? __builtin_amdgcn_readfirstlane(*(LAS u32*)(L + L_INFO + 8)) : (bid >> 3);
      const int u = ffn_unit(rk, bid & 7);
      if (u < NFU) {
        const int t = w, q4 = c >> 2, p4 = c & 3, cb = c & 3, cr = c >> 2;
        const __amdgpu_buffer_rsrc_t w2r = mkrsrc(p.w2 + (size_t)32 * u * DM, 32 * DM * 2);
        const int vo2 = r * (DM * 2) + (c ^ (2 * (r + 4 * (t & 1)))) * 16;
#pragma unroll
        for (int i = 0; i < 10; ++i) dma_nt(w2r, L + L_W2 + (10 * t + i) * 1024, vo2, t * (8 * DM * 2) + (i / 5) * 4 * DM * 2 + (i % 5) * 256);
        wait_vm<0>();
        lds_arrive(flags + 11, lane);
        lds_wait<1>(flags + 11, 4);
        const int sw2 = 2 * (q4 + 4 * (r & 1));
        v4u bd[10];
#pragma unroll
        for (int T = 0; T < 10; ++T) { const int col = 160 * t + 16 * T + 4 * p4; bd[T] = trfrag(L + L_W2 + ((2 * r) * 5 + (col >> 7)) * 1024 + q4 * 256 + ((((col & 127) >> 3) ^ sw2) * 16) + (p4 & 1) * 8, 5 * 1024); }
        asm volatile("" :: "v"(bd[0]), "v"(bd[1]), "v"(bd[2]), "v"(bd[3]), "v"(bd[4]), "v"(bd[5]), "v"(bd[6]), "v"(bd[7]), "v"(bd[8]), "v"(bd[9]));
        STAMP(10);
        __syncthreads();
        __syncthreads();
        STAMP(11);
        const v4u a = *(LAS v4u*)(L + L_HH + cb * 64 + r * 16);
        const int zo = OFF_X4 + cb * X4_BLK + u * 128 + (cr & 1) * 64 + r * 16;
        const f4 zero = {0.f, 0.f, 0.f, 0.f};
        v4u am[4];
#pragma unroll
        for (int k = 0; k < 4; ++k) am[k] = cr == k ? a : v4u{0u, 0u, 0u, 0u};
        f4 z0 = mfma(bd[0], am[0], zero), z1 = mfma(bd[4], am[0], zero), z2 = mfma(bd[8], am[0], zero);
        z0 = mfma(bd[1], am[1], z0); z1 = mfma(bd[5], am[1], z1); z2 = mfma(bd[9], am[1], z2);
        z0 = mfma(bd[2], am[2], z0); z1 = mfma(bd[6], am[2], z1);
        z0 = mfma(bd[3], am[3], z0); z1 = mfma(bd[7], am[3], z1);
        if (cr < 2) st_sc1(wsr, zo + (5 * t + 4) * 4 * X4_BLK, enc4(z2.x, z2.y, z2.z, z2.w, stag));
        st_sc1(wsr, zo + (5 * t + (cr >> 1)) * 4 * X4_BLK, enc4(z0.x, z0.y, z0.z, z0.w, stag));
        st_sc1(wsr, zo + (5 * t + 2 + (cr >> 1)) * 4 * X4_BLK, enc4(z1.x, z1.y, z1.z, z1.w, stag));
        STAMP(6);
      }
    }
#endif
    FLUSH_STAMPS();
  }
}

extern "C" void kernel_launch(void* const* d_in, const int* in_sizes, int n_in, void* d_out, int out_size, void* d_ws, size_t ws_size, hipStream_t stream) {
  Params p;
  p.x = (const u16*)d_in[0]; p.K = (const u16*)d_in[1]; p.V = (const u16*)d_in[2]; p.wq = (const u16*)d_in[3]; p.wo = (const u16*)d_in[4];
  p.w1 = (const u16*)d_in[6]; p.w3 = (const u16*)d_in[7]; p.w2 = (const u16*)d_in[8];
  p.out = (u16*)d_out; p.ws = (unsigned char*)d_ws;
  hipLaunchKernelGGL(fused_block, dim3(256), dim3(NTHR), 0, stream, p);
}
```

```cpp
#include <hip/hip_runtime.h>
#include <stdint.h>

typedef unsigned short u16;
typedef unsigned u32;
typedef unsigned long long u64;
typedef u32 v4u __attribute__((ext_vector_type(4)));
typedef u32 v2u __attribute__((ext_vector_type(2)));
typedef float f4 __attribute__((ext_vector_type(4)));
typedef float f2 __attribute__((ext_vector_type(2)));
typedef short s4 __attribute__((ext_vector_type(4)));
typedef __bf16 bf2 __attribute__((ext_vector_type(2)));
typedef __bf16 bf8 __attribute__((ext_vector_type(8)));
#define GAS __attribute__((address_space(1)))
#define LAS __attribute__((address_space(3)))
#define RLX_AGENT __ATOMIC_RELAXED, __HIP_MEMORY_SCOPE_AGENT
#define RLX_WG __ATOMIC_RELAXED, __HIP_MEMORY_SCOPE_WORKGROUP

namespace {
constexpr int DM = 640, NH = 32, DH = 128, SK = 8192, WIN = 2048, FF = 1728;
constexpr int NTHR = 768;
constexpr int RD = 24;
constexpr int W0 = 3;
constexpr int W1 = 4;
constexpr int RTRIG = 16;
constexpr int WTRIG = 22;
constexpr int TSCALE = 0;
constexpr int FLEAD = 0, OLEAD = 0;
constexpr u32 CODE_BYTES = 18944u;
constexpr int NFU = 54;
constexpr int AUX_NT = 2, AUX_SC1 = 16, RSRC_W3 = 0x00020000;
constexpr int OFF_CNT = 0x800;
constexpr int OFF_XL = 0x1000;
constexpr int OFF_XQ = 0x10000;
constexpr int OFF_X1 = 0x90000;
constexpr int OFF_X2 = 0xB0000;
constexpr int X2_BLK = 33 * 128;
constexpr int OFF_X3 = 0x108000;
constexpr int X3_REP = 10240 + 128;
constexpr int OFF_X4 = 0x120000;
constexpr int X4_BLK = 55 * 128;
constexpr int WS_USED = 0x1B0000;
constexpr u32 SPIN_CAP = 1u << 16;
constexpr float LOG2E = 1.4426950408889634f;
constexpr int L_FLAG = 0;
constexpr int L_STAT = 64;
constexpr int L_INFO = 96;
constexpr int L_M = 128;
constexpr int L_FAC = 192;
constexpr int L_Q = 256;
constexpr int L_HH = 512;
constexpr int L_O = 1024;
constexpr int O_RS = 320;
constexpr int L_LN2 = 3328;
constexpr int LN2_RS = 1344;
constexpr int L_RED = 8704;
constexpr int L_W = L_RED + 8192;
constexpr int L_W2 = L_W + 2 * 640 * 64;
constexpr int L_BYTES = L_W2 + 32 * 1280;
enum { F_STAT = 0, F_QPART = 1, F_W13 = 2, F_Q = 3, F_KDONE = 5, F_PART = 6, F_WTRIG = 7, F_FAC = 8, F_RTRIG = 9, F_ROLE = 10, F_W2 = 11 };

struct Params {
  const u16 *x, *K, *V, *wq, *wo, *w1, *w3, *w2;
  u16* out;
  unsigned char* ws;
};

__device__ __forceinline__ float rb(float f) { return (float)(__bf16)f; }
__device__ __forceinline__ u32 pack2(float lo, float hi) { bf2 v = {(__bf16)lo, (__bf16)hi}; return __builtin_bit_cast(u32, v); }
__device__ __forceinline__ float blo(u32 v) { return __uint_as_float(v << 16); }
__device__ __forceinline__ float bhi(u32 v) { return __uint_as_float(v & 0xffff0000u); }
__device__ __forceinline__ float dot2(u32 a, u32 b, float c) {
  return __builtin_amdgcn_fdot2_f32_bf16(__builtin_bit_cast(bf2, a), __builtin_bit_cast(bf2, b), c, false);
}
__device__ __forceinline__ f4 mfma(v4u a, v4u b, f4 c) {
  return __builtin_amdgcn_mfma_f32_16x16x32_bf16(__builtin_bit_cast(bf8, a), __builtin_bit_cast(bf8, b), c, 0, 0, 0);
}
template <int CTRL> __device__ __forceinline__ u32 dppu(u32 x) { return (u32)__builtin_amdgcn_mov_dpp((int)x, CTRL, 0xf, 0xf, true); }
template <int CTRL> __device__ __forceinline__ float dppf(float x) { return __uint_as_float(dppu<CTRL>(__float_as_uint(x))); }
constexpr int XOR1 = 0xB1, XOR2 = 0x4E, XOR7 = 0x141, XOR8 = 0x128, XOR15 = 0x140;
__device__ __forceinline__ float xrow_sum(float x) {
  auto s = __builtin_amdgcn_permlane16_swap(__float_as_uint(x), __float_as_uint(x), false, false);
  x = __uint_as_float(s[0]) + __uint_as_float(s[1]);
  auto t = __builtin_amdgcn_permlane32_swap(__float_as_uint(x), __float_as_uint(x), false, false);
  return __uint_as_float(t[0]) + __uint_as_float(t[1]);
}
__device__ __forceinline__ float xhalf_sum(float x) {
  auto t = __builtin_amdgcn_permlane32_swap(__float_as_uint(x), __float_as_uint(x), false, false);
  return __uint_as_float(t[0]) + __uint_as_float(t[1]);
}
__device__ __forceinline__ float xrow_max(float x) {
  auto s = __builtin_amdgcn_permlane16_swap(__float_as_uint(x), __float_as_uint(x), false, false);
  x = fmaxf(__uint_as_float(s[0]), __uint_as_float(s[1]));
  auto t = __builtin_amdgcn_permlane32_swap(__float_as_uint(x), __float_as_uint(x), false, false);
  return fmaxf(__uint_as_float(t[0]), __uint_as_float(t[1]));
}
__device__ __forceinline__ float wave_sum(float x) {
  x += dppf<XOR1>(x); x += dppf<XOR2>(x); x += dppf<XOR7>(x); x += dppf<XOR15>(x);
  return xrow_sum(x);
}
__device__ __forceinline__ float wave_max(float x) {
  x = fmaxf(x, dppf<XOR1>(x)); x = fmaxf(x, dppf<XOR2>(x)); x = fmaxf(x, dppf<XOR7>(x)); x = fmaxf(x, dppf<XOR15>(x));
  return xrow_max(x);
}
__device__ __forceinline__ float sum8p(float x) { x += dppf<XOR8>(x); return xrow_sum(x); }
__device__ __forceinline__ float first_lane(float x) { return __uint_as_float(__builtin_amdgcn_readfirstlane(__float_as_uint(x))); }

__device__ __forceinline__ __amdgpu_buffer_rsrc_t mkrsrc(const void* p, int bytes) { return __builtin_amdgcn_make_buffer_rsrc((void*)p, 0, bytes, RSRC_W3); }
__device__ __forceinline__ v4u ld_nt(__amdgpu_buffer_rsrc_t r, int voff, int soff) { return __builtin_amdgcn_raw_buffer_load_b128(r, voff, soff, AUX_NT); }
__device__ __forceinline__ void dma_nt(__amdgpu_buffer_rsrc_t r, LAS unsigned char* dst, int voff, int soff) {
  __builtin_amdgcn_raw_ptr_buffer_load_lds(r, (LAS void*)dst, 16, voff, soff, 0, AUX_NT);
}
__device__ __forceinline__ v4u ld_poll(__amdgpu_buffer_rsrc_t r, int voff) { return __builtin_amdgcn_raw_buffer_load_b128(r, voff, 0, AUX_SC1); }
__device__ __forceinline__ v4u ld_poll_nt(__amdgpu_buffer_rsrc_t r, int voff) { return __builtin_amdgcn_raw_buffer_load_b128(r, voff, 0, AUX_SC1 | AUX_NT); }
__device__ __forceinline__ void st_sc1(__amdgpu_buffer_rsrc_t r, int voff, v4u v) { __builtin_amdgcn_raw_buffer_store_b128(v, r, voff, 0, AUX_SC1); }
__device__ __forceinline__ u32 enc1(float a, u32 tag) { return ((__float_as_uint(a) + 0x200u) & ~0x3ffu) | tag; }
__device__ __forceinline__ v4u enc4(f4 a, u32 tag) { return v4u{enc1(a.x, tag), enc1(a.y, tag), enc1(a.z, tag), enc1(a.w, tag)}; }
__device__ __forceinline__ u32 bad4(v4u g, u32 tag) { return ((g.x ^ tag) | (g.y ^ tag)) | ((g.z ^ tag) | (g.w ^ tag)); }
__device__ __forceinline__ bool all_ok(u32 bad) { return __all((bad & 0x3ffu) == 0u); }
__device__ __forceinline__ float dec(u32 w) { return __uint_as_float(w & ~0x3ffu); }
__device__ __forceinline__ f4 dec4(v4u g) { return f4{dec(g.x), dec(g.y), dec(g.z), dec(g.w)}; }

__device__ __forceinline__ void ln_finish(float pivot, float s1, float s2, float& mb, float& ri) {
  const float m1 = wave_sum(s1) * (1.0f / DM), m2 = wave_sum(s2) * (1.0f / DM);
  const float var = fmaxf(m2 - m1 * m1, 0.f);
  mb = rb(pivot + m1);
  ri = __builtin_amdgcn_rcpf(rb(__builtin_amdgcn_sqrtf(rb(rb(var) + 1e-5f))));
}
__device__ __forceinline__ u32 mksel(int j, int k, bool act) {
  const u32 s = (j & 1) ? 0x01000c0cu : 0x0c0c0100u;
  return (act && (j >> 1) == k) ? s : 0x0c0c0c0cu;
}
__device__ __forceinline__ v4u sparse(u32 x, const u32 (&sel)[4]) {
  return v4u{__builtin_amdgcn_perm(x, x, sel[0]), __builtin_amdgcn_perm(x, x, sel[1]), __builtin_amdgcn_perm(x, x, sel[2]), __builtin_amdgcn_perm(x, x, sel[3])};
}
__device__ __forceinline__ v4u trfrag(LAS unsigned char* a0, int step4) {
  const v2u lo = __builtin_bit_cast(v2u, __builtin_amdgcn_ds_read_tr16_b64_v4i16((LAS s4*)a0));
  const v2u hi = __builtin_bit_cast(v2u, __builtin_amdgcn_ds_read_tr16_b64_v4i16((LAS s4*)(a0 + step4)));
  return v4u{lo.x, lo.y, hi.x, hi.y};
}
__device__ __forceinline__ void lds_arrive(LAS u32* cnt, int lane) {
  asm volatile("s_waitcnt lgkmcnt(0)" ::: "memory");
  if (lane == 0) __hip_atomic_fetch_add(cnt, 1u, RLX_WG);
}
template <int SL> __device__ __forceinline__ void lds_wait(LAS u32* cnt, u32 n) {
#pragma nounroll
  for (u32 spin = 0; spin < (1u << 19); ++spin) { if (__hip_atomic_load(cnt, RLX_WG) >= n) break; __builtin_amdgcn_s_sleep(SL); }
  asm volatile("" ::: "memory");
}
#define SB() __builtin_amdgcn_sched_barrier(0)
#define MEMCLOB() asm volatile("" ::: "memory")
template <int N> __device__ __forceinline__ void wait_vm() { asm volatile("s_waitcnt vmcnt(%0)" ::"n"(N) : "memory"); }

constexpr int issued(int n) { return n + 1 + W1 < RD ? RD : n + 1 + W1 > 64 ? 64 : n + 1 + W1; }
__device__ __forceinline__ int ffn_unit(int rk, int grp) { return rk < 6 ? 2 * (grp + 8 * (rk >> 1)) + (rk & 1) : rk == 6 ? 48 + grp : NFU; }
}

extern "C" __global__ void __launch_bounds__(NTHR, 3) fused_block(Params p) {
  __shared__ __attribute__((aligned(16))) unsigned char smem[L_BYTES];
  LAS unsigned char* L = (LAS unsigned char*)smem;
  const int tid = threadIdx.x, lane = tid & 63, wid = __builtin_amdgcn_readfirstlane(tid >> 6);
  const int bid = blockIdx.x, grp = bid & 7;
  const int r = lane >> 4, c = lane & 15;
  asm volatile("" :: "s"(p.x), "s"(p.K), "s"(p.V), "s"(p.wq), "s"(p.wo), "s"(p.w1), "s"(p.w3), "s"(p.w2), "s"(p.out), "s"(p.ws));
  const int h = (bid & 7) + 8 * (bid >> 6), bs = (bid >> 3) & 7, b = bs >> 1, s = bs & 1;
  LAS u32* flags = (LAS u32*)(L + L_FLAG);
  LAS float* mbuf = (LAS float*)(L + L_M);
  LAS float* lbuf = mbuf + 8;
  const __amdgpu_buffer_rsrc_t wsr = mkrsrc(p.ws, WS_USED);
  const int t = wid & 3;

  const size_t kvoff = ((size_t)(b * NH + h) * SK + (SK - WIN) + s * 1024) * DH;
  const __amdgpu_buffer_rsrc_t kr = mkrsrc(p.K + kvoff, 1024 * DH * 2), vr = mkrsrc(p.V + kvoff, 1024 * DH * 2);
  const int kvo = wid * 1024 + lane * 16;
  v4u ring[RD];
  u32 xr[5];
  v4u wqv[5];
  u64 epoch = 0;
  if (wid < 8) {
#pragma unroll
    for (int n = 0; n < W0; ++n) ring[n] = ld_nt(kr, kvo, n * 8192);
  } else {
#pragma unroll
    for (int k = 0; k < 5; ++k) xr[k] = ((const GAS u32*)p.x)[t * 320 + lane + 64 * k];
    const __amdgpu_buffer_rsrc_t qr = mkrsrc(p.wq + h * DH, DM * NH * DH * 2 - h * DH * 2);
#pragma unroll
    for (int i = 0; i < 5; ++i) wqv[i] = ld_nt(qr, (80 * bs + 20 * t + r) * (NH * DH * 2) + c * 16, i * 4 * (NH * DH * 2));
    epoch = __hip_atomic_load((GAS u64*)p.ws, RLX_AGENT);
    if (wid == 8 && lane < 16) flags[lane] = 0u;
  }
  SB();
  __syncthreads();

  if (__builtin_expect(wid >= 8, 1)) {
    const u32 tstart = (u32)__builtin_amdgcn_s_memrealtime();
    u32 pfv = 0;
    {
      u64 pc; u32 off;
      asm volatile("s_getpc_b64 %0\n.Lpf_here%=:\n\ts_mov_b32 %1, .Lpf_here%=-fused_block" : "=s"(pc), "=s"(off));
      if (bid < 8) {
        const u32 o = (u32)t * 8192u + lane * 128u;
        if (o < CODE_BYTES) pfv = *(const GAS u32*)(pc - off + o);
      }
    }
    u32 tag, t16;
    {
      const int bsel = (lane >> 3) & 1;
      LAS u32* xw = (LAS u32*)(L + L_LN2 + t * LN2_RS);
#pragma unroll
      for (int k = 0; k < 5; ++k) xw[lane + 64 * k] = xr[k];
      const float pivot = first_lane(blo(xr[0]));
      float s1 = 0.f, s2 = 0.f;
#pragma unroll
      for (int k = 0; k < 5; ++k) { const float d0 = blo(xr[k]) - pivot, d1 = bhi(xr[k]) - pivot; s1 += d0 + d1; s2 = fmaf(d0, d0, fmaf(d1, d1, s2)); }
      float mb, ri;
      ln_finish(pivot, s1, s2, mb, ri);
      if (lane == 0) *(LAS f2*)(L + L_STAT + t * 8) = f2{mb, ri};
      lds_arrive(flags + F_STAT, lane);
      u32 sel[4];
#pragma unroll
      for (int k = 0; k < 4; ++k) sel[k] = mksel(lane & 7, k, true);
      lds_wait<0>(flags + F_STAT, 4);
      f2 st[2];
      st[0] = *(LAS f2*)(L + L_STAT + bsel * 8); st[1] = *(LAS f2*)(L + L_STAT + (2 + bsel) * 8);
      u32 xe[5][2];
#pragma unroll
      for (int i = 0; i < 5; ++i)
#pragma unroll
        for (int P = 0; P < 2; ++P) xe[i][P] = *(LAS u16*)(L + L_LN2 + (2 * P + bsel) * LN2_RS + (80 * bs + 20 * t + r + 4 * i) * 2);
      f4 acc[2] = {{0.f, 0.f, 0.f, 0.f}, {0.f, 0.f, 0.f, 0.f}};
#pragma unroll
      for (int i = 0; i < 5; ++i)
#pragma unroll
        for (int P = 0; P < 2; ++P) acc[P] = mfma(sparse(pack2(rb(blo(xe[i][P]) - st[P].x) * st[P].y, 0.f), sel), wqv[i], acc[P]);
#pragma unroll
      for (int P = 0; P < 2; ++P) *(LAS f4*)(L + L_RED + t * 2048 + P * 1024 + lane * 16) = acc[P];
      lds_arrive(flags + F_QPART, lane);
      tag = __builtin_amdgcn_readfirstlane(((u32)epoch + 1u) & 0x3ffu);
      t16 = __builtin_amdgcn_readfirstlane(((u32)epoch + 1u) & 0xffffu);
      lds_wait<0>(flags + F_QPART, 4);
      if (lane < 32) {
        f4 sum = {0.f, 0.f, 0.f, 0.f};
#pragma unroll
        for (int ww = 0; ww < 4; ++ww) sum += *(LAS f4*)(L + L_RED + ww * 2048 + (t >> 1) * 1024 + (16 * (2 * (t & 1) + (lane & 1)) + (lane >> 1)) * 16);
        st_sc1(wsr, OFF_XQ + ((h * 4 + t) * 8 + bs) * 512 + lane * 16, enc4(sum, tag));
      }
    }
    if (bid < 8 && t == 1 && lane == 0) __hip_atomic_store((GAS u32*)(p.ws + OFF_CNT + bid * 128), t16 << 16, RLX_AGENT);
    if (t == 0) {
      __builtin_amdgcn_s_sleep(16);
      v4u g[4];
      const int go = OFF_XQ + (h * 4 + b) * 4096 + lane * 16;
#pragma nounroll
      for (u32 spin = 0;; ++spin) {
        MEMCLOB();
        u32 bad = 0;
#pragma unroll
        for (int k = 0; k < 4; ++k) g[k] = ld_poll_nt(wsr, go + k * 1024);
#pragma unroll
        for (int k = 0; k < 4; ++k) bad |= bad4(g[k], tag);
        if (all_ok(bad) || spin > SPIN_CAP) break;
        __builtin_amdgcn_s_sleep(4);
      }
      const f4 sum = (dec4(g[0]) + dec4(g[1])) + (dec4(g[2]) + dec4(g[3]));
      const float q0 = xhalf_sum(sum.x), q1 = xhalf_sum(sum.y), q2 = xhalf_sum(sum.z), q3 = xhalf_sum(sum.w);
      if (lane < 32) *(LAS v2u*)(L + L_Q + lane * 8) = v2u{pack2(q0, q1), pack2(q2, q3)};
      lds_arrive(flags + F_Q, lane);
      lds_wait<8>(flags + F_KDONE, 4);
      const u32 now = (u32)__builtin_amdgcn_s_memrealtime();
      lds_wait<16>(flags + F_KDONE, 8);
      const float mw = mbuf[lane & 7], lw = lbuf[lane & 7];
      float M = fmaxf(mw, dppf<XOR1>(mw)); M = fmaxf(M, dppf<XOR2>(M)); M = fmaxf(M, dppf<XOR7>(M));
      float Ls = lw * __builtin_amdgcn_exp2f((mw - M) * LOG2E); Ls += dppf<XOR1>(Ls); Ls += dppf<XOR2>(Ls); Ls += dppf<XOR7>(Ls);
      Ls = first_lane(Ls);
      const v4u mine = v4u{enc1(M, tag), enc1(Ls, tag), (now << 10) | tag, (now << 10) | tag};
      if (lane == 0) st_sc1(wsr, OFF_XL + (h * 8 + bs) * 128, mine);
      v4u gl;
#pragma nounroll
      for (u32 spin = 0;; ++spin) {
        MEMCLOB();
        gl = ld_poll_nt(wsr, OFF_XL + (h * 8 + (bs ^ 1)) * 128);
        if (all_ok(bad4(gl, tag)) || spin > SPIN_CAP) break;
        __builtin_amdgcn_s_sleep(16);
      }
      const float M0 = dec(mine.x), L0 = dec(mine.y), M1 = dec(gl.x), L1 = dec(gl.y), Mx = fmaxf(M0, M1);
      const float e0 = L0 * __builtin_amdgcn_exp2f((M0 - Mx) * LOG2E), e1 = L1 * __builtin_amdgcn_exp2f((M1 - Mx) * LOG2E);
      if (lane < 8) *(LAS float*)(L + L_FAC + lane * 4) = __builtin_amdgcn_exp2f((mw - Mx) * LOG2E) * __builtin_amdgcn_rcpf(e0 + e1);
      if (lane == 0) *(LAS u32*)(L + L_INFO) = tag;
      lds_arrive(flags + F_FAC, lane);
    }
    if (t == 1) {
#pragma nounroll
      for (u32 spin = 0; spin <= SPIN_CAP; ++spin) {
        MEMCLOB();
        const u32 v = __builtin_amdgcn_raw_buffer_load_b32(wsr, OFF_CNT + grp * 128, 0, AUX_SC1 | AUX_NT);
        if (__builtin_amdgcn_readfirstlane(v >> 16) == t16) break;
        __builtin_amdgcn_s_sleep(16);
      }
      lds_wait<8>(flags + F_RTRIG, 8);
      u32 old = 0;
      if (lane == 0) old = __hip_atomic_fetch_add((GAS u32*)(p.ws + OFF_CNT + grp * 128), 1u, RLX_AGENT);
      if (lane == 0) *(LAS u32*)(L + L_INFO + 8) = old & 31u;
      lds_arrive(flags + F_ROLE, lane);
    }
    asm volatile("" :: "v"(pfv));
    lds_wait<8>(flags + F_ROLE, 1);
    const int rk = __builtin_amdgcn_readfirstlane(*(LAS u32*)(L + L_INFO + 8));
    const int u = ffn_unit(rk, grp);
    const bool ffn = u < NFU, oproj = rk >= 12;
    const int uo = grp * 20 + rk - 12, hh_o = uo / 5, j_o = uo - 5 * hh_o;
    const int task = grp * 10 + rk - 12, cs = task >> 2, sb = task & 3;
    const bool owner = rk >= 12 && rk < 22 && t == 0;
    const int q4 = c >> 2, p4 = c & 3, sw2 = 2 * (q4 + 4 * (r & 1));
    v4u bw[20];
    if (oproj || ffn) {
      lds_wait<8>(flags + F_WTRIG, 8);
      {
        u32 tk = tstart;
        const v4u gx = ld_poll(wsr, OFF_XL + ((lane & 31) * 8 + (lane & 7)) * 128);
        if ((bad4(gx, tag) & 0x3ffu) == 0u) tk = gx.z >> 10;
        u32 el = (tk - tstart) & 0x3fffffu;
        el = max(el, dppu<XOR1>(el)); el = max(el, dppu<XOR2>(el)); el = max(el, dppu<XOR7>(el)); el = max(el, dppu<XOR15>(el));
        { auto sw = __builtin_amdgcn_permlane16_swap(el, el, false, false); el = max(sw[0], sw[1]); }
        el = __builtin_amdgcn_readfirstlane(el);
        if (el > 200u && el < 4000u) {
          const u32 target = el + ((el * TSCALE) >> 8) - (ffn ? FLEAD : OLEAD);
#pragma nounroll
          for (u32 spin = 0; spin < 4096; ++spin) { if ((((u32)__builtin_amdgcn_s_memrealtime() - tstart) & 0x3fffffu) >= target) break; __builtin_amdgcn_s_sleep(2); }
        }
      }
      if (oproj) {
        const __amdgpu_buffer_rsrc_t orr = mkrsrc(p.wo + (size_t)hh_o * DH * DM + 128 * j_o, DH * DM * 2 - 256 * j_o);
        const int vo0 = (32 * t + r) * (DM * 2) + (c ^ (2 * r)) * 16, vo1 = (32 * t + r) * (DM * 2) + (c ^ (2 * r + 8)) * 16;
#pragma unroll
        for (int i = 0; i < 8; ++i) dma_nt(orr, L + L_W + (32 * t + 4 * i) * 256, (i & 2) ? vo1 : vo0, i * 4 * DM * 2);
      } else {
        const __amdgpu_buffer_rsrc_t w1r = mkrsrc(p.w1 + 32 * u, DM * FF * 2 - 64 * u), w3r = mkrsrc(p.w3 + 32 * u, DM * FF * 2 - 64 * u);
        const int rw = lane >> 2, c4 = lane & 3, gp = (rw >> 3) & 1;
        const int vo1 = (160 * t + rw) * (FF * 2) + (c4 ^ gp) * 16, vo3 = (160 * t + rw) * (FF * 2) + (c4 ^ (2 + gp)) * 16;
#pragma unroll
        for (int i = 0; i < 10; ++i) dma_nt(w1r, L + L_W + (160 * t + 16 * i) * 64, vo1, i * 16 * FF * 2);
#pragma unroll
        for (int i = 0; i < 10; ++i) dma_nt(w3r, L + L_W + 640 * 64 + (160 * t + 16 * i) * 64, vo3, i * 16 * FF * 2);
        wait_vm<0>();
        lds_arrive(flags + F_W13, lane);
        lds_wait<0>(flags + F_W13, 4);
#pragma unroll
        for (int ks = 0; ks < 20; ++ks) bw[ks] = trfrag(L + L_W + (p4 >> 1) * (640 * 64) + (32 * ks + 8 * r + q4) * 64 + ((t ^ (2 * (p4 >> 1) + (r & 1))) * 16) + (p4 & 1) * 8, 4 * 64);
      }
    }
    const int cb = c & 3, cr = c >> 2;
    if (oproj) {
      v4u g;
      const int go = OFF_X1 + (hh_o * 8 + 2 * t) * 512 + lane * 16;
#pragma nounroll
      for (u32 spin = 0;; ++spin) {
        MEMCLOB();
        g = ld_poll(wsr, go);
        if (all_ok(bad4(g, tag)) || spin > SPIN_CAP) break;
      }
      const f4 og = dec4(g);
      const float o0 = xhalf_sum(og.x), o1 = xhalf_sum(og.y), o2 = xhalf_sum(og.z), o3 = xhalf_sum(og.w);
      if (lane < 32) *(LAS v2u*)(L + L_O + t * O_RS + lane * 8) = v2u{pack2(o0, o1), pack2(o2, o3)};
      wait_vm<0>();
      __syncthreads();
      f4 d0 = {0.f, 0.f, 0.f, 0.f}, d1 = {0.f, 0.f, 0.f, 0.f};
#pragma unroll
      for (int ks = 0; ks < 4; ++ks) {
        const v4u a = *(LAS v4u*)(L + L_O + cb * O_RS + ks * 64 + r * 16);
        d0 = mfma(trfrag(L + L_W + (32 * ks + 8 * r + q4) * 256 + (((4 * t + (p4 >> 1)) ^ sw2) * 16) + (p4 & 1) * 8, 4 * 256), a, d0);
        d1 = mfma(trfrag(L + L_W + (32 * ks + 8 * r + q4) * 256 + (((4 * t + 2 + (p4 >> 1)) ^ sw2) * 16) + (p4 & 1) * 8, 4 * 256), a, d1);
      }
      const f4 y = (cr & 1) ? d1 : d0;
      if (cr < 2) st_sc1(wsr, OFF_X2 + ((4 * j_o + t) * 4 + cb) * X2_BLK + hh_o * 128 + cr * 64 + r * 16, enc4(y, tag));
    } else if (ffn) {
      v4u g[3];
      const int go = OFF_X3 + grp * X3_REP + t * 2560 + lane * 16;
      const bool has2 = lane < 32;
#pragma nounroll
      for (u32 spin = 0;; ++spin) {
        MEMCLOB();
        g[0] = ld_poll(wsr, go); g[1] = ld_poll(wsr, go + 1024); g[2] = ld_poll(wsr, go + (has2 ? 2048 : 0));
        if (all_ok(bad4(g[0], tag) | bad4(g[1], tag) | bad4(g[2], tag)) || spin > SPIN_CAP) break;
      }
      const float pivot = first_lane(dec(g[0].x));
      float s1 = 0.f, s2 = 0.f;
#pragma unroll
      for (int k = 0; k < 3; ++k)
#pragma unroll
        for (int e = 0; e < 4; ++e) { const float d = (k < 2 || has2) ? dec(g[k][e]) - pivot : 0.f; s1 += d; s2 = fmaf(d, d, s2); }
      float mb, ri;
      ln_finish(pivot, s1, s2, mb, ri);
      LAS unsigned char* lp = L + L_LN2 + t * LN2_RS + lane * 8;
#pragma unroll
      for (int k = 0; k < 3; ++k)
        if (k < 2 || has2) *(LAS v2u*)(lp + 512 * k) = v2u{pack2(rb(dec(g[k].x) - mb) * ri, rb(dec(g[k].y) - mb) * ri), pack2(rb(dec(g[k].z) - mb) * ri, rb(dec(g[k].w) - mb) * ri)};
      __syncthreads();
      f4 da = {0.f, 0.f, 0.f, 0.f}, db = {0.f, 0.f, 0.f, 0.f};
#pragma unroll
      for (int ks = 0; ks < 20; ks += 2) {
        da = mfma(bw[ks], *(LAS v4u*)(L + L_LN2 + cb * LN2_RS + ks * 64 + r * 16), da);
        db = mfma(bw[ks + 1], *(LAS v4u*)(L + L_LN2 + cb * LN2_RS + ks * 64 + 64 + r * 16), db);
      }
      const f4 d = da + db;
      const float pre = rb(cr == 0 ? d.x : cr == 1 ? d.y : cr == 2 ? d.z : d.w);
      auto sw = __builtin_amdgcn_permlane32_swap(__float_as_uint(pre), __float_as_uint(pre), false, false);
      const float gs = __uint_as_float(sw[0]), us = __uint_as_float(sw[1]);
      const float si = rb(gs * __builtin_amdgcn_rcpf(1.0f + __builtin_amdgcn_exp2f(-gs * LOG2E)));
      if (lane < 32) *(LAS u16*)(L + L_HH + cb * 64 + (8 * t + 4 * r + cr) * 2) = (u16)(pack2(si * us, 0.f) & 0xffffu);
      __syncthreads();
    }
    if (owner) {
      const int pp = lane >> 3, q = lane & 7;
      const v2u xv = *(LAS v2u*)(L + L_LN2 + sb * LN2_RS + (32 * cs + 4 * q) * 2);
      f4 hv;
      {
        v4u g[4];
        const int go = OFF_X2 + (cs * 4 + sb) * X2_BLK + pp * 128 + q * 16;
#pragma nounroll
        for (u32 spin = 0;; ++spin) {
          MEMCLOB();
#pragma unroll
          for (int m = 0; m < 4; ++m) g[m] = ld_poll(wsr, go + m * 1024);
          if (all_ok((bad4(g[0], tag) | bad4(g[1], tag)) | (bad4(g[2], tag) | bad4(g[3], tag))) || spin > SPIN_CAP) break;
        }
        const f4 y = (dec4(g[0]) + dec4(g[1])) + (dec4(g[2]) + dec4(g[3]));
        hv.x = rb(blo(xv.x) + rb(sum8p(y.x))); hv.y = rb(bhi(xv.x) + rb(sum8p(y.y)));
        hv.z = rb(blo(xv.y) + rb(sum8p(y.z))); hv.w = rb(bhi(xv.y) + rb(sum8p(y.w)));
      }
      st_sc1(wsr, OFF_X3 + pp * X3_REP + sb * 2560 + cs * 128 + q * 16, enc4(hv, tag));
      __builtin_amdgcn_s_sleep(40);
      const int go = OFF_X4 + (cs * 4 + sb) * X4_BLK + pp * 128 + q * 16;
      v4u g[7];
      const bool last = pp < NFU - 48;
#pragma nounroll
      for (u32 spin = 0;; ++spin) {
        MEMCLOB();
        g[0] = ld_poll(wsr, go);
        if (all_ok(bad4(g[0], tag)) || spin > SPIN_CAP) break;
      }
#pragma nounroll
      for (u32 spin = 0;; ++spin) {
        MEMCLOB();
#pragma unroll
        for (int m = 1; m < 6; ++m) g[m] = ld_poll_nt(wsr, go + m * 1024);
        g[6] = ld_poll_nt(wsr, go + (last ? 6144 : 1024));
        u32 bad = 0;
#pragma unroll
        for (int m = 1; m < 7; ++m) bad |= bad4(g[m], tag);
        if (all_ok(bad) || spin > SPIN_CAP) break;
      }
      f4 z = ((dec4(g[0]) + dec4(g[1])) + (dec4(g[2]) + dec4(g[3]))) + (dec4(g[4]) + dec4(g[5]));
      if (last) z += dec4(g[6]);
      const float o0 = hv.x + rb(sum8p(z.x)), o1 = hv.y + rb(sum8p(z.y)), o2 = hv.z + rb(sum8p(z.z)), o3 = hv.w + rb(sum8p(z.w));
      if (lane < 8) ((GAS v2u*)p.out)[sb * 160 + 8 * cs + q] = v2u{pack2(o0, o1), pack2(o2, o3)};
      if (task == 0 && lane == 0) __hip_atomic_store((GAS u64*)p.ws, epoch + 1, RLX_AGENT);
    }
    return;
  }

  const int w = wid;
#pragma unroll
  for (int n = W0; n < RD; ++n) { wait_vm<W0 - 1>(); ring[n] = ld_nt(kr, kvo, n * 8192); SB(); }
  u32 selA[4];
#pragma unroll
  for (int k = 0; k < 4; ++k) selA[k] = mksel(c & 7, k, c < 8);
  lds_wait<1>(flags + F_Q, 1);
  const v4u qv = *(LAS v4u*)(L + L_Q + c * 16);
#define REFILL(n) { _Pragma("unroll") for (int m_ = issued((n) - 1); m_ < issued(n); ++m_) { if (m_ < 32) ring[m_ % RD] = ld_nt(kr, kvo, m_ * 8192); else ring[m_ % RD] = ld_nt(vr, kvo, (m_ - 32) * 8192); } SB(); }
  float sc[2];
  float x[16];
#define QK_STEP(n) { const v4u kk = ring[(n) % RD]; x[(n) & 15] = dot2(kk.w, qv.w, dot2(kk.z, qv.z, dot2(kk.y, qv.y, dot2(kk.x, qv.x, 0.f)))); REFILL(n) }
#define QK4(n) QK_STEP(n) QK_STEP((n) + 1) QK_STEP((n) + 2) QK_STEP((n) + 3)
#define TR_STEP(HALF, CTRL, BIT) _Pragma("unroll") for (int hh = 0; hh < (HALF); ++hh) { \
    const float lo_ = x[hh], hi_ = x[hh + (HALF)]; const float t1_ = lo_ + dppf<CTRL>(lo_), t2_ = hi_ + dppf<CTRL>(hi_); x[hh] = (c & (BIT)) ? t2_ : t1_; }
#define TR_ALL() TR_STEP(8, XOR8, 8) TR_STEP(4, XOR7, 4) TR_STEP(2, XOR2, 2) TR_STEP(1, XOR1, 1)
  QK4(0) QK4(4) QK4(8) QK4(12)
  TR_ALL()
  sc[0] = rb(rb(x[0]) * 0.08838834764831845f);
  QK4(16) QK4(20) QK4(24) QK4(28)
  TR_ALL()
  sc[1] = rb(rb(x[0]) * 0.08838834764831845f);
  const float mw = wave_max(fmaxf(sc[0], sc[1]));
  const float pe0 = __builtin_amdgcn_exp2f((sc[0] - mw) * LOG2E), pe1 = __builtin_amdgcn_exp2f((sc[1] - mw) * LOG2E);
  { const float lw = wave_sum(pe0 + pe1); if (lane == 0) { mbuf[w] = mw; lbuf[w] = lw; } }
  lds_arrive(flags + F_KDONE, lane);
  u32 pb[2];
  pb[0] = pack2(pe0, 0.f); pb[1] = pack2(pe1, 0.f);
  f4 acc = {0.f, 0.f, 0.f, 0.f};
#define PV_STEP(i) { const v4u vv = ring[(32 + (i)) % RD]; \
    const u32 xx = (u32)__builtin_amdgcn_update_dpp(0, (int)pb[(i) >> 4], 0x150 + ((i) & 15), 0xf, 0xf, false);     \
    acc = mfma(sparse(xx, selA), vv, acc); REFILL(32 + (i)) \
    if ((i) == WTRIG && lane == 0) __hip_atomic_fetch_add(flags + F_WTRIG, 1u, RLX_WG); \
    if ((i) == RTRIG && lane == 0) __hip_atomic_fetch_add(flags + F_RTRIG, 1u, RLX_WG); }
#define PV4(i) PV_STEP(i) PV_STEP((i) + 1) PV_STEP((i) + 2) PV_STEP((i) + 3)
  PV4(0) PV4(4) PV4(8) PV4(12) PV4(16) PV4(20) PV4(24)
  MEMCLOB();
  const u32 iflag = __hip_atomic_load(flags + F_FAC, RLX_WG);
  float fw = *(volatile LAS float*)(L + L_FAC + w * 4);
  u32 stag = *(volatile LAS u32*)(L + L_INFO);
  PV4(28)
  if (__builtin_amdgcn_readfirstlane(iflag) == 0u) { lds_wait<0>(flags + F_FAC, 1); fw = *(volatile LAS float*)(L + L_FAC + w * 4); stag = *(volatile LAS u32*)(L + L_INFO); }
  acc *= fw;
  if (lane < 32) *(LAS f4*)(L + L_RED + w * 512 + (8 * c + 4 * r) * 4) = acc;
  asm volatile("s_waitcnt lgkmcnt(0)" ::: "memory");
  u32 old = 0;
  if (lane == 0) old = __hip_atomic_fetch_add(flags + F_PART, 1u, RLX_WG);
  if (__builtin_amdgcn_readfirstlane(old) == 7u) {
    MEMCLOB();
    if (lane < 32) {
      f4 sum = {0.f, 0.f, 0.f, 0.f};
#pragma unroll
      for (int ww = 0; ww < 8; ++ww) sum += *(LAS f4*)(L + L_RED + ww * 512 + lane * 16);
      st_sc1(wsr, OFF_X1 + (h * 8 + bs) * 512 + lane * 16, enc4(sum, stag));
    }
  }
  if (w < 4) {
    lds_wait<8>(flags + F_ROLE, 1);
    const int u = ffn_unit(__builtin_amdgcn_readfirstlane(*(LAS u32*)(L + L_INFO + 8)), grp);
    if (u < NFU) {
      const int q4 = c >> 2, p4 = c & 3, cb = c & 3, cr = c >> 2;
      const __amdgpu_buffer_rsrc_t w2r = mkrsrc(p.w2 + (size_t)32 * u * DM, 32 * DM * 2);
      const int vo2 = r * (DM * 2) + (c ^ (2 * (r + 4 * (w & 1)))) * 16;
#pragma unroll
      for (int i = 0; i < 10; ++i) dma_nt(w2r, L + L_W2 + (10 * w + i) * 1024, vo2, w * (8 * DM * 2) + (i / 5) * 4 * DM * 2 + (i % 5) * 256);
      wait_vm<0>();
      lds_arrive(flags + F_W2, lane);
      lds_wait<1>(flags + F_W2, 4);
      const int sw2 = 2 * (q4 + 4 * (r & 1));
      v4u bd[10];
#pragma unroll
      for (int T = 0; T < 10; ++T) { const int col = 160 * w + 16 * T + 4 * p4; bd[T] = trfrag(L + L_W2 + ((2 * r) * 5 + (col >> 7)) * 1024 + q4 * 256 + ((((col & 127) >> 3) ^ sw2) * 16) + (p4 & 1) * 8, 5 * 1024); }
      asm volatile("" :: "v"(bd[0]), "v"(bd[1]), "v"(bd[2]), "v"(bd[3]), "v"(bd[4]), "v"(bd[5]), "v"(bd[6]), "v"(bd[7]), "v"(bd[8]), "v"(bd[9]));
      __syncthreads();
      __syncthreads();
      const v4u a = *(LAS v4u*)(L + L_HH + cb * 64 + r * 16);
      const int zo = OFF_X4 + cb * X4_BLK + u * 128 + (cr & 1) * 64 + r * 16;
      const f4 zero = {0.f, 0.f, 0.f, 0.f};
      v4u am[4];
#pragma unroll
      for (int k = 0; k < 4; ++k) am[k] = cr == k ? a : v4u{0u, 0u, 0u, 0u};
      f4 z0 = mfma(bd[0], am[0], zero), z1 = mfma(bd[4], am[0], zero), z2 = mfma(bd[8], am[0], zero);
      z0 = mfma(bd[1], am[1], z0); z1 = mfma(bd[5], am[1], z1); z2 = mfma(bd[9], am[1], z2);
      z0 = mfma(bd[2], am[2], z0); z1 = mfma(bd[6], am[2], z1);
      z0 = mfma(bd[3], am[3], z0); z1 = mfma(bd[7], am[3], z1);
      if (cr < 2) st_sc1(wsr, zo + (5 * w + 4) * 4 * X4_BLK, enc4(z2, stag));
      st_sc1(wsr, zo + (5 * w + (cr >> 1)) * 4 * X4_BLK, enc4(z0, stag));
      st_sc1(wsr, zo + (5 * w + 2 + (cr >> 1)) * 4 * X4_BLK, enc4(z1, stag));
    }
  }
}

extern "C" void kernel_launch(void* const* d_in, const int* in_sizes, int n_in, void* d_out, int out_size, void* d_ws, size_t ws_size, hipStream_t stream) {
  Params p;
  p.x = (const u16*)d_in[0]; p.K = (const u16*)d_in[1]; p.V = (const u16*)d_in[2]; p.wq = (const u16*)d_in[3]; p.wo = (const u16*)d_in[4];
  p.w1 = (const u16*)d_in[6]; p.w3 = (const u16*)d_in[7]; p.w2 = (const u16*)d_in[8];
  p.out = (u16*)d_out; p.ws = (unsigned char*)d_ws;
  hipLaunchKernelGGL(fused_block, dim3(256), dim3(NTHR), 0, stream, p);
}
```

```cpp
#include <hip/hip_runtime.h>
#include <stdint.h>

typedef unsigned short u16;
typedef unsigned u32;
typedef unsigned long long u64;
typedef u32 v4u __attribute__((ext_vector_type(4)));
typedef u32 v2u __attribute__((ext_vector_type(2)));
typedef float f4 __attribute__((ext_vector_type(4)));
typedef float f2 __attribute__((ext_vector_type(2)));
typedef short s4 __attribute__((ext_vector_type(4)));
typedef __bf16 bf2 __attribute__((ext_vector_type(2)));
typedef __bf16 bf8 __attribute__((ext_vector_type(8)));
#define GAS __attribute__((address_space(1)))
#define LAS __attribute__((address_space(3)))
#define RLX_AGENT __ATOMIC_RELAXED, __HIP_MEMORY_SCOPE_AGENT
#define RLX_WG __ATOMIC_RELAXED, __HIP_MEMORY_SCOPE_WORKGROUP

namespace {
constexpr int DM = 640, NH = 32, DH = 128, SK = 8192, WIN = 2048, FF = 1728;
constexpr int NTHR = 768;
constexpr int RD = 24;
constexpr int W0 = 3;
constexpr int W1 = 4;
constexpr int RTRIG = 16;
constexpr int WTRIG = 22;
constexpr bool PRETOUCH = false;
constexpr u32 CODE_BYTES = 18944u;
constexpr int NFU = 54;
constexpr int AUX_NT = 2, AUX_SC1 = 16, RSRC_W3 = 0x00020000;
constexpr int OFF_CNT = 0x800;
constexpr int OFF_XL = 0x1000;
constexpr int OFF_XQ = 0x10000;
constexpr int OFF_X1 = 0x90000;
constexpr int OFF_X2 = 0xB0000;
constexpr int X2_BLK = 33 * 128;
constexpr int OFF_X3 = 0x108000;
constexpr int X3_REP = 10240 + 128;
constexpr int OFF_X4 = 0x120000;
constexpr int X4_BLK = 55 * 128;
constexpr int WS_USED = 0x1B0000;
constexpr u32 SPIN_CAP = 1u << 16;
constexpr float LOG2E = 1.4426950408889634f;
constexpr int L_FLAG = 0;
constexpr int L_STAT = 64;
constexpr int L_INFO = 96;
constexpr int L_M = 128;
constexpr int L_FAC = 192;
constexpr int L_Q = 256;
constexpr int L_HH = 512;
constexpr int L_O = 1024;
constexpr int O_RS = 320;
constexpr int L_LN2 = 3328;
constexpr int LN2_RS = 1344;
constexpr int L_RED = 8704;
constexpr int L_W = L_RED + 8192;
constexpr int L_W2 = L_W + 2 * 640 * 64;
constexpr int L_BYTES = L_W2 + 32 * 1280;
enum { F_STAT = 0, F_QPART = 1, F_W13 = 2, F_Q = 3, F_KDONE = 5, F_PART = 6, F_WTRIG = 7, F_FAC = 8, F_RTRIG = 9, F_ROLE = 10, F_W2 = 11 };

struct Params {
  const u16 *x, *K, *V, *wq, *wo, *w1, *w3, *w2;
  u16* out;
  unsigned char* ws;
};

__device__ __forceinline__ float rb(float f) { return (float)(__bf16)f; }
__device__ __forceinline__ u32 pack2(float lo, float hi) { bf2 v = {(__bf16)lo, (__bf16)hi}; return __builtin_bit_cast(u32, v); }
__device__ __forceinline__ float blo(u32 v) { return __uint_as_float(v << 16); }
__device__ __forceinline__ float bhi(u32 v) { return __uint_as_float(v & 0xffff0000u); }
__device__ __forceinline__ float dot2(u32 a, u32 b, float c) {
  return __builtin_amdgcn_fdot2_f32_bf16(__builtin_bit_cast(bf2, a), __builtin_bit_cast(bf2, b), c, false);
}
__device__ __forceinline__ f4 mfma(v4u a, v4u b, f4 c) {
  return __builtin_amdgcn_mfma_f32_16x16x32_bf16(__builtin_bit_cast(bf8, a), __builtin_bit_cast(bf8, b), c, 0, 0, 0);
}
template <int CTRL> __device__ __forceinline__ u32 dppu(u32 x) { return (u32)__builtin_amdgcn_mov_dpp((int)x, CTRL, 0xf, 0xf, true); }
template <int CTRL> __device__ __forceinline__ float dppf(float x) { return __uint_as_float(dppu<CTRL>(__float_as_uint(x))); }
constexpr int XOR1 = 0xB1, XOR2 = 0x4E, XOR7 = 0x141, XOR8 = 0x128, XOR15 = 0x140;
__device__ __forceinline__ float xrow_sum(float x) {
  auto s = __builtin_amdgcn_permlane16_swap(__float_as_uint(x), __float_as_uint(x), false, false);
  x = __uint_as_float(s[0]) + __uint_as_float(s[1]);
  auto t = __builtin_amdgcn_permlane32_swap(__float_as_uint(x), __float_as_uint(x), false, false);
  return __uint_as_float(t[0]) + __uint_as_float(t[1]);
}
__device__ __forceinline__ float xhalf_sum(float x) {
  auto t = __builtin_amdgcn_permlane32_swap(__float_as_uint(x), __float_as_uint(x), false, false);
  return __uint_as_float(t[0]) + __uint_as_float(t[1]);
}
__device__ __forceinline__ float xrow_max(float x) {
  auto s = __builtin_amdgcn_permlane16_swap(__float_as_uint(x), __float_as_uint(x), false, false);
  x = fmaxf(__uint_as_float(s[0]), __uint_as_float(s[1]));
  auto t = __builtin_amdgcn_permlane32_swap(__float_as_uint(x), __float_as_uint(x), false, false);
  return fmaxf(__uint_as_float(t[0]), __uint_as_float(t[1]));
}
__device__ __forceinline__ float wave_sum(float x) {
  x += dppf<XOR1>(x); x += dppf<XOR2>(x); x += dppf<XOR7>(x); x += dppf<XOR15>(x);
  return xrow_sum(x);
}
__device__ __forceinline__ float wave_max(float x) {
  x = fmaxf(x, dppf<XOR1>(x)); x = fmaxf(x, dppf<XOR2>(x)); x = fmaxf(x, dppf<XOR7>(x)); x = fmaxf(x, dppf<XOR15>(x));
  return xrow_max(x);
}
__device__ __forceinline__ float sum8p(float x) { x += dppf<XOR8>(x); return xrow_sum(x); }
__device__ __forceinline__ float first_lane(float x) { return __uint_as_float(__builtin_amdgcn_readfirstlane(__float_as_uint(x))); }

__device__ __forceinline__ __amdgpu_buffer_rsrc_t mkrsrc(const void* p, int bytes) { return __builtin_amdgcn_make_buffer_rsrc((void*)p, 0, bytes, RSRC_W3); }
__device__ __forceinline__ v4u ld_nt(__amdgpu_buffer_rsrc_t r, int voff, int soff) { return __builtin_amdgcn_raw_buffer_load_b128(r, voff, soff, AUX_NT); }
__device__ __forceinline__ void dma_nt(__amdgpu_buffer_rsrc_t r, LAS unsigned char* dst, int voff, int soff) {
  __builtin_amdgcn_raw_ptr_buffer_load_lds(r, (LAS void*)dst, 16, voff, soff, 0, AUX_NT);
}
__device__ __forceinline__ v4u ld_poll(__amdgpu_buffer_rsrc_t r, int voff) { return __builtin_amdgcn_raw_buffer_load_b128(r, voff, 0, AUX_SC1); }
__device__ __forceinline__ v4u ld_poll_nt(__amdgpu_buffer_rsrc_t r, int voff) { return __builtin_amdgcn_raw_buffer_load_b128(r, voff, 0, AUX_SC1 | AUX_NT); }
__device__ __forceinline__ void st_sc1(__amdgpu_buffer_rsrc_t r, int voff, v4u v) { __builtin_amdgcn_raw_buffer_store_b128(v, r, voff, 0, AUX_SC1); }
__device__ __forceinline__ u32 enc1(float a, u32 tag) { return ((__float_as_uint(a) + 0x200u) & ~0x3ffu) | tag; }
__device__ __forceinline__ v4u enc4(f4 a, u32 tag) { return v4u{enc1(a.x, tag), enc1(a.y, tag), enc1(a.z, tag), enc1(a.w, tag)}; }
__device__ __forceinline__ u32 bad4(v4u g, u32 tag) { return ((g.x ^ tag) | (g.y ^ tag)) | ((g.z ^ tag) | (g.w ^ tag)); }
__device__ __forceinline__ bool all_ok(u32 bad) { return __all((bad & 0x3ffu) == 0u); }
__device__ __forceinline__ float dec(u32 w) { return __uint_as_float(w & ~0x3ffu); }
__device__ __forceinline__ f4 dec4(v4u g) { return f4{dec(g.x), dec(g.y), dec(g.z), dec(g.w)}; }

__device__ __forceinline__ void ln_finish(float pivot, float s1, float s2, float& mb, float& ri) {
  const float m1 = wave_sum(s1) * (1.0f / DM), m2 = wave_sum(s2) * (1.0f / DM);
  const float var = fmaxf(m2 - m1 * m1, 0.f);
  mb = rb(pivot + m1);
  ri = __builtin_amdgcn_rcpf(rb(__builtin_amdgcn_sqrtf(rb(rb(var) + 1e-5f))));
}
__device__ __forceinline__ u32 mksel(int j, int k, bool act) {
  const u32 s = (j & 1) ? 0x01000c0cu : 0x0c0c0100u;
  return (act && (j >> 1) == k) ? s : 0x0c0c0c0cu;
}
__device__ __forceinline__ v4u sparse(u32 x, const u32 (&sel)[4]) {
  return v4u{__builtin_amdgcn_perm(x, x, sel[0]), __builtin_amdgcn_perm(x, x, sel[1]), __builtin_amdgcn_perm(x, x, sel[2]), __builtin_amdgcn_perm(x, x, sel[3])};
}
__device__ __forceinline__ v4u trfrag(LAS unsigned char* a0, int step4) {
  const v2u lo = __builtin_bit_cast(v2u, __builtin_amdgcn_ds_read_tr16_b64_v4i16((LAS s4*)a0));
  const v2u hi = __builtin_bit_cast(v2u, __builtin_amdgcn_ds_read_tr16_b64_v4i16((LAS s4*)(a0 + step4)));
  return v4u{lo.x, lo.y, hi.x, hi.y};
}
__device__ __forceinline__ void lds_arrive(LAS u32* cnt, int lane) {
  asm volatile("s_waitcnt lgkmcnt(0)" ::: "memory");
  if (lane == 0) __hip_atomic_fetch_add(cnt, 1u, RLX_WG);
}
template <int SL> __device__ __forceinline__ void lds_wait(LAS u32* cnt, u32 n) {
#pragma nounroll
  for (u32 spin = 0; spin < (1u << 19); ++spin) { if (__hip_atomic_load(cnt, RLX_WG) >= n) break; __builtin_amdgcn_s_sleep(SL); }
  asm volatile("" ::: "memory");
}
#define SB() __builtin_amdgcn_sched_barrier(0)
#define MEMCLOB() asm volatile("" ::: "memory")
template <int N> __device__ __forceinline__ void wait_vm() { asm volatile("s_waitcnt vmcnt(%0)" ::"n"(N) : "memory"); }

constexpr int issued(int n) { return n + 1 + W1 < RD ? RD : n + 1 + W1 > 64 ? 64 : n + 1 + W1; }
__device__ __forceinline__ int ffn_unit(int rk, int grp) { return rk < 6 ? 2 * (grp + 8 * (rk >> 1)) + (rk & 1) : rk == 6 ? 48 + grp : NFU; }
}

extern "C" __global__ void __launch_bounds__(NTHR, 3) fused_block(Params p) {
  __shared__ __attribute__((aligned(16))) unsigned char smem[L_BYTES];
  LAS unsigned char* L = (LAS unsigned char*)smem;
  const int tid = threadIdx.x, lane = tid & 63, wid = __builtin_amdgcn_readfirstlane(tid >> 6);
  const int bid = blockIdx.x, grp = bid & 7;
  const int r = lane >> 4, c = lane & 15;
  asm volatile("" :: "s"(p.x), "s"(p.K), "s"(p.V), "s"(p.wq), "s"(p.wo), "s"(p.w1), "s"(p.w3), "s"(p.w2), "s"(p.out), "s"(p.ws));
  const int h = (bid & 7) + 8 * (bid >> 6), bs = (bid >> 3) & 7, b = bs >> 1, s = bs & 1;
  LAS u32* flags = (LAS u32*)(L + L_FLAG);
  LAS float* mbuf = (LAS float*)(L + L_M);
  LAS float* lbuf = mbuf + 8;
  const __amdgpu_buffer_rsrc_t wsr = mkrsrc(p.ws, WS_USED);
  const int t = wid & 3;

  const size_t kvoff = ((size_t)(b * NH + h) * SK + (SK - WIN) + s * 1024) * DH;
  const __amdgpu_buffer_rsrc_t kr = mkrsrc(p.K + kvoff, 1024 * DH * 2), vr = mkrsrc(p.V + kvoff, 1024 * DH * 2);
  const int kvo = wid * 1024 + lane * 16;
  v4u ring[RD];
  u32 xr[5];
  v4u wqv[5];
  u64 epoch = 0;
  if (wid < 8) {
#pragma unroll
    for (int n = 0; n < W0; ++n) ring[n] = ld_nt(kr, kvo, n * 8192);
  } else {
#pragma unroll
    for (int k = 0; k < 5; ++k) xr[k] = ((const GAS u32*)p.x)[t * 320 + lane + 64 * k];
    const __amdgpu_buffer_rsrc_t qr = mkrsrc(p.wq + h * DH, DM * NH * DH * 2 - h * DH * 2);
#pragma unroll
    for (int i = 0; i < 5; ++i) wqv[i] = ld_nt(qr, (80 * bs + 20 * t + r) * (NH * DH * 2) + c * 16, i * 4 * (NH * DH * 2));
    epoch = __hip_atomic_load((GAS u64*)p.ws, RLX_AGENT);
    if (wid == 8 && lane < 16) flags[lane] = 0u;
  }
  SB();
  __syncthreads();

  if (__builtin_expect(wid >= 8, 1)) {
    u32 pfv = 0;
    {
      u64 pc; u32 off;
      asm volatile("s_getpc_b64 %0\n.Lpf_here%=:\n\ts_mov_b32 %1, .Lpf_here%=-fused_block" : "=s"(pc), "=s"(off));
      if (bid < 8) {
        const u32 o = (u32)t * 8192u + lane * 128u;
        if (o < CODE_BYTES) pfv = *(const GAS u32*)(pc - off + o);
        if (PRETOUCH && t == 3) {
          const GAS unsigned char* vb = (const GAS unsigned char*)p.V + (size_t)(SK - WIN) * DH * 2;
          pfv = __builtin_nontemporal_load((const GAS u32*)(vb + (size_t)lane * (SK * DH * 2))) ^ __builtin_nontemporal_load((const GAS u32*)(vb + (size_t)(lane + 64) * (SK * DH * 2)));
          const GAS unsigned char* wb = lane < 5 ? (const GAS unsigned char*)p.wo + lane * (1 << 20) : lane < 7 ? (const GAS unsigned char*)p.w1 + (lane - 5) * (1 << 20) + 65536
                                      : lane < 9 ? (const GAS unsigned char*)p.w3 + (lane - 7) * (1 << 20) + 65536 : lane < 11 ? (const GAS unsigned char*)p.w2 + (lane - 9) * (1 << 20) + 65536 : (const GAS unsigned char*)p.out;
          if (lane < 12) pfv ^= __builtin_nontemporal_load((const GAS u32*)wb);
        }
      }
    }
    u32 tag, t16;
    {
      const int bsel = (lane >> 3) & 1;
      LAS u32* xw = (LAS u32*)(L + L_LN2 + t * LN2_RS);
#pragma unroll
      for (int k = 0; k < 5; ++k) xw[lane + 64 * k] = xr[k];
      const float pivot = first_lane(blo(xr[0]));
      float s1 = 0.f, s2 = 0.f;
#pragma unroll
      for (int k = 0; k < 5; ++k) { const float d0 = blo(xr[k]) - pivot, d1 = bhi(xr[k]) - pivot; s1 += d0 + d1; s2 = fmaf(d0, d0, fmaf(d1, d1, s2)); }
      float mb, ri;
      ln_finish(pivot, s1, s2, mb, ri);
      if (lane == 0) *(LAS f2*)(L + L_STAT + t * 8) = f2{mb, ri};
      lds_arrive(flags + F_STAT, lane);
      u32 sel[4];
#pragma unroll
      for (int k = 0; k < 4; ++k) sel[k] = mksel(lane & 7, k, true);
      lds_wait<0>(flags + F_STAT, 4);
      f2 st[2];
      st[0] = *(LAS f2*)(L + L_STAT + bsel * 8); st[1] = *(LAS f2*)(L + L_STAT + (2 + bsel) * 8);
      u32 xe[5][2];
#pragma unroll
      for (int i = 0; i < 5; ++i)
#pragma unroll
        for (int P = 0; P < 2; ++P) xe[i][P] = *(LAS u16*)(L + L_LN2 + (2 * P + bsel) * LN2_RS + (80 * bs + 20 * t + r + 4 * i) * 2);
      f4 acc[2] = {{0.f, 0.f, 0.f, 0.f}, {0.f, 0.f, 0.f, 0.f}};
#pragma unroll
      for (int i = 0; i < 5; ++i)
#pragma unroll
        for (int P = 0; P < 2; ++P) acc[P] = mfma(sparse(pack2(rb(blo(xe[i][P]) - st[P].x) * st[P].y, 0.f), sel), wqv[i], acc[P]);
#pragma unroll
      for (int P = 0; P < 2; ++P) *(LAS f4*)(L + L_RED + t * 2048 + P * 1024 + lane * 16) = acc[P];
      lds_arrive(flags + F_QPART, lane);
      tag = __builtin_amdgcn_readfirstlane(((u32)epoch + 1u) & 0x3ffu);
      t16 = __builtin_amdgcn_readfirstlane(((u32)epoch + 1u) & 0xffffu);
      lds_wait<0>(flags + F_QPART, 4);
      if (lane < 32) {
        f4 sum = {0.f, 0.f, 0.f, 0.f};
#pragma unroll
        for (int ww = 0; ww < 4; ++ww) sum += *(LAS f4*)(L + L_RED + ww * 2048 + (t >> 1) * 1024 + (16 * (2 * (t & 1) + (lane & 1)) + (lane >> 1)) * 16);
        st_sc1(wsr, OFF_XQ + ((h * 4 + t) * 8 + bs) * 512 + lane * 16, enc4(sum, tag));
      }
    }
    if (bid < 8 && t == 1 && lane == 0) __hip_atomic_store((GAS u32*)(p.ws + OFF_CNT + bid * 128), t16 << 16, RLX_AGENT);
    if (t == 0) {
      __builtin_amdgcn_s_sleep(16);
      v4u g[4];
      const int go = OFF_XQ + (h * 4 + b) * 4096 + lane * 16;
#pragma nounroll
      for (u32 spin = 0;; ++spin) {
        MEMCLOB();
        u32 bad = 0;
#pragma unroll
        for (int k = 0; k < 4; ++k) g[k] = ld_poll_nt(wsr, go + k * 1024);
#pragma unroll
        for (int k = 0; k < 4; ++k) bad |= bad4(g[k], tag);
        if (all_ok(bad) || spin > SPIN_CAP) break;
        __builtin_amdgcn_s_sleep(4);
      }
      const f4 sum = (dec4(g[0]) + dec4(g[1])) + (dec4(g[2]) + dec4(g[3]));
      const float q0 = xhalf_sum(sum.x), q1 = xhalf_sum(sum.y), q2 = xhalf_sum(sum.z), q3 = xhalf_sum(sum.w);
      if (lane < 32) *(LAS v2u*)(L + L_Q + lane * 8) = v2u{pack2(q0, q1), pack2(q2, q3)};
      lds_arrive(flags + F_Q, lane);
      lds_wait<16>(flags + F_KDONE, 8);
      const float mw = mbuf[lane & 7], lw = lbuf[lane & 7];
      float M = fmaxf(mw, dppf<XOR1>(mw)); M = fmaxf(M, dppf<XOR2>(M)); M = fmaxf(M, dppf<XOR7>(M));
      float Ls = lw * __builtin_amdgcn_exp2f((mw - M) * LOG2E); Ls += dppf<XOR1>(Ls); Ls += dppf<XOR2>(Ls); Ls += dppf<XOR7>(Ls);
      Ls = first_lane(Ls);
      const v4u mine = enc4(f4{M, Ls, M, Ls}, tag);
      if (lane == 0) st_sc1(wsr, OFF_XL + (h * 8 + bs) * 128, mine);
      v4u gl;
#pragma nounroll
      for (u32 spin = 0;; ++spin) {
        MEMCLOB();
        gl = ld_poll_nt(wsr, OFF_XL + (h * 8 + (bs ^ 1)) * 128);
        if (all_ok(bad4(gl, tag)) || spin > SPIN_CAP) break;
        __builtin_amdgcn_s_sleep(16);
      }
      const float M0 = dec(mine.x), L0 = dec(mine.y), M1 = dec(gl.x), L1 = dec(gl.y), Mx = fmaxf(M0, M1);
      const float e0 = L0 * __builtin_amdgcn_exp2f((M0 - Mx) * LOG2E), e1 = L1 * __builtin_amdgcn_exp2f((M1 - Mx) * LOG2E);
      if (lane < 8) *(LAS float*)(L + L_FAC + lane * 4) = __builtin_amdgcn_exp2f((mw - Mx) * LOG2E) * __builtin_amdgcn_rcpf(e0 + e1);
      if (lane == 0) *(LAS u32*)(L + L_INFO) = tag;
      lds_arrive(flags + F_FAC, lane);
    }
    if (t == 1) {
#pragma nounroll
      for (u32 spin = 0; spin <= SPIN_CAP; ++spin) {
        MEMCLOB();
        const u32 v = __builtin_amdgcn_raw_buffer_load_b32(wsr, OFF_CNT + grp * 128, 0, AUX_SC1 | AUX_NT);
        if (__builtin_amdgcn_readfirstlane(v >> 16) == t16) break;
        __builtin_amdgcn_s_sleep(16);
      }
      lds_wait<8>(flags + F_RTRIG, 8);
      u32 old = 0;
      if (lane == 0) old = __hip_atomic_fetch_add((GAS u32*)(p.ws + OFF_CNT + grp * 128), 1u, RLX_AGENT);
      if (lane == 0) *(LAS u32*)(L + L_INFO + 8) = old & 31u;
      lds_arrive(flags + F_ROLE, lane);
    }
    asm volatile("" :: "v"(pfv));
    lds_wait<8>(flags + F_ROLE, 1);
    const int rk = __builtin_amdgcn_readfirstlane(*(LAS u32*)(L + L_INFO + 8));
    const int u = ffn_unit(rk, grp);
    const bool ffn = u < NFU, oproj = rk >= 12;
    const int uo = grp * 20 + rk - 12, hh_o = uo / 5, j_o = uo - 5 * hh_o;
    const int task = grp * 10 + rk - 12, cs = task >> 2, sb = task & 3;
    const bool owner = rk >= 12 && rk < 22 && t == 0;
    const int q4 = c >> 2, p4 = c & 3, sw2 = 2 * (q4 + 4 * (r & 1));
    v4u bw[20];
    if (oproj || ffn) {
      lds_wait<8>(flags + F_WTRIG, 8);
      if (oproj) {
        const __amdgpu_buffer_rsrc_t orr = mkrsrc(p.wo + (size_t)hh_o * DH * DM + 128 * j_o, DH * DM * 2 - 256 * j_o);
        const int vo0 = (32 * t + r) * (DM * 2) + (c ^ (2 * r)) * 16, vo1 = (32 * t + r) * (DM * 2) + (c ^ (2 * r + 8)) * 16;
#pragma unroll
        for (int i = 0; i < 8; ++i) dma_nt(orr, L + L_W + (32 * t + 4 * i) * 256, (i & 2) ? vo1 : vo0, i * 4 * DM * 2);
      } else {
        const __amdgpu_buffer_rsrc_t w1r = mkrsrc(p.w1 + 32 * u, DM * FF * 2 - 64 * u), w3r = mkrsrc(p.w3 + 32 * u, DM * FF * 2 - 64 * u);
        const int rw = lane >> 2, c4 = lane & 3, gp = (rw >> 3) & 1;
        const int vo1 = (160 * t + rw) * (FF * 2) + (c4 ^ gp) * 16, vo3 = (160 * t + rw) * (FF * 2) + (c4 ^ (2 + gp)) * 16;
#pragma unroll
        for (int i = 0; i < 10; ++i) dma_nt(w1r, L + L_W + (160 * t + 16 * i) * 64, vo1, i * 16 * FF * 2);
#pragma unroll
        for (int i = 0; i < 10; ++i) dma_nt(w3r, L + L_W + 640 * 64 + (160 * t + 16 * i) * 64, vo3, i * 16 * FF * 2);
        wait_vm<0>();
        lds_arrive(flags + F_W13, lane);
        lds_wait<0>(flags + F_W13, 4);
#pragma unroll
        for (int ks = 0; ks < 20; ++ks) bw[ks] = trfrag(L + L_W + (p4 >> 1) * (640 * 64) + (32 * ks + 8 * r + q4) * 64 + ((t ^ (2 * (p4 >> 1) + (r & 1))) * 16) + (p4 & 1) * 8, 4 * 64);
      }
    }
    const int cb = c & 3, cr = c >> 2;
    if (oproj) {
      v4u g;
      const int go = OFF_X1 + (hh_o * 8 + 2 * t) * 512 + lane * 16;
#pragma nounroll
      for (u32 spin = 0;; ++spin) {
        MEMCLOB();
        g = ld_poll(wsr, go);
        if (all_ok(bad4(g, tag)) || spin > SPIN_CAP) break;
      }
      const f4 og = dec4(g);
      const float o0 = xhalf_sum(og.x), o1 = xhalf_sum(og.y), o2 = xhalf_sum(og.z), o3 = xhalf_sum(og.w);
      if (lane < 32) *(LAS v2u*)(L + L_O + t * O_RS + lane * 8) = v2u{pack2(o0, o1), pack2(o2, o3)};
      wait_vm<0>();
      __syncthreads();
      f4 d0 = {0.f, 0.f, 0.f, 0.f}, d1 = {0.f, 0.f, 0.f, 0.f};
#pragma unroll
      for (int ks = 0; ks < 4; ++ks) {
        const v4u a = *(LAS v4u*)(L + L_O + cb * O_RS + ks * 64 + r * 16);
        d0 = mfma(trfrag(L + L_W + (32 * ks + 8 * r + q4) * 256 + (((4 * t + (p4 >> 1)) ^ sw2) * 16) + (p4 & 1) * 8, 4 * 256), a, d0);
        d1 = mfma(trfrag(L + L_W + (32 * ks + 8 * r + q4) * 256 + (((4 * t + 2 + (p4 >> 1)) ^ sw2) * 16) + (p4 & 1) * 8, 4 * 256), a, d1);
      }
      const f4 y = (cr & 1) ? d1 : d0;
      if (cr < 2) st_sc1(wsr, OFF_X2 + ((4 * j_o + t) * 4 + cb) * X2_BLK + hh_o * 128 + cr * 64 + r * 16, enc4(y, tag));
    } else if (ffn) {
      v4u g[3];
      const int go = OFF_X3 + grp * X3_REP + t * 2560 + lane * 16;
      const bool has2 = lane < 32;
#pragma nounroll
      for (u32 spin = 0;; ++spin) {
        MEMCLOB();
        g[0] = ld_poll(wsr, go); g[1] = ld_poll(wsr, go + 1024); g[2] = ld_poll(wsr, go + (has2 ? 2048 : 0));
        if (all_ok(bad4(g[0], tag) | bad4(g[1], tag) | bad4(g[2], tag)) || spin > SPIN_CAP) break;
      }
      const float pivot = first_lane(dec(g[0].x));
      float s1 = 0.f, s2 = 0.f;
#pragma unroll
      for (int k = 0; k < 3; ++k)
#pragma unroll
        for (int e = 0; e < 4; ++e) { const float d = (k < 2 || has2) ? dec(g[k][e]) - pivot : 0.f; s1 += d; s2 = fmaf(d, d, s2); }
      float mb, ri;
      ln_finish(pivot, s1, s2, mb, ri);
      LAS unsigned char* lp = L + L_LN2 + t * LN2_RS + lane * 8;
#pragma unroll
      for (int k = 0; k < 3; ++k)
        if (k < 2 || has2) *(LAS v2u*)(lp + 512 * k) = v2u{pack2(rb(dec(g[k].x) - mb) * ri, rb(dec(g[k].y) - mb) * ri), pack2(rb(dec(g[k].z) - mb) * ri, rb(dec(g[k].w) - mb) * ri)};
      __syncthreads();
      f4 da = {0.f, 0.f, 0.f, 0.f}, db = {0.f, 0.f, 0.f, 0.f};
#pragma unroll
      for (int ks = 0; ks < 20; ks += 2) {
        da = mfma(bw[ks], *(LAS v4u*)(L + L_LN2 + cb * LN2_RS + ks * 64 + r * 16), da);
        db = mfma(bw[ks + 1], *(LAS v4u*)(L + L_LN2 + cb * LN2_RS + ks * 64 + 64 + r * 16), db);
      }
      const f4 d = da + db;
      const float pre = rb(cr == 0 ? d.x : cr == 1 ? d.y : cr == 2 ? d.z : d.w);
      auto sw = __builtin_amdgcn_permlane32_swap(__float_as_uint(pre), __float_as_uint(pre), false, false);
      const float gs = __uint_as_float(sw[0]), us = __uint_as_float(sw[1]);
      const float si = rb(gs * __builtin_amdgcn_rcpf(1.0f + __builtin_amdgcn_exp2f(-gs * LOG2E)));
      if (lane < 32) *(LAS u16*)(L + L_HH + cb * 64 + (8 * t + 4 * r + cr) * 2) = (u16)(pack2(si * us, 0.f) & 0xffffu);
      __syncthreads();
    }
    if (owner) {
      const int pp = lane >> 3, q = lane & 7;
      const v2u xv = *(LAS v2u*)(L + L_LN2 + sb * LN2_RS + (32 * cs + 4 * q) * 2);
      f4 hv;
      {
        v4u g[4];
        const int go = OFF_X2 + (cs * 4 + sb) * X2_BLK + pp * 128 + q * 16;
#pragma nounroll
        for (u32 spin = 0;; ++spin) {
          MEMCLOB();
#pragma unroll
          for (int m = 0; m < 4; ++m) g[m] = ld_poll(wsr, go + m * 1024);
          if (all_ok((bad4(g[0], tag) | bad4(g[1], tag)) | (bad4(g[2], tag) | bad4(g[3], tag))) || spin > SPIN_CAP) break;
        }
        const f4 y = (dec4(g[0]) + dec4(g[1])) + (dec4(g[2]) + dec4(g[3]));
        hv.x = rb(blo(xv.x) + rb(sum8p(y.x))); hv.y = rb(bhi(xv.x) + rb(sum8p(y.y)));
        hv.z = rb(blo(xv.y) + rb(sum8p(y.z))); hv.w = rb(bhi(xv.y) + rb(sum8p(y.w)));
      }
      st_sc1(wsr, OFF_X3 + pp * X3_REP + sb * 2560 + cs * 128 + q * 16, enc4(hv, tag));
      __builtin_amdgcn_s_sleep(40);
      const int go = OFF_X4 + (cs * 4 + sb) * X4_BLK + pp * 128 + q * 16;
      v4u g[7];
      const bool last = pp < NFU - 48;
#pragma nounroll
      for (u32 spin = 0;; ++spin) {
        MEMCLOB();
        g[0] = ld_poll(wsr, go);
        if (all_ok(bad4(g[0], tag)) || spin > SPIN_CAP) break;
      }
#pragma nounroll
      for (u32 spin = 0;; ++spin) {
        MEMCLOB();
#pragma unroll
        for (int m = 1; m < 6; ++m) g[m] = ld_poll_nt(wsr, go + m * 1024);
        g[6] = ld_poll_nt(wsr, go + (last ? 6144 : 1024));
        u32 bad = 0;
#pragma unroll
        for (int m = 1; m < 7; ++m) bad |= bad4(g[m], tag);
        if (all_ok(bad) || spin > SPIN_CAP) break;
      }
      f4 z = ((dec4(g[0]) + dec4(g[1])) + (dec4(g[2]) + dec4(g[3]))) + (dec4(g[4]) + dec4(g[5]));
      if (last) z += dec4(g[6]);
      const float o0 = hv.x + rb(sum8p(z.x)), o1 = hv.y + rb(sum8p(z.y)), o2 = hv.z + rb(sum8p(z.z)), o3 = hv.w + rb(sum8p(z.w));
      if (lane < 8) ((GAS v2u*)p.out)[sb * 160 + 8 * cs + q] = v2u{pack2(o0, o1), pack2(o2, o3)};
      if (task == 0 && lane == 0) __hip_atomic_store((GAS u64*)p.ws, epoch + 1, RLX_AGENT);
    }
    return;
  }

  const int w = wid;
#pragma unroll
  for (int n = W0; n < RD; ++n) { wait_vm<W0 - 1>(); ring[n] = ld_nt(kr, kvo, n * 8192); SB(); }
  u32 selA[4];
#pragma unroll
  for (int k = 0; k < 4; ++k) selA[k] = mksel(c & 7, k, c < 8);
  lds_wait<1>(flags + F_Q, 1);
  const v4u qv = *(LAS v4u*)(L + L_Q + c * 16);
#define REFILL(n) { _Pragma("unroll") for (int m_ = issued((n) - 1); m_ < issued(n); ++m_) { if (m_ < 32) ring[m_ % RD] = ld_nt(kr, kvo, m_ * 8192); else ring[m_ % RD] = ld_nt(vr, kvo, (m_ - 32) * 8192); } SB(); }
  float sc[2];
  float x[16];
#define QK_STEP(n) { const v4u kk = ring[(n) % RD]; x[(n) & 15] = dot2(kk.w, qv.w, dot2(kk.z, qv.z, dot2(kk.y, qv.y, dot2(kk.x, qv.x, 0.f)))); REFILL(n) }
#define QK4(n) QK_STEP(n) QK_STEP((n) + 1) QK_STEP((n) + 2) QK_STEP((n) + 3)
#define TR_STEP(HALF, CTRL, BIT) _Pragma("unroll") for (int hh = 0; hh < (HALF); ++hh) { \
    const float lo_ = x[hh], hi_ = x[hh + (HALF)]; const float t1_ = lo_ + dppf<CTRL>(lo_), t2_ = hi_ + dppf<CTRL>(hi_); x[hh] = (c & (BIT)) ? t2_ : t1_; }
#define TR_ALL() TR_STEP(8, XOR8, 8) TR_STEP(4, XOR7, 4) TR_STEP(2, XOR2, 2) TR_STEP(1, XOR1, 1)
  QK4(0) QK4(4) QK4(8) QK4(12)
  TR_ALL()
  sc[0] = rb(rb(x[0]) * 0.08838834764831845f);
  QK4(16) QK4(20) QK4(24) QK4(28)
  TR_ALL()
  sc[1] = rb(rb(x[0]) * 0.08838834764831845f);
  const float mw = wave_max(fmaxf(sc[0], sc[1]));
  const float pe0 = __builtin_amdgcn_exp2f((sc[0] - mw) * LOG2E), pe1 = __builtin_amdgcn_exp2f((sc[1] - mw) * LOG2E);
  { const float lw = wave_sum(pe0 + pe1); if (lane == 0) { mbuf[w] = mw; lbuf[w] = lw; } }
  lds_arrive(flags + F_KDONE, lane);
  u32 pb[2];
  pb[0] = pack2(pe0, 0.f); pb[1] = pack2(pe1, 0.f);
  f4 acc = {0.f, 0.f, 0.f, 0.f};
#define PV_STEP(i) { const v4u vv = ring[(32 + (i)) % RD]; \
    const u32 xx = (u32)__builtin_amdgcn_update_dpp(0, (int)pb[(i) >> 4], 0x150 + ((i) & 15), 0xf, 0xf, false);     \
    acc = mfma(sparse(xx, selA), vv, acc); REFILL(32 + (i)) \
    if ((i) == WTRIG && lane == 0) __hip_atomic_fetch_add(flags + F_WTRIG, 1u, RLX_WG); \
    if ((i) == RTRIG && lane == 0) __hip_atomic_fetch_add(flags + F_RTRIG, 1u, RLX_WG); }
#define PV4(i) PV_STEP(i) PV_STEP((i) + 1) PV_STEP((i) + 2) PV_STEP((i) + 3)
  PV4(0) PV4(4) PV4(8) PV4(12) PV4(16) PV4(20) PV4(24)
  MEMCLOB();
  const u32 iflag = __hip_atomic_load(flags + F_FAC, RLX_WG);
  float fw = *(volatile LAS float*)(L + L_FAC + w * 4);
  u32 stag = *(volatile LAS u32*)(L + L_INFO);
  PV4(28)
  if (__builtin_amdgcn_readfirstlane(iflag) == 0u) { lds_wait<0>(flags + F_FAC, 1); fw = *(volatile LAS float*)(L + L_FAC + w * 4); stag = *(volatile LAS u32*)(L + L_INFO); }
  acc *= fw;
  if (lane < 32) *(LAS f4*)(L + L_RED + w * 512 + (8 * c + 4 * r) * 4) = acc;
  asm volatile("s_waitcnt lgkmcnt(0)" ::: "memory");
  u32 old = 0;
  if (lane == 0) old = __hip_atomic_fetch_add(flags + F_PART, 1u, RLX_WG);
  if (__builtin_amdgcn_readfirstlane(old) == 7u) {
    MEMCLOB();
    if (lane < 32) {
      f4 sum = {0.f, 0.f, 0.f, 0.f};
#pragma unroll
      for (int ww = 0; ww < 8; ++ww) sum += *(LAS f4*)(L + L_RED + ww * 512 + lane * 16);
      st_sc1(wsr, OFF_X1 + (h * 8 + bs) * 512 + lane * 16, enc4(sum, stag));
    }
  }
  if (w < 4) {
    lds_wait<8>(flags + F_ROLE, 1);
    const int u = ffn_unit(__builtin_amdgcn_readfirstlane(*(LAS u32*)(L + L_INFO + 8)), grp);
    if (u < NFU) {
      const int q4 = c >> 2, p4 = c & 3, cb = c & 3, cr = c >> 2;
      const __amdgpu_buffer_rsrc_t w2r = mkrsrc(p.w2 + (size_t)32 * u * DM, 32 * DM * 2);
      const int vo2 = r * (DM * 2) + (c ^ (2 * (r + 4 * (w & 1)))) * 16;
#pragma unroll
      for (int i = 0; i < 10; ++i) dma_nt(w2r, L + L_W2 + (10 * w + i) * 1024, vo2, w * (8 * DM * 2) + (i / 5) * 4 * DM * 2 + (i % 5) * 256);
      wait_vm<0>();
      lds_arrive(flags + F_W2, lane);
      lds_wait<1>(flags + F_W2, 4);
      const int sw2 = 2 * (q4 + 4 * (r & 1));
      v4u bd[10];
#pragma unroll
      for (int T = 0; T < 10; ++T) { const int col = 160 * w + 16 * T + 4 * p4; bd[T] = trfrag(L + L_W2 + ((2 * r) * 5 + (col >> 7)) * 1024 + q4 * 256 + ((((col & 127) >> 3) ^ sw2) * 16) + (p4 & 1) * 8, 5 * 1024); }
      asm volatile("" :: "v"(bd[0]), "v"(bd[1]), "v"(bd[2]), "v"(bd[3]), "v"(bd[4]), "v"(bd[5]), "v"(bd[6]), "v"(bd[7]), "v"(bd[8]), "v"(bd[9]));
      __syncthreads();
      __syncthreads();
      const v4u a = *(LAS v4u*)(L + L_HH + cb * 64 + r * 16);
      const int zo = OFF_X4 + cb * X4_BLK + u * 128 + (cr & 1) * 64 + r * 16;
      const f4 zero = {0.f, 0.f, 0.f, 0.f};
      v4u am[4];
#pragma unroll
      for (int k = 0; k < 4; ++k) am[k] = cr == k ? a : v4u{0u, 0u, 0u, 0u};
      f4 z0 = mfma(bd[0], am[0], zero), z1 = mfma(bd[4], am[0], zero), z2 = mfma(bd[8], am[0], zero);
      z0 = mfma(bd[1], am[1], z0); z1 = mfma(bd[5], am[1], z1); z2 = mfma(bd[9], am[1], z2);
      z0 = mfma(bd[2], am[2], z0); z1 = mfma(bd[6], am[2], z1);
      z0 = mfma(bd[3], am[3], z0); z1 = mfma(bd[7], am[3], z1);
      if (cr < 2) st_sc1(wsr, zo + (5 * w + 4) * 4 * X4_BLK, enc4(z2, stag));
      st_sc1(wsr, zo + (5 * w + (cr >> 1)) * 4 * X4_BLK, enc4(z0, stag));
      st_sc1(wsr, zo + (5 * w + 2 + (cr >> 1)) * 4 * X4_BLK, enc4(z1, stag));
    }
  }
}

extern "C" void kernel_launch(void* const* d_in, const int* in_sizes, int n_in, void* d_out, int out_size, void* d_ws, size_t ws_size, hipStream_t stream) {
  Params p;
  p.x = (const u16*)d_in[0]; p.K = (const u16*)d_in[1]; p.V = (const u16*)d_in[2]; p.wq = (const u16*)d_in[3]; p.wo = (const u16*)d_in[4];
  p.w1 = (const u16*)d_in[6]; p.w3 = (const u16*)d_in[7]; p.w2 = (const u16*)d_in[8];
  p.out = (u16*)d_out; p.ws = (unsigned char*)d_ws;
  hipLaunchKernelGGL(fused_block, dim3(256), dim3(NTHR), 0, stream, p);
}
```

```cpp
#define PROBE 0
#include <hip/hip_runtime.h>
#include <stdint.h>

typedef unsigned short u16;
typedef unsigned u32;
typedef unsigned long long u64;
typedef u32 v4u __attribute__((ext_vector_type(4)));
typedef u32 v2u __attribute__((ext_vector_type(2)));
typedef float f4 __attribute__((ext_vector_type(4)));
typedef float f2 __attribute__((ext_vector_type(2)));
typedef short s4 __attribute__((ext_vector_type(4)));
typedef __bf16 bf2 __attribute__((ext_vector_type(2)));
typedef __bf16 bf8 __attribute__((ext_vector_type(8)));
#define GAS __attribute__((address_space(1)))
#define LAS __attribute__((address_space(3)))
#define RLX_AGENT __ATOMIC_RELAXED, __HIP_MEMORY_SCOPE_AGENT
#define RLX_WG __ATOMIC_RELAXED, __HIP_MEMORY_SCOPE_WORKGROUP

namespace {
constexpr int DM = 640, NH = 32, DH = 128, SK = 8192, WIN = 2048, FF = 1728;
constexpr int NTHR = 768;
constexpr int RD = 24;
constexpr int W0 = 3;
constexpr int W1 = 4;
constexpr int RTRIG = 16;
constexpr int WTRIG = 22;
constexpr u32 CODE_BYTES = 18688u;
constexpr int NFU = 54;
constexpr int AUX_NT = 2, AUX_SC1 = 16, RSRC_W3 = 0x00020000;
constexpr int OFF_CNT = 0x800;
constexpr int OFF_XL = 0x1000;
constexpr int OFF_XQ = 0x10000;
constexpr int OFF_X1 = 0x90000;
constexpr int OFF_X2 = 0xB0000;
constexpr int X2_BLK = 33 * 128;
constexpr int OFF_X3 = 0x108000;
constexpr int X3_REP = 10240 + 128;
constexpr int OFF_X4 = 0x120000;
constexpr int X4_BLK = 55 * 128;
constexpr int WS_USED = 0x1B0000;
constexpr u32 SPIN_CAP = 1u << 16;
constexpr float LOG2E = 1.4426950408889634f;
constexpr int L_FLAG = 0;
constexpr int L_STAT = 64;
constexpr int L_INFO = 96;
constexpr int L_M = 128;
constexpr int L_FAC = 192;
constexpr int L_Q = 256;
constexpr int L_HH = 512;
constexpr int L_O = 1024;
constexpr int O_RS = 320;
constexpr int L_LN2 = 3328;
constexpr int LN2_RS = 1344;
constexpr int L_RED = 8704;
constexpr int L_W = L_RED + 8192;
constexpr int L_W2 = L_W + 2 * 640 * 64;
constexpr int L_BYTES = L_W2 + 32 * 1280;
enum { F_STAT = 0, F_QPART = 1, F_W13 = 2, F_Q = 3, F_KDONE = 5, F_PART = 6, F_WTRIG = 7, F_FAC = 8, F_RTRIG = 9, F_ROLE = 10, F_W2 = 11 };

struct Params {
  const u16 *x, *K, *V, *wq, *wo, *w1, *w3, *w2;
  u16* out;
  unsigned char* ws;
};

__device__ __forceinline__ float rb(float f) { return (float)(__bf16)f; }
__device__ __forceinline__ u32 pack2(float lo, float hi) { bf2 v = {(__bf16)lo, (__bf16)hi}; return __builtin_bit_cast(u32, v); }
__device__ __forceinline__ float blo(u32 v) { return __uint_as_float(v << 16); }
__device__ __forceinline__ float bhi(u32 v) { return __uint_as_float(v & 0xffff0000u); }
__device__ __forceinline__ float dot2(u32 a, u32 b, float c) {
  return __builtin_amdgcn_fdot2_f32_bf16(__builtin_bit_cast(bf2, a), __builtin_bit_cast(bf2, b), c, false);
}
__device__ __forceinline__ f4 mfma(v4u a, v4u b, f4 c) {
  return __builtin_amdgcn_mfma_f32_16x16x32_bf16(__builtin_bit_cast(bf8, a), __builtin_bit_cast(bf8, b), c, 0, 0, 0);
}
template <int CTRL> __device__ __forceinline__ u32 dppu(u32 x) { return (u32)__builtin_amdgcn_mov_dpp((int)x, CTRL, 0xf, 0xf, true); }
template <int CTRL> __device__ __forceinline__ float dppf(float x) { return __uint_as_float(dppu<CTRL>(__float_as_uint(x))); }
constexpr int XOR1 = 0xB1, XOR2 = 0x4E, XOR7 = 0x141, XOR8 = 0x128, XOR15 = 0x140;
__device__ __forceinline__ float xrow_sum(float x) {
  auto s = __builtin_amdgcn_permlane16_swap(__float_as_uint(x), __float_as_uint(x), false, false);
  x = __uint_as_float(s[0]) + __uint_as_float(s[1]);
  auto t = __builtin_amdgcn_permlane32_swap(__float_as_uint(x), __float_as_uint(x), false, false);
  return __uint_as_float(t[0]) + __uint_as_float(t[1]);
}
__device__ __forceinline__ float xhalf_sum(float x) {
  auto t = __builtin_amdgcn_permlane32_swap(__float_as_uint(x), __float_as_uint(x), false, false);
  return __uint_as_float(t[0]) + __uint_as_float(t[1]);
}
__device__ __forceinline__ float xrow_max(float x) {
  auto s = __builtin_amdgcn_permlane16_swap(__float_as_uint(x), __float_as_uint(x), false, false);
  x = fmaxf(__uint_as_float(s[0]), __uint_as_float(s[1]));
  auto t = __builtin_amdgcn_permlane32_swap(__float_as_uint(x), __float_as_uint(x), false, false);
  return fmaxf(__uint_as_float(t[0]), __uint_as_float(t[1]));
}
__device__ __forceinline__ float wave_sum(float x) {
  x += dppf<XOR1>(x); x += dppf<XOR2>(x); x += dppf<XOR7>(x); x += dppf<XOR15>(x);
  return xrow_sum(x);
}
__device__ __forceinline__ float wave_max(float x) {
  x = fmaxf(x, dppf<XOR1>(x)); x = fmaxf(x, dppf<XOR2>(x)); x = fmaxf(x, dppf<XOR7>(x)); x = fmaxf(x, dppf<XOR15>(x));
  return xrow_max(x);
}
__device__ __forceinline__ float sum8p(float x) { x += dppf<XOR8>(x); return xrow_sum(x); }
__device__ __forceinline__ float first_lane(float x) { return __uint_as_float(__builtin_amdgcn_readfirstlane(__float_as_uint(x))); }

__device__ __forceinline__ __amdgpu_buffer_rsrc_t mkrsrc(const void* p, int bytes) { return __builtin_amdgcn_make_buffer_rsrc((void*)p, 0, bytes, RSRC_W3); }
__device__ __forceinline__ v4u ld_nt(__amdgpu_buffer_rsrc_t r, int voff, int soff) { return __builtin_amdgcn_raw_buffer_load_b128(r, voff, soff, AUX_NT); }
__device__ __forceinline__ void dma_nt(__amdgpu_buffer_rsrc_t r, LAS unsigned char* dst, int voff, int soff) {
  __builtin_amdgcn_raw_ptr_buffer_load_lds(r, (LAS void*)dst, 16, voff, soff, 0, AUX_NT);
}
__device__ __forceinline__ v4u ld_poll(__amdgpu_buffer_rsrc_t r, int voff) { return __builtin_amdgcn_raw_buffer_load_b128(r, voff, 0, AUX_SC1); }
__device__ __forceinline__ v4u ld_poll_nt(__amdgpu_buffer_rsrc_t r, int voff) { return __builtin_amdgcn_raw_buffer_load_b128(r, voff, 0, AUX_SC1 | AUX_NT); }
__device__ __forceinline__ void st_sc1(__amdgpu_buffer_rsrc_t r, int voff, v4u v) { __builtin_amdgcn_raw_buffer_store_b128(v, r, voff, 0, AUX_SC1); }
__device__ __forceinline__ u32 enc1(float a, u32 tag) { return ((__float_as_uint(a) + 0x200u) & ~0x3ffu) | tag; }
__device__ __forceinline__ v4u enc4(f4 a, u32 tag) { return v4u{enc1(a.x, tag), enc1(a.y, tag), enc1(a.z, tag), enc1(a.w, tag)}; }
__device__ __forceinline__ u32 bad4(v4u g, u32 tag) { return ((g.x ^ tag) | (g.y ^ tag)) | ((g.z ^ tag) | (g.w ^ tag)); }
__device__ __forceinline__ bool all_ok(u32 bad) { return __all((bad & 0x3ffu) == 0u); }
__device__ __forceinline__ float dec(u32 w) { return __uint_as_float(w & ~0x3ffu); }
__device__ __forceinline__ f4 dec4(v4u g) { return f4{dec(g.x), dec(g.y), dec(g.z), dec(g.w)}; }

__device__ __forceinline__ void ln_finish(float pivot, float s1, float s2, float& mb, float& ri) {
  const float m1 = wave_sum(s1) * (1.0f / DM), m2 = wave_sum(s2) * (1.0f / DM);
  const float var = fmaxf(m2 - m1 * m1, 0.f);
  mb = rb(pivot + m1);
  ri = __builtin_amdgcn_rcpf(rb(__builtin_amdgcn_sqrtf(rb(rb(var) + 1e-5f))));
}
__device__ __forceinline__ u32 mksel(int j, int k, bool act) {
  const u32 s = (j & 1) ? 0x01000c0cu : 0x0c0c0100u;
  return (act && (j >> 1) == k) ? s : 0x0c0c0c0cu;
}
__device__ __forceinline__ v4u sparse(u32 x, const u32 (&sel)[4]) {
  return v4u{__builtin_amdgcn_perm(x, x, sel[0]), __builtin_amdgcn_perm(x, x, sel[1]), __builtin_amdgcn_perm(x, x, sel[2]), __builtin_amdgcn_perm(x, x, sel[3])};
}
__device__ __forceinline__ v4u trfrag(LAS unsigned char* a0, int step4) {
  const v2u lo = __builtin_bit_cast(v2u, __builtin_amdgcn_ds_read_tr16_b64_v4i16((LAS s4*)a0));
  const v2u hi = __builtin_bit_cast(v2u, __builtin_amdgcn_ds_read_tr16_b64_v4i16((LAS s4*)(a0 + step4)));
  return v4u{lo.x, lo.y, hi.x, hi.y};
}
__device__ __forceinline__ void lds_arrive(LAS u32* cnt, int lane) {
  asm volatile("s_waitcnt lgkmcnt(0)" ::: "memory");
  if (lane == 0) __hip_atomic_fetch_add(cnt, 1u, RLX_WG);
}
template <int SL> __device__ __forceinline__ void lds_wait(LAS u32* cnt, u32 n) {
#pragma nounroll
  for (u32 spin = 0; spin < (1u << 19); ++spin) { if (__hip_atomic_load(cnt, RLX_WG) >= n) break; __builtin_amdgcn_s_sleep(SL); }
  asm volatile("" ::: "memory");
}
#define SB() __builtin_amdgcn_sched_barrier(0)
#define MEMCLOB() asm volatile("" ::: "memory")
template <int N> __device__ __forceinline__ void wait_vm() { asm volatile("s_waitcnt vmcnt(%0)" ::"n"(N) : "memory"); }

constexpr int issued(int n) { return n + 1 + W1 < RD ? RD : n + 1 + W1 > 64 ? 64 : n + 1 + W1; }
__device__ __forceinline__ int ffn_unit(int rk, int grp) { return rk < 6 ? 2 * (grp + 8 * (rk >> 1)) + (rk & 1) : rk == 6 ? 48 + grp : NFU; }
}

extern "C" __global__ void __launch_bounds__(NTHR, 3) fused_block(Params p) {
  __shared__ __attribute__((aligned(16))) unsigned char smem[L_BYTES];
  LAS unsigned char* L = (LAS unsigned char*)smem;
  const int tid = threadIdx.x, lane = tid & 63, wid = __builtin_amdgcn_readfirstlane(tid >> 6);
  const int bid = blockIdx.x, grp = bid & 7;
  const int r = lane >> 4, c = lane & 15;
  asm volatile("" :: "s"(p.x), "s"(p.K), "s"(p.V), "s"(p.wq), "s"(p.wo), "s"(p.w1), "s"(p.w3), "s"(p.w2), "s"(p.out), "s"(p.ws));
  const int h = (bid & 7) + 8 * (bid >> 6), bs = (bid >> 3) & 7, b = bs >> 1, s = bs & 1;
  LAS u32* flags = (LAS u32*)(L + L_FLAG);
  LAS float* mbuf = (LAS float*)(L + L_M);
  LAS float* lbuf = mbuf + 8;
  const __amdgpu_buffer_rsrc_t wsr = mkrsrc(p.ws, WS_USED);
  const int t = wid & 3;

  const size_t kvoff = ((size_t)(b * NH + h) * SK + (SK - WIN) + s * 1024) * DH;
  const __amdgpu_buffer_rsrc_t kr = mkrsrc(p.K + kvoff, 1024 * DH * 2), vr = mkrsrc(p.V + kvoff, 1024 * DH * 2);
  const int kvo = wid * 1024 + lane * 16;
  v4u ring[RD];
  u32 xr[5];
  v4u wqv[5];
  u64 epoch = 0;
  if (wid < 8) {
#pragma unroll
    for (int n = 0; n < W0; ++n) ring[n] = ld_nt(kr, kvo, n * 8192);
  } else {
#pragma unroll
    for (int k = 0; k < 5; ++k) xr[k] = ((const GAS u32*)p.x)[t * 320 + lane + 64 * k];
    const __amdgpu_buffer_rsrc_t qr = mkrsrc(p.wq + h * DH, DM * NH * DH * 2 - h * DH * 2);
#pragma unroll
    for (int i = 0; i < 5; ++i) wqv[i] = ld_nt(qr, (80 * bs + 20 * t + r) * (NH * DH * 2) + c * 16, i * 4 * (NH * DH * 2));
    epoch = __hip_atomic_load((GAS u64*)p.ws, RLX_AGENT);
    if (wid == 8 && lane < 16) flags[lane] = 0u;
  }
  SB();
  __syncthreads();

  if (__builtin_expect(wid >= 8, 1)) {
    const u64 pt0 = __builtin_amdgcn_s_memrealtime(); u64 ptx = pt0, ptq = pt0;
    u32 pfv = 0;
    {
      u64 pc; u32 off;
      asm volatile("s_getpc_b64 %0\n.Lpf_here%=:\n\ts_mov_b32 %1, .Lpf_here%=-fused_block" : "=s"(pc), "=s"(off));
      if (bid < 8) {
        const u32 o = (u32)t * 8192u + lane * 128u;
        if (o < CODE_BYTES) pfv = *(const GAS u32*)(pc - off + o);
      }
    }
    u32 tag, t16;
    {
      const int bsel = (lane >> 3) & 1;
      LAS u32* xw = (LAS u32*)(L + L_LN2 + t * LN2_RS);
#pragma unroll
      for (int k = 0; k < 5; ++k) xw[lane + 64 * k] = xr[k];
      const float pivot = first_lane(blo(xr[0]));
      float s1 = 0.f, s2 = 0.f;
#pragma unroll
      for (int k = 0; k < 5; ++k) { const float d0 = blo(xr[k]) - pivot, d1 = bhi(xr[k]) - pivot; s1 += d0 + d1; s2 = fmaf(d0, d0, fmaf(d1, d1, s2)); }
      float mb, ri;
      ln_finish(pivot, s1, s2, mb, ri);
      if (lane == 0) *(LAS f2*)(L + L_STAT + t * 8) = f2{mb, ri};
      lds_arrive(flags + F_STAT, lane);
      u32 sel[4];
#pragma unroll
      for (int k = 0; k < 4; ++k) sel[k] = mksel(lane & 7, k, true);
      lds_wait<0>(flags + F_STAT, 4);
      f2 st[2];
      st[0] = *(LAS f2*)(L + L_STAT + bsel * 8); st[1] = *(LAS f2*)(L + L_STAT + (2 + bsel) * 8);
      u32 xe[5][2];
#pragma unroll
      for (int i = 0; i < 5; ++i)
#pragma unroll
        for (int P = 0; P < 2; ++P) xe[i][P] = *(LAS u16*)(L + L_LN2 + (2 * P + bsel) * LN2_RS + (80 * bs + 20 * t + r + 4 * i) * 2);
      f4 acc[2] = {{0.f, 0.f, 0.f, 0.f}, {0.f, 0.f, 0.f, 0.f}};
#pragma unroll
      for (int i = 0; i < 5; ++i)
#pragma unroll
        for (int P = 0; P < 2; ++P) acc[P] = mfma(sparse(pack2(rb(blo(xe[i][P]) - st[P].x) * st[P].y, 0.f), sel), wqv[i], acc[P]);
#pragma unroll
      for (int P = 0; P < 2; ++P) *(LAS f4*)(L + L_RED + t * 2048 + P * 1024 + lane * 16) = acc[P];
      lds_arrive(flags + F_QPART, lane);
      tag = __builtin_amdgcn_readfirstlane(((u32)epoch + 1u) & 0x3ffu);
      t16 = __builtin_amdgcn_readfirstlane(((u32)epoch + 1u) & 0xffffu);
      lds_wait<0>(flags + F_QPART, 4);
      if (lane < 32) {
        f4 sum = {0.f, 0.f, 0.f, 0.f};
#pragma unroll
        for (int ww = 0; ww < 4; ++ww) sum += *(LAS f4*)(L + L_RED + ww * 2048 + (t >> 1) * 1024 + (16 * (2 * (t & 1) + (lane & 1)) + (lane >> 1)) * 16);
        st_sc1(wsr, OFF_XQ + ((h * 4 + t) * 8 + bs) * 512 + lane * 16, enc4(sum, tag));
      }
    }
    if (bid < 8 && t == 1 && lane == 0) __hip_atomic_store((GAS u32*)(p.ws + OFF_CNT + bid * 128), t16 << 16, RLX_AGENT);
    if (t == 0) {
      __builtin_amdgcn_s_sleep(16);
      v4u g[4];
      const int go = OFF_XQ + (h * 4 + b) * 4096 + lane * 16;
#pragma nounroll
      for (u32 spin = 0;; ++spin) {
        MEMCLOB();
        u32 bad = 0;
#pragma unroll
        for (int k = 0; k < 4; ++k) g[k] = ld_poll_nt(wsr, go + k * 1024);
#pragma unroll
        for (int k = 0; k < 4; ++k) bad |= bad4(g[k], tag);
        if (all_ok(bad) || spin > SPIN_CAP) break;
        __builtin_amdgcn_s_sleep(4);
      }
      const f4 sum = (dec4(g[0]) + dec4(g[1])) + (dec4(g[2]) + dec4(g[3]));
      const float q0 = xhalf_sum(sum.x), q1 = xhalf_sum(sum.y), q2 = xhalf_sum(sum.z), q3 = xhalf_sum(sum.w);
      if (lane < 32) *(LAS v2u*)(L + L_Q + lane * 8) = v2u{pack2(q0, q1), pack2(q2, q3)};
      lds_arrive(flags + F_Q, lane);
      ptq = __builtin_amdgcn_s_memrealtime();
      lds_wait<16>(flags + F_KDONE, 8);
      const float mw = mbuf[lane & 7], lw = lbuf[lane & 7];
      float M = fmaxf(mw, dppf<XOR1>(mw)); M = fmaxf(M, dppf<XOR2>(M)); M = fmaxf(M, dppf<XOR7>(M));
      float Ls = lw * __builtin_amdgcn_exp2f((mw - M) * LOG2E); Ls += dppf<XOR1>(Ls); Ls += dppf<XOR2>(Ls); Ls += dppf<XOR7>(Ls);
      Ls = first_lane(Ls);
      const v4u mine = enc4(f4{M, Ls, M, Ls}, tag);
      if (lane == 0) st_sc1(wsr, OFF_XL + (h * 8 + bs) * 128, mine);
      v4u gl;
#pragma nounroll
      for (u32 spin = 0;; ++spin) {
        MEMCLOB();
        gl = ld_poll_nt(wsr, OFF_XL + (h * 8 + (bs ^ 1)) * 128);
        if (all_ok(bad4(gl, tag)) || spin > SPIN_CAP) break;
        __builtin_amdgcn_s_sleep(16);
      }
      const float M0 = dec(mine.x), L0 = dec(mine.y), M1 = dec(gl.x), L1 = dec(gl.y), Mx = fmaxf(M0, M1);
      const float e0 = L0 * __builtin_amdgcn_exp2f((M0 - Mx) * LOG2E), e1 = L1 * __builtin_amdgcn_exp2f((M1 - Mx) * LOG2E);
      if (lane < 8) *(LAS float*)(L + L_FAC + lane * 4) = __builtin_amdgcn_exp2f((mw - Mx) * LOG2E) * __builtin_amdgcn_rcpf(e0 + e1);
      if (lane == 0) *(LAS u32*)(L + L_INFO) = tag;
      lds_arrive(flags + F_FAC, lane);
    }
    if (t == 1) {
#pragma nounroll
      for (u32 spin = 0; spin <= SPIN_CAP; ++spin) {
        MEMCLOB();
        const u32 v = __builtin_amdgcn_raw_buffer_load_b32(wsr, OFF_CNT + grp * 128, 0, AUX_SC1 | AUX_NT);
        if (__builtin_amdgcn_readfirstlane(v >> 16) == t16) break;
        __builtin_amdgcn_s_sleep(16);
      }
      lds_wait<8>(flags + F_RTRIG, 8);
      u32 old = 0;
      if (lane == 0) old = __hip_atomic_fetch_add((GAS u32*)(p.ws + OFF_CNT + grp * 128), 1u, RLX_AGENT);
      if (lane == 0) *(LAS u32*)(L + L_INFO + 8) = old & 31u;
      lds_arrive(flags + F_ROLE, lane);
    }
    asm volatile("" :: "v"(pfv));
    lds_wait<8>(flags + F_ROLE, 1);
    const int rk = __builtin_amdgcn_readfirstlane(*(LAS u32*)(L + L_INFO + 8));
    const int u = ffn_unit(rk, grp);
    const bool ffn = u < NFU, oproj = rk >= 12;
    const int uo = grp * 20 + rk - 12, hh_o = uo / 5, j_o = uo - 5 * hh_o;
    const int task = grp * 10 + rk - 12, cs = task >> 2, sb = task & 3;
    const bool owner = rk >= 12 && rk < 22 && t == 0;
    const int q4 = c >> 2, p4 = c & 3, sw2 = 2 * (q4 + 4 * (r & 1));
    v4u bw[20];
    if (oproj || ffn) {
      lds_wait<8>(flags + F_WTRIG, 8);
      if (oproj) {
        const __amdgpu_buffer_rsrc_t orr = mkrsrc(p.wo + (size_t)hh_o * DH * DM + 128 * j_o, DH * DM * 2 - 256 * j_o);
        const int vo0 = (32 * t + r) * (DM * 2) + (c ^ (2 * r)) * 16, vo1 = (32 * t + r) * (DM * 2) + (c ^ (2 * r + 8)) * 16;
#pragma unroll
        for (int i = 0; i < 8; ++i) dma_nt(orr, L + L_W + (32 * t + 4 * i) * 256, (i & 2) ? vo1 : vo0, i * 4 * DM * 2);
      } else {
        const __amdgpu_buffer_rsrc_t w1r = mkrsrc(p.w1 + 32 * u, DM * FF * 2 - 64 * u), w3r = mkrsrc(p.w3 + 32 * u, DM * FF * 2 - 64 * u);
        const int rw = lane >> 2, c4 = lane & 3, gp = (rw >> 3) & 1;
        const int vo1 = (160 * t + rw) * (FF * 2) + (c4 ^ gp) * 16, vo3 = (160 * t + rw) * (FF * 2) + (c4 ^ (2 + gp)) * 16;
#pragma unroll
        for (int i = 0; i < 10; ++i) dma_nt(w1r, L + L_W + (160 * t + 16 * i) * 64, vo1, i * 16 * FF * 2);
#pragma unroll
        for (int i = 0; i < 10; ++i) dma_nt(w3r, L + L_W + 640 * 64 + (160 * t + 16 * i) * 64, vo3, i * 16 * FF * 2);
        wait_vm<0>();
        lds_arrive(flags + F_W13, lane);
        lds_wait<0>(flags + F_W13, 4);
#pragma unroll
        for (int ks = 0; ks < 20; ++ks) bw[ks] = trfrag(L + L_W + (p4 >> 1) * (640 * 64) + (32 * ks + 8 * r + q4) * 64 + ((t ^ (2 * (p4 >> 1) + (r & 1))) * 16) + (p4 & 1) * 8, 4 * 64);
      }
    }
    const int cb = c & 3, cr = c >> 2;
    if (oproj) {
      v4u g;
      const int go = OFF_X1 + (hh_o * 8 + 2 * t) * 512 + lane * 16;
#pragma nounroll
      for (u32 spin = 0;; ++spin) {
        MEMCLOB();
        g = ld_poll(wsr, go);
        if (all_ok(bad4(g, tag)) || spin > SPIN_CAP) break;
      }
      const f4 og = dec4(g);
      const float o0 = xhalf_sum(og.x), o1 = xhalf_sum(og.y), o2 = xhalf_sum(og.z), o3 = xhalf_sum(og.w);
      if (lane < 32) *(LAS v2u*)(L + L_O + t * O_RS + lane * 8) = v2u{pack2(o0, o1), pack2(o2, o3)};
      wait_vm<0>();
      __syncthreads();
      f4 d0 = {0.f, 0.f, 0.f, 0.f}, d1 = {0.f, 0.f, 0.f, 0.f};
#pragma unroll
      for (int ks = 0; ks < 4; ++ks) {
        const v4u a = *(LAS v4u*)(L + L_O + cb * O_RS + ks * 64 + r * 16);
        d0 = mfma(trfrag(L + L_W + (32 * ks + 8 * r + q4) * 256 + (((4 * t + (p4 >> 1)) ^ sw2) * 16) + (p4 & 1) * 8, 4 * 256), a, d0);
        d1 = mfma(trfrag(L + L_W + (32 * ks + 8 * r + q4) * 256 + (((4 * t + 2 + (p4 >> 1)) ^ sw2) * 16) + (p4 & 1) * 8, 4 * 256), a, d1);
      }
      const f4 y = (cr & 1) ? d1 : d0;
      if (cr < 2) st_sc1(wsr, OFF_X2 + ((4 * j_o + t) * 4 + cb) * X2_BLK + hh_o * 128 + cr * 64 + r * 16, enc4(y, tag));
    } else if (ffn) {
      v4u g[3];
      const int go = OFF_X3 + grp * X3_REP + t * 2560 + lane * 16;
      const bool has2 = lane < 32;
#pragma nounroll
      for (u32 spin = 0;; ++spin) {
        MEMCLOB();
        g[0] = ld_poll(wsr, go); g[1] = ld_poll(wsr, go + 1024); g[2] = ld_poll(wsr, go + (has2 ? 2048 : 0));
        if (all_ok(bad4(g[0], tag) | bad4(g[1], tag) | bad4(g[2], tag)) || spin > SPIN_CAP) break;
      }
      const float pivot = first_lane(dec(g[0].x));
      float s1 = 0.f, s2 = 0.f;
#pragma unroll
      for (int k = 0; k < 3; ++k)
#pragma unroll
        for (int e = 0; e < 4; ++e) { const float d = (k < 2 || has2) ? dec(g[k][e]) - pivot : 0.f; s1 += d; s2 = fmaf(d, d, s2); }
      float mb, ri;
      ln_finish(pivot, s1, s2, mb, ri);
      LAS unsigned char* lp = L + L_LN2 + t * LN2_RS + lane * 8;
#pragma unroll
      for (int k = 0; k < 3; ++k)
        if (k < 2 || has2) *(LAS v2u*)(lp + 512 * k) = v2u{pack2(rb(dec(g[k].x) - mb) * ri, rb(dec(g[k].y) - mb) * ri), pack2(rb(dec(g[k].z) - mb) * ri, rb(dec(g[k].w) - mb) * ri)};
      __syncthreads();
      f4 da = {0.f, 0.f, 0.f, 0.f}, db = {0.f, 0.f, 0.f, 0.f};
#pragma unroll
      for (int ks = 0; ks < 20; ks += 2) {
        da = mfma(bw[ks], *(LAS v4u*)(L + L_LN2 + cb * LN2_RS + ks * 64 + r * 16), da);
        db = mfma(bw[ks + 1], *(LAS v4u*)(L + L_LN2 + cb * LN2_RS + ks * 64 + 64 + r * 16), db);
      }
      const f4 d = da + db;
      const float pre = rb(cr == 0 ? d.x : cr == 1 ? d.y : cr == 2 ? d.z : d.w);
      auto sw = __builtin_amdgcn_permlane32_swap(__float_as_uint(pre), __float_as_uint(pre), false, false);
      const float gs = __uint_as_float(sw[0]), us = __uint_as_float(sw[1]);
      const float si = rb(gs * __builtin_amdgcn_rcpf(1.0f + __builtin_amdgcn_exp2f(-gs * LOG2E)));
      if (lane < 32) *(LAS u16*)(L + L_HH + cb * 64 + (8 * t + 4 * r + cr) * 2) = (u16)(pack2(si * us, 0.f) & 0xffffu);
      __syncthreads();
    }
    if (owner) {
      const int pp = lane >> 3, q = lane & 7;
      const v2u xv = *(LAS v2u*)(L + L_LN2 + sb * LN2_RS + (32 * cs + 4 * q) * 2);
      f4 hv;
      {
        v4u g[4];
        const int go = OFF_X2 + (cs * 4 + sb) * X2_BLK + pp * 128 + q * 16;
#pragma nounroll
        for (u32 spin = 0;; ++spin) {
          MEMCLOB();
#pragma unroll
          for (int m = 0; m < 4; ++m) g[m] = ld_poll(wsr, go + m * 1024);
          if (all_ok((bad4(g[0], tag) | bad4(g[1], tag)) | (bad4(g[2], tag) | bad4(g[3], tag))) || spin > SPIN_CAP) break;
        }
        ptx = __builtin_amdgcn_s_memrealtime();
        const f4 y = (dec4(g[0]) + dec4(g[1])) + (dec4(g[2]) + dec4(g[3]));
        hv.x = rb(blo(xv.x) + rb(sum8p(y.x))); hv.y = rb(bhi(xv.x) + rb(sum8p(y.y)));
        hv.z = rb(blo(xv.y) + rb(sum8p(y.z))); hv.w = rb(bhi(xv.y) + rb(sum8p(y.w)));
      }
      st_sc1(wsr, OFF_X3 + pp * X3_REP + sb * 2560 + cs * 128 + q * 16, enc4(hv, tag));
      __builtin_amdgcn_s_sleep(40);
      const int go = OFF_X4 + (cs * 4 + sb) * X4_BLK + pp * 128 + q * 16;
      v4u g[7];
      const bool last = pp < NFU - 48;
#pragma nounroll
      for (u32 spin = 0;; ++spin) {
        MEMCLOB();
        g[0] = ld_poll(wsr, go);
        if (all_ok(bad4(g[0], tag)) || spin > SPIN_CAP) break;
      }
#pragma nounroll
      for (u32 spin = 0;; ++spin) {
        MEMCLOB();
#pragma unroll
        for (int m = 1; m < 6; ++m) g[m] = ld_poll_nt(wsr, go + m * 1024);
        g[6] = ld_poll_nt(wsr, go + (last ? 6144 : 1024));
        u32 bad = 0;
#pragma unroll
        for (int m = 1; m < 7; ++m) bad |= bad4(g[m], tag);
        if (all_ok(bad) || spin > SPIN_CAP) break;
      }
      f4 z = ((dec4(g[0]) + dec4(g[1])) + (dec4(g[2]) + dec4(g[3]))) + (dec4(g[4]) + dec4(g[5]));
      if (last) z += dec4(g[6]);
      const float o0 = hv.x + rb(sum8p(z.x)), o1 = hv.y + rb(sum8p(z.y)), o2 = hv.z + rb(sum8p(z.z)), o3 = hv.w + rb(sum8p(z.w));
      if (lane < 8) ((GAS v2u*)p.out)[sb * 160 + 8 * cs + q] = v2u{pack2(o0, o1), pack2(o2, o3)};
      if (task == 0 && lane == 0) __hip_atomic_store((GAS u64*)p.ws, epoch + 1, RLX_AGENT);
      if (task == 0) { const u64 pe = __builtin_amdgcn_s_memrealtime(); const u64 d = PROBE == 0 ? 0 : ((PROBE == 1 ? pe : PROBE == 2 ? ptx : ptq) - pt0) * 4; while (__builtin_amdgcn_s_memrealtime() < pe + d) __builtin_amdgcn_s_sleep(2); }
    }
    return;
  }

  const int w = wid;
#pragma unroll
  for (int n = W0; n < RD; ++n) { wait_vm<W0 - 1>(); ring[n] = ld_nt(kr, kvo, n * 8192); SB(); }
  u32 selA[4];
#pragma unroll
  for (int k = 0; k < 4; ++k) selA[k] = mksel(c & 7, k, c < 8);
  lds_wait<1>(flags + F_Q, 1);
  const v4u qv = *(LAS v4u*)(L + L_Q + c * 16);
#define REFILL(n) { _Pragma("unroll") for (int m_ = issued((n) - 1); m_ < issued(n); ++m_) { if (m_ < 32) ring[m_ % RD] = ld_nt(kr, kvo, m_ * 8192); else ring[m_ % RD] = ld_nt(vr, kvo, (m_ - 32) * 8192); } SB(); }
  float sc[2];
  float x[16];
#define QK_STEP(n) { const v4u kk = ring[(n) % RD]; x[(n) & 15] = dot2(kk.w, qv.w, dot2(kk.z, qv.z, dot2(kk.y, qv.y, dot2(kk.x, qv.x, 0.f)))); REFILL(n) }
#define QK4(n) QK_STEP(n) QK_STEP((n) + 1) QK_STEP((n) + 2) QK_STEP((n) + 3)
#define TR_STEP(HALF, CTRL, BIT) _Pragma("unroll") for (int hh = 0; hh < (HALF); ++hh) { \
    const float lo_ = x[hh], hi_ = x[hh + (HALF)]; const float t1_ = lo_ + dppf<CTRL>(lo_), t2_ = hi_ + dppf<CTRL>(hi_); x[hh] = (c & (BIT)) ? t2_ : t1_; }
#define TR_ALL() TR_STEP(8, XOR8, 8) TR_STEP(4, XOR7, 4) TR_STEP(2, XOR2, 2) TR_STEP(1, XOR1, 1)
  QK4(0) QK4(4) QK4(8) QK4(12)
  TR_ALL()
  sc[0] = rb(rb(x[0]) * 0.08838834764831845f);
  QK4(16) QK4(20) QK4(24) QK4(28)
  TR_ALL()
  sc[1] = rb(rb(x[0]) * 0.08838834764831845f);
  const float mw = wave_max(fmaxf(sc[0], sc[1]));
  const float pe0 = __builtin_amdgcn_exp2f((sc[0] - mw) * LOG2E), pe1 = __builtin_amdgcn_exp2f((sc[1] - mw) * LOG2E);
  { const float lw = wave_sum(pe0 + pe1); if (lane == 0) { mbuf[w] = mw; lbuf[w] = lw; } }
  lds_arrive(flags + F_KDONE, lane);
  u32 pb[2];
  pb[0] = pack2(pe0, 0.f); pb[1] = pack2(pe1, 0.f);
  f4 acc = {0.f, 0.f, 0.f, 0.f};
#define PV_STEP(i) { const v4u vv = ring[(32 + (i)) % RD]; \
    const u32 xx = (u32)__builtin_amdgcn_update_dpp(0, (int)pb[(i) >> 4], 0x150 + ((i) & 15), 0xf, 0xf, false);     \
    acc = mfma(sparse(xx, selA), vv, acc); REFILL(32 + (i)) \
    if ((i) == WTRIG && lane == 0) __hip_atomic_fetch_add(flags + F_WTRIG, 1u, RLX_WG); \
    if ((i) == RTRIG && lane == 0) __hip_atomic_fetch_add(flags + F_RTRIG, 1u, RLX_WG); }
#define PV4(i) PV_STEP(i) PV_STEP((i) + 1) PV_STEP((i) + 2) PV_STEP((i) + 3)
  PV4(0) PV4(4) PV4(8) PV4(12) PV4(16) PV4(20) PV4(24)
  MEMCLOB();
  const u32 iflag = __hip_atomic_load(flags + F_FAC, RLX_WG);
  float fw = *(volatile LAS float*)(L + L_FAC + w * 4);
  u32 stag = *(volatile LAS u32*)(L + L_INFO);
  PV4(28)
  if (__builtin_amdgcn_readfirstlane(iflag) == 0u) { lds_wait<0>(flags + F_FAC, 1); fw = *(volatile LAS float*)(L + L_FAC + w * 4); stag = *(volatile LAS u32*)(L + L_INFO); }
  acc *= fw;
  if (lane < 32) *(LAS f4*)(L + L_RED + w * 512 + (8 * c + 4 * r) * 4) = acc;
  asm volatile("s_waitcnt lgkmcnt(0)" ::: "memory");
  u32 old = 0;
  if (lane == 0) old = __hip_atomic_fetch_add(flags + F_PART, 1u, RLX_WG);
  if (__builtin_amdgcn_readfirstlane(old) == 7u) {
    MEMCLOB();
    if (lane < 32) {
      f4 sum = {0.f, 0.f, 0.f, 0.f};
#pragma unroll
      for (int ww = 0; ww < 8; ++ww) sum += *(LAS f4*)(L + L_RED + ww * 512 + lane * 16);
      st_sc1(wsr, OFF_X1 + (h * 8 + bs) * 512 + lane * 16, enc4(sum, stag));
    }
  }
  if (w < 4) {
    lds_wait<8>(flags + F_ROLE, 1);
    const int u = ffn_unit(__builtin_amdgcn_readfirstlane(*(LAS u32*)(L + L_INFO + 8)), grp);
    if (u < NFU) {
      const int q4 = c >> 2, p4 = c & 3, cb = c & 3, cr = c >> 2;
      const __amdgpu_buffer_rsrc_t w2r = mkrsrc(p.w2 + (size_t)32 * u * DM, 32 * DM * 2);
      const int vo2 = r * (DM * 2) + (c ^ (2 * (r + 4 * (w & 1)))) * 16;
#pragma unroll
      for (int i = 0; i < 10; ++i) dma_nt(w2r, L + L_W2 + (10 * w + i) * 1024, vo2, w * (8 * DM * 2) + (i / 5) * 4 * DM * 2 + (i % 5) * 256);
      wait_vm<0>();
      lds_arrive(flags + F_W2, lane);
      lds_wait<1>(flags + F_W2, 4);
      const int sw2 = 2 * (q4 + 4 * (r & 1));
      v4u bd[10];
#pragma unroll
      for (int T = 0; T < 10; ++T) { const int col = 160 * w + 16 * T + 4 * p4; bd[T] = trfrag(L + L_W2 + ((2 * r) * 5 + (col >> 7)) * 1024 + q4 * 256 + ((((col & 127) >> 3) ^ sw2) * 16) + (p4 & 1) * 8, 5 * 1024); }
      asm volatile("" :: "v"(bd[0]), "v"(bd[1]), "v"(bd[2]), "v"(bd[3]), "v"(bd[4]), "v"(bd[5]), "v"(bd[6]), "v"(bd[7]), "v"(bd[8]), "v"(bd[9]));
      __syncthreads();
      __syncthreads();
      const v4u a = *(LAS v4u*)(L + L_HH + cb * 64 + r * 16);
      const int zo = OFF_X4 + cb * X4_BLK + u * 128 + (cr & 1) * 64 + r * 16;
      const f4 zero = {0.f, 0.f, 0.f, 0.f};
      v4u am[4];
#pragma unroll
      for (int k = 0; k < 4; ++k) am[k] = cr == k ? a : v4u{0u, 0u, 0u, 0u};
      f4 z0 = mfma(bd[0], am[0], zero), z1 = mfma(bd[4], am[0], zero), z2 = mfma(bd[8], am[0], zero);
      z0 = mfma(bd[1], am[1], z0); z1 = mfma(bd[5], am[1], z1); z2 = mfma(bd[9], am[1], z2);
      z0 = mfma(bd[2], am[2], z0); z1 = mfma(bd[6], am[2], z1);
      z0 = mfma(bd[3], am[3], z0); z1 = mfma(bd[7], am[3], z1);
      if (cr < 2) st_sc1(wsr, zo + (5 * w + 4) * 4 * X4_BLK, enc4(z2, stag));
      st_sc1(wsr, zo + (5 * w + (cr >> 1)) * 4 * X4_BLK, enc4(z0, stag));
      st_sc1(wsr, zo + (5 * w + 2 + (cr >> 1)) * 4 * X4_BLK, enc4(z1, stag));
    }
  }
}

extern "C" void kernel_launch(void* const* d_in, const int* in_sizes, int n_in, void* d_out, int out_size, void* d_ws, size_t ws_size, hipStream_t stream) {
  Params p;
  p.x = (const u16*)d_in[0]; p.K = (const u16*)d_in[1]; p.V = (const u16*)d_in[2]; p.wq = (const u16*)d_in[3]; p.wo = (const u16*)d_in[4];
  p.w1 = (const u16*)d_in[6]; p.w3 = (const u16*)d_in[7]; p.w2 = (const u16*)d_in[8];
  p.out = (u16*)d_out; p.ws = (unsigned char*)d_ws;
  hipLaunchKernelGGL(fused_block, dim3(256), dim3(NTHR), 0, stream, p);
}
```

```cpp
#include <hip/hip_runtime.h>
#include <stdint.h>

typedef unsigned short u16;
typedef unsigned u32;
typedef unsigned long long u64;
typedef u32 v4u __attribute__((ext_vector_type(4)));
typedef u32 v2u __attribute__((ext_vector_type(2)));
typedef float f4 __attribute__((ext_vector_type(4)));
typedef float f2 __attribute__((ext_vector_type(2)));
typedef short s4 __attribute__((ext_vector_type(4)));
typedef __bf16 bf2 __attribute__((ext_vector_type(2)));
typedef __bf16 bf8 __attribute__((ext_vector_type(8)));
#define GAS __attribute__((address_space(1)))
#define LAS __attribute__((address_space(3)))
#define RLX_AGENT __ATOMIC_RELAXED, __HIP_MEMORY_SCOPE_AGENT
#define RLX_WG __ATOMIC_RELAXED, __HIP_MEMORY_SCOPE_WORKGROUP

namespace {
constexpr int DM = 640, NH = 32, DH = 128, SK = 8192, WIN = 2048, FF = 1728;
constexpr int NTHR = 768;
constexpr int RD = 24;
constexpr int W0 = 3;
constexpr int W1 = 4;
constexpr int RTRIG = 16;
constexpr int WTRIG = 22;
constexpr u32 CODE_BYTES = 18688u;
constexpr int NFU = 54;
constexpr int AUX_NT = 2, AUX_SC1 = 16, RSRC_W3 = 0x00020000;
constexpr int OFF_CNT = 0x800;
constexpr int OFF_XL = 0x1000;
constexpr int OFF_XQ = 0x10000;
constexpr int OFF_X1 = 0x90000;
constexpr int OFF_X2 = 0xB0000;
constexpr int X2_BLK = 33 * 128;
constexpr int OFF_X3 = 0x108000;
constexpr int X3_REP = 10240 + 128;
constexpr int OFF_X4 = 0x120000;
constexpr int X4_BLK = 55 * 128;
constexpr int WS_USED = 0x1B0000;
constexpr u32 SPIN_CAP = 1u << 16;
constexpr float LOG2E = 1.4426950408889634f;
constexpr int L_FLAG = 0;
constexpr int L_STAT = 64;
constexpr int L_INFO = 96;
constexpr int L_M = 128;
constexpr int L_FAC = 192;
constexpr int L_Q = 256;
constexpr int L_HH = 512;
constexpr int L_O = 1024;
constexpr int O_RS = 320;
constexpr int L_LN2 = 3328;
constexpr int LN2_RS = 1344;
constexpr int L_RED = 8704;
constexpr int L_W = L_RED + 8192;
constexpr int L_W2 = L_W + 2 * 640 * 64;
constexpr int L_BYTES = L_W2 + 32 * 1280;
enum { F_STAT = 0, F_QPART = 1, F_W13 = 2, F_Q = 3, F_KDONE = 5, F_PART = 6, F_WTRIG = 7, F_FAC = 8, F_RTRIG = 9, F_ROLE = 10, F_W2 = 11 };

struct Params {
  const u16 *x, *K, *V, *wq, *wo, *w1, *w3, *w2;
  u16* out;
  unsigned char* ws;
};

__device__ __forceinline__ float rb(float f) { return (float)(__bf16)f; }
__device__ __forceinline__ u32 pack2(float lo, float hi) { bf2 v = {(__bf16)lo, (__bf16)hi}; return __builtin_bit_cast(u32, v); }
__device__ __forceinline__ float blo(u32 v) { return __uint_as_float(v << 16); }
__device__ __forceinline__ float bhi(u32 v) { return __uint_as_float(v & 0xffff0000u); }
__device__ __forceinline__ float dot2(u32 a, u32 b, float c) {
  return __builtin_amdgcn_fdot2_f32_bf16(__builtin_bit_cast(bf2, a), __builtin_bit_cast(bf2, b), c, false);
}
__device__ __forceinline__ f4 mfma(v4u a, v4u b, f4 c) {
  return __builtin_amdgcn_mfma_f32_16x16x32_bf16(__builtin_bit_cast(bf8, a), __builtin_bit_cast(bf8, b), c, 0, 0, 0);
}
template <int CTRL> __device__ __forceinline__ u32 dppu(u32 x) { return (u32)__builtin_amdgcn_mov_dpp((int)x, CTRL, 0xf, 0xf, true); }
template <int CTRL> __device__ __forceinline__ float dppf(float x) { return __uint_as_float(dppu<CTRL>(__float_as_uint(x))); }
constexpr int XOR1 = 0xB1, XOR2 = 0x4E, XOR7 = 0x141, XOR8 = 0x128, XOR15 = 0x140;
__device__ __forceinline__ float xrow_sum(float x) {
  auto s = __builtin_amdgcn_permlane16_swap(__float_as_uint(x), __float_as_uint(x), false, false);
  x = __uint_as_float(s[0]) + __uint_as_float(s[1]);
  auto t = __builtin_amdgcn_permlane32_swap(__float_as_uint(x), __float_as_uint(x), false, false);
  return __uint_as_float(t[0]) + __uint_as_float(t[1]);
}
__device__ __forceinline__ float xhalf_sum(float x) {
  auto t = __builtin_amdgcn_permlane32_swap(__float_as_uint(x), __float_as_uint(x), false, false);
  return __uint_as_float(t[0]) + __uint_as_float(t[1]);
}
__device__ __forceinline__ float xrow_max(float x) {
  auto s = __builtin_amdgcn_permlane16_swap(__float_as_uint(x), __float_as_uint(x), false, false);
  x = fmaxf(__uint_as_float(s[0]), __uint_as_float(s[1]));
  auto t = __builtin_amdgcn_permlane32_swap(__float_as_uint(x), __float_as_uint(x), false, false);
  return fmaxf(__uint_as_float(t[0]), __uint_as_float(t[1]));
}
__device__ __forceinline__ float wave_sum(float x) {
  x += dppf<XOR1>(x); x += dppf<XOR2>(x); x += dppf<XOR7>(x); x += dppf<XOR15>(x);
  return xrow_sum(x);
}
__device__ __forceinline__ float wave_max(float x) {
  x = fmaxf(x, dppf<XOR1>(x)); x = fmaxf(x, dppf<XOR2>(x)); x = fmaxf(x, dppf<XOR7>(x)); x = fmaxf(x, dppf<XOR15>(x));
  return xrow_max(x);
}
__device__ __forceinline__ float sum8p(float x) { x += dppf<XOR8>(x); return xrow_sum(x); }
__device__ __forceinline__ float first_lane(float x) { return __uint_as_float(__builtin_amdgcn_readfirstlane(__float_as_uint(x))); }

__device__ __forceinline__ __amdgpu_buffer_rsrc_t mkrsrc(const void* p, int bytes) { return __builtin_amdgcn_make_buffer_rsrc((void*)p, 0, bytes, RSRC_W3); }
__device__ __forceinline__ v4u ld_nt(__amdgpu_buffer_rsrc_t r, int voff, int soff) { return __builtin_amdgcn_raw_buffer_load_b128(r, voff, soff, AUX_NT); }
__device__ __forceinline__ void dma_nt(__amdgpu_buffer_rsrc_t r, LAS unsigned char* dst, int voff, int soff) {
  __builtin_amdgcn_raw_ptr_buffer_load_lds(r, (LAS void*)dst, 16, voff, soff, 0, AUX_NT);
}
__device__ __forceinline__ v4u ld_poll(__amdgpu_buffer_rsrc_t r, int voff) { return __builtin_amdgcn_raw_buffer_load_b128(r, voff, 0, AUX_SC1); }
__device__ __forceinline__ v4u ld_poll_nt(__amdgpu_buffer_rsrc_t r, int voff) { return __builtin_amdgcn_raw_buffer_load_b128(r, voff, 0, AUX_SC1 | AUX_NT); }
__device__ __forceinline__ void st_sc1(__amdgpu_buffer_rsrc_t r, int voff, v4u v) { __builtin_amdgcn_raw_buffer_store_b128(v, r, voff, 0, AUX_SC1); }
__device__ __forceinline__ u32 enc1(float a, u32 tag) { return ((__float_as_uint(a) + 0x200u) & ~0x3ffu) | tag; }
__device__ __forceinline__ v4u enc4(f4 a, u32 tag) { return v4u{enc1(a.x, tag), enc1(a.y, tag), enc1(a.z, tag), enc1(a.w, tag)}; }
__device__ __forceinline__ u32 bad4(v4u g, u32 tag) { return ((g.x ^ tag) | (g.y ^ tag)) | ((g.z ^ tag) | (g.w ^ tag)); }
__device__ __forceinline__ bool all_ok(u32 bad) { return __all((bad & 0x3ffu) == 0u); }
__device__ __forceinline__ float dec(u32 w) { return __uint_as_float(w & ~0x3ffu); }
__device__ __forceinline__ f4 dec4(v4u g) { return f4{dec(g.x), dec(g.y), dec(g.z), dec(g.w)}; }

__device__ __forceinline__ void ln_finish(float pivot, float s1, float s2, float& mb, float& ri) {
  const float m1 = wave_sum(s1) * (1.0f / DM), m2 = wave_sum(s2) * (1.0f / DM);
  const float var = fmaxf(m2 - m1 * m1, 0.f);
  mb = rb(pivot + m1);
  ri = __builtin_amdgcn_rcpf(rb(__builtin_amdgcn_sqrtf(rb(rb(var) + 1e-5f))));
}
__device__ __forceinline__ u32 mksel(int j, int k, bool act) {
  const u32 s = (j & 1) ? 0x01000c0cu : 0x0c0c0100u;
  return (act && (j >> 1) == k) ? s : 0x0c0c0c0cu;
}
__device__ __forceinline__ v4u sparse(u32 x, const u32 (&sel)[4]) {
  return v4u{__builtin_amdgcn_perm(x, x, sel[0]), __builtin_amdgcn_perm(x, x, sel[1]), __builtin_amdgcn_perm(x, x, sel[2]), __builtin_amdgcn_perm(x, x, sel[3])};
}
__device__ __forceinline__ v4u trfrag(LAS unsigned char* a0, int step4) {
  const v2u lo = __builtin_bit_cast(v2u, __builtin_amdgcn_ds_read_tr16_b64_v4i16((LAS s4*)a0));
  const v2u hi = __builtin_bit_cast(v2u, __builtin_amdgcn_ds_read_tr16_b64_v4i16((LAS s4*)(a0 + step4)));
  return v4u{lo.x, lo.y, hi.x, hi.y};
}
__device__ __forceinline__ void lds_arrive(LAS u32* cnt, int lane) {
  asm volatile("s_waitcnt lgkmcnt(0)" ::: "memory");
  if (lane == 0) __hip_atomic_fetch_add(cnt, 1u, RLX_WG);
}
template <int SL> __device__ __forceinline__ void lds_wait(LAS u32* cnt, u32 n) {
#pragma nounroll
  for (u32 spin = 0; spin < (1u << 19); ++spin) { if (__hip_atomic_load(cnt, RLX_WG) >= n) break; __builtin_amdgcn_s_sleep(SL); }
  asm volatile("" ::: "memory");
}
#define SB() __builtin_amdgcn_sched_barrier(0)
#define MEMCLOB() asm volatile("" ::: "memory")
template <int N> __device__ __forceinline__ void wait_vm() { asm volatile("s_waitcnt vmcnt(%0)" ::"n"(N) : "memory"); }

constexpr int issued(int n) { return n + 1 + W1 < RD ? RD : n + 1 + W1 > 64 ? 64 : n + 1 + W1; }
__device__ __forceinline__ int ffn_unit(int rk, int grp) { return rk < 6 ? 2 * (grp + 8 * (rk >> 1)) + (rk & 1) : rk == 6 ? 48 + grp : NFU; }
}

extern "C" __global__ void __launch_bounds__(NTHR, 3) fused_block(Params p) {
  __shared__ __attribute__((aligned(16))) unsigned char smem[L_BYTES];
  LAS unsigned char* L = (LAS unsigned char*)smem;
  const int tid = threadIdx.x, lane = tid & 63, wid = __builtin_amdgcn_readfirstlane(tid >> 6);
  const int bid = blockIdx.x, grp = bid & 7;
  const int r = lane >> 4, c = lane & 15;
  asm volatile("" :: "s"(p.x), "s"(p.K), "s"(p.V), "s"(p.wq), "s"(p.wo), "s"(p.w1), "s"(p.w3), "s"(p.w2), "s"(p.out), "s"(p.ws));
  const int h = (bid & 7) + 8 * (bid >> 6), bs = (bid >> 3) & 7, b = bs >> 1, s = bs & 1;
  LAS u32* flags = (LAS u32*)(L + L_FLAG);
  LAS float* mbuf = (LAS float*)(L + L_M);
  LAS float* lbuf = mbuf + 8;
  const __amdgpu_buffer_rsrc_t wsr = mkrsrc(p.ws, WS_USED);
  const int t = wid & 3;

  const size_t kvoff = ((size_t)(b * NH + h) * SK + (SK - WIN) + s * 1024) * DH;
  const __amdgpu_buffer_rsrc_t kr = mkrsrc(p.K + kvoff, 1024 * DH * 2), vr = mkrsrc(p.V + kvoff, 1024 * DH * 2);
  const int kvo = wid * 1024 + lane * 16;
  v4u ring[RD];
  u32 xr[5];
  v4u wqv[5];
  u64 epoch = 0;
  if (wid < 8) {
#pragma unroll
    for (int n = 0; n < W0; ++n) ring[n] = ld_nt(kr, kvo, n * 8192);
  } else {
#pragma unroll
    for (int k = 0; k < 5; ++k) xr[k] = ((const GAS u32*)p.x)[t * 320 + lane + 64 * k];
    const __amdgpu_buffer_rsrc_t qr = mkrsrc(p.wq + h * DH, DM * NH * DH * 2 - h * DH * 2);
#pragma unroll
    for (int i = 0; i < 5; ++i) wqv[i] = ld_nt(qr, (80 * bs + 20 * t + r) * (NH * DH * 2) + c * 16, i * 4 * (NH * DH * 2));
    epoch = __hip_atomic_load((GAS u64*)p.ws, RLX_AGENT);
    if (wid == 8 && lane < 16) flags[lane] = 0u;
  }
  SB();
  __syncthreads();

  if (__builtin_expect(wid >= 8, 1)) {
    u32 pfv = 0;
    {
      u64 pc; u32 off;
      asm volatile("s_getpc_b64 %0\n.Lpf_here%=:\n\ts_mov_b32 %1, .Lpf_here%=-fused_block" : "=s"(pc), "=s"(off));
      if (bid < 8) {
        const u32 o = (u32)t * 8192u + lane * 128u;
        if (o < CODE_BYTES) pfv = *(const GAS u32*)(pc - off + o);
      }
    }
    u32 tag, t16;
    {
      const int bsel = (lane >> 3) & 1;
      LAS u32* xw = (LAS u32*)(L + L_LN2 + t * LN2_RS);
#pragma unroll
      for (int k = 0; k < 5; ++k) xw[lane + 64 * k] = xr[k];
      const float pivot = first_lane(blo(xr[0]));
      float s1 = 0.f, s2 = 0.f;
#pragma unroll
      for (int k = 0; k < 5; ++k) { const float d0 = blo(xr[k]) - pivot, d1 = bhi(xr[k]) - pivot; s1 += d0 + d1; s2 = fmaf(d0, d0, fmaf(d1, d1, s2)); }
      float mb, ri;
      ln_finish(pivot, s1, s2, mb, ri);
      if (lane == 0) *(LAS f2*)(L + L_STAT + t * 8) = f2{mb, ri};
      lds_arrive(flags + F_STAT, lane);
      u32 sel[4];
#pragma unroll
      for (int k = 0; k < 4; ++k) sel[k] = mksel(lane & 7, k, true);
      lds_wait<0>(flags + F_STAT, 4);
      f2 st[2];
      st[0] = *(LAS f2*)(L + L_STAT + bsel * 8); st[1] = *(LAS f2*)(L + L_STAT + (2 + bsel) * 8);
      u32 xe[5][2];
#pragma unroll
      for (int i = 0; i < 5; ++i)
#pragma unroll
        for (int P = 0; P < 2; ++P) xe[i][P] = *(LAS u16*)(L + L_LN2 + (2 * P + bsel) * LN2_RS + (80 * bs + 20 * t + r + 4 * i) * 2);
      f4 acc[2] = {{0.f, 0.f, 0.f, 0.f}, {0.f, 0.f, 0.f, 0.f}};
#pragma unroll
      for (int i = 0; i < 5; ++i)
#pragma unroll
        for (int P = 0; P < 2; ++P) acc[P] = mfma(sparse(pack2(rb(blo(xe[i][P]) - st[P].x) * st[P].y, 0.f), sel), wqv[i], acc[P]);
#pragma unroll
      for (int P = 0; P < 2; ++P) *(LAS f4*)(L + L_RED + t * 2048 + P * 1024 + lane * 16) = acc[P];
      lds_arrive(flags + F_QPART, lane);
      tag = __builtin_amdgcn_readfirstlane(((u32)epoch + 1u) & 0x3ffu);
      t16 = __builtin_amdgcn_readfirstlane(((u32)epoch + 1u) & 0xffffu);
      lds_wait<0>(flags + F_QPART, 4);
      if (lane < 32) {
        f4 sum = {0.f, 0.f, 0.f, 0.f};
#pragma unroll
        for (int ww = 0; ww < 4; ++ww) sum += *(LAS f4*)(L + L_RED + ww * 2048 + (t >> 1) * 1024 + (16 * (2 * (t & 1) + (lane & 1)) + (lane >> 1)) * 16);
        st_sc1(wsr, OFF_XQ + ((h * 4 + t) * 8 + bs) * 512 + lane * 16, enc4(sum, tag));
      }
    }
    if (bid < 8 && t == 1 && lane == 0) __hip_atomic_store((GAS u32*)(p.ws + OFF_CNT + bid * 128), t16 << 16, RLX_AGENT);
    if (t == 0) {
      __builtin_amdgcn_s_sleep(16);
      v4u g[4];
      const int go = OFF_XQ + (h * 4 + b) * 4096 + lane * 16;
#pragma nounroll
      for (u32 spin = 0;; ++spin) {
        MEMCLOB();
        u32 bad = 0;
#pragma unroll
        for (int k = 0; k < 4; ++k) g[k] = ld_poll_nt(wsr, go + k * 1024);
#pragma unroll
        for (int k = 0; k < 4; ++k) bad |= bad4(g[k], tag);
        if (all_ok(bad) || spin > SPIN_CAP) break;
        __builtin_amdgcn_s_sleep(4);
      }
      const f4 sum = (dec4(g[0]) + dec4(g[1])) + (dec4(g[2]) + dec4(g[3]));
      const float q0 = xhalf_sum(sum.x), q1 = xhalf_sum(sum.y), q2 = xhalf_sum(sum.z), q3 = xhalf_sum(sum.w);
      if (lane < 32) *(LAS v2u*)(L + L_Q + lane * 8) = v2u{pack2(q0, q1), pack2(q2, q3)};
      lds_arrive(flags + F_Q, lane);
      lds_wait<16>(flags + F_KDONE, 8);
      const float mw = mbuf[lane & 7], lw = lbuf[lane & 7];
      float M = fmaxf(mw, dppf<XOR1>(mw)); M = fmaxf(M, dppf<XOR2>(M)); M = fmaxf(M, dppf<XOR7>(M));
      float Ls = lw * __builtin_amdgcn_exp2f((mw - M) * LOG2E); Ls += dppf<XOR1>(Ls); Ls += dppf<XOR2>(Ls); Ls += dppf<XOR7>(Ls);
      Ls = first_lane(Ls);
      const v4u mine = enc4(f4{M, Ls, M, Ls}, tag);
      if (lane == 0) st_sc1(wsr, OFF_XL + (h * 8 + bs) * 128, mine);
      v4u gl;
#pragma nounroll
      for (u32 spin = 0;; ++spin) {
        MEMCLOB();
        gl = ld_poll_nt(wsr, OFF_XL + (h * 8 + (bs ^ 1)) * 128);
        if (all_ok(bad4(gl, tag)) || spin > SPIN_CAP) break;
        __builtin_amdgcn_s_sleep(16);
      }
      const float M0 = dec(mine.x), L0 = dec(mine.y), M1 = dec(gl.x), L1 = dec(gl.y), Mx = fmaxf(M0, M1);
      const float e0 = L0 * __builtin_amdgcn_exp2f((M0 - Mx) * LOG2E), e1 = L1 * __builtin_amdgcn_exp2f((M1 - Mx) * LOG2E);
      if (lane < 8) *(LAS float*)(L + L_FAC + lane * 4) = __builtin_amdgcn_exp2f((mw - Mx) * LOG2E) * __builtin_amdgcn_rcpf(e0 + e1);
      if (lane == 0) *(LAS u32*)(L + L_INFO) = tag;
      lds_arrive(flags + F_FAC, lane);
    }
    if (t == 1) {
#pragma nounroll
      for (u32 spin = 0; spin <= SPIN_CAP; ++spin) {
        MEMCLOB();
        const u32 v = __builtin_amdgcn_raw_buffer_load_b32(wsr, OFF_CNT + grp * 128, 0, AUX_SC1 | AUX_NT);
        if (__builtin_amdgcn_readfirstlane(v >> 16) == t16) break;
        __builtin_amdgcn_s_sleep(16);
      }
      lds_wait<8>(flags + F_RTRIG, 8);
      u32 old = 0;
      if (lane == 0) old = __hip_atomic_fetch_add((GAS u32*)(p.ws + OFF_CNT + grp * 128), 1u, RLX_AGENT);
      if (lane == 0) *(LAS u32*)(L + L_INFO + 8) = old & 31u;
      lds_arrive(flags + F_ROLE, lane);
    }
    asm volatile("" :: "v"(pfv));
    lds_wait<8>(flags + F_ROLE, 1);
    const int rk = __builtin_amdgcn_readfirstlane(*(LAS u32*)(L + L_INFO + 8));
    const int u = ffn_unit(rk, grp);
    const bool ffn = u < NFU, oproj = rk >= 12;
    const int uo = grp * 20 + rk - 12, hh_o = uo / 5, j_o = uo - 5 * hh_o;
    const int task = grp * 10 + rk - 12, cs = task >> 2, sb = task & 3;
    const bool owner = rk >= 12 && rk < 22 && t == 0;
    const int q4 = c >> 2, p4 = c & 3, sw2 = 2 * (q4 + 4 * (r & 1));
    v4u bw[20];
    if (oproj || ffn) {
      lds_wait<8>(flags + F_WTRIG, 8);
      if (oproj) {
        const __amdgpu_buffer_rsrc_t orr = mkrsrc(p.wo + (size_t)hh_o * DH * DM + 128 * j_o, DH * DM * 2 - 256 * j_o);
        const int vo0 = (32 * t + r) * (DM * 2) + (c ^ (2 * r)) * 16, vo1 = (32 * t + r) * (DM * 2) + (c ^ (2 * r + 8)) * 16;
#pragma unroll
        for (int i = 0; i < 8; ++i) dma_nt(orr, L + L_W + (32 * t + 4 * i) * 256, (i & 2) ? vo1 : vo0, i * 4 * DM * 2);
      } else {
        const __amdgpu_buffer_rsrc_t w1r = mkrsrc(p.w1 + 32 * u, DM * FF * 2 - 64 * u), w3r = mkrsrc(p.w3 + 32 * u, DM * FF * 2 - 64 * u);
        const int rw = lane >> 2, c4 = lane & 3, gp = (rw >> 3) & 1;
        const int vo1 = (160 * t + rw) * (FF * 2) + (c4 ^ gp) * 16, vo3 = (160 * t + rw) * (FF * 2) + (c4 ^ (2 + gp)) * 16;
#pragma unroll
        for (int i = 0; i < 10; ++i) dma_nt(w1r, L + L_W + (160 * t + 16 * i) * 64, vo1, i * 16 * FF * 2);
#pragma unroll
        for (int i = 0; i < 10; ++i) dma_nt(w3r, L + L_W + 640 * 64 + (160 * t + 16 * i) * 64, vo3, i * 16 * FF * 2);
        wait_vm<0>();
        lds_arrive(flags + F_W13, lane);
        lds_wait<0>(flags + F_W13, 4);
#pragma unroll
        for (int ks = 0; ks < 20; ++ks) bw[ks] = trfrag(L + L_W + (p4 >> 1) * (640 * 64) + (32 * ks + 8 * r + q4) * 64 + ((t ^ (2 * (p4 >> 1) + (r & 1))) * 16) + (p4 & 1) * 8, 4 * 64);
      }
    }
    const int cb = c & 3, cr = c >> 2;
    if (oproj) {
      v4u g;
      const int go = OFF_X1 + (hh_o * 8 + 2 * t) * 512 + lane * 16;
#pragma nounroll
      for (u32 spin = 0;; ++spin) {
        MEMCLOB();
        g = ld_poll(wsr, go);
        if (all_ok(bad4(g, tag)) || spin > SPIN_CAP) break;
      }
      const f4 og = dec4(g);
      const float o0 = xhalf_sum(og.x), o1 = xhalf_sum(og.y), o2 = xhalf_sum(og.z), o3 = xhalf_sum(og.w);
      if (lane < 32) *(LAS v2u*)(L + L_O + t * O_RS + lane * 8) = v2u{pack2(o0, o1), pack2(o2, o3)};
      wait_vm<0>();
      __syncthreads();
      f4 d0 = {0.f, 0.f, 0.f, 0.f}, d1 = {0.f, 0.f, 0.f, 0.f};
#pragma unroll
      for (int ks = 0; ks < 4; ++ks) {
        const v4u a = *(LAS v4u*)(L + L_O + cb * O_RS + ks * 64 + r * 16);
        d0 = mfma(trfrag(L + L_W + (32 * ks + 8 * r + q4) * 256 + (((4 * t + (p4 >> 1)) ^ sw2) * 16) + (p4 & 1) * 8, 4 * 256), a, d0);
        d1 = mfma(trfrag(L + L_W + (32 * ks + 8 * r + q4) * 256 + (((4 * t + 2 + (p4 >> 1)) ^ sw2) * 16) + (p4 & 1) * 8, 4 * 256), a, d1);
      }
      const f4 y = (cr & 1) ? d1 : d0;
      if (cr < 2) st_sc1(wsr, OFF_X2 + ((4 * j_o + t) * 4 + cb) * X2_BLK + hh_o * 128 + cr * 64 + r * 16, enc4(y, tag));
    } else if (ffn) {
      v4u g[3];
      const int go = OFF_X3 + grp * X3_REP + t * 2560 + lane * 16;
      const bool has2 = lane < 32;
#pragma nounroll
      for (u32 spin = 0;; ++spin) {
        MEMCLOB();
        g[0] = ld_poll(wsr, go); g[1] = ld_poll(wsr, go + 1024); g[2] = ld_poll(wsr, go + (has2 ? 2048 : 0));
        if (all_ok(bad4(g[0], tag) | bad4(g[1], tag) | bad4(g[2], tag)) || spin > SPIN_CAP) break;
      }
      const float pivot = first_lane(dec(g[0].x));
      float s1 = 0.f, s2 = 0.f;
#pragma unroll
      for (int k = 0; k < 3; ++k)
#pragma unroll
        for (int e = 0; e < 4; ++e) { const float d = (k < 2 || has2) ? dec(g[k][e]) - pivot : 0.f; s1 += d; s2 = fmaf(d, d, s2); }
      float mb, ri;
      ln_finish(pivot, s1, s2, mb, ri);
      LAS unsigned char* lp = L + L_LN2 + t * LN2_RS + lane * 8;
#pragma unroll
      for (int k = 0; k < 3; ++k)
        if (k < 2 || has2) *(LAS v2u*)(lp + 512 * k) = v2u{pack2(rb(dec(g[k].x) - mb) * ri, rb(dec(g[k].y) - mb) * ri), pack2(rb(dec(g[k].z) - mb) * ri, rb(dec(g[k].w) - mb) * ri)};
      __syncthreads();
      f4 da = {0.f, 0.f, 0.f, 0.f}, db = {0.f, 0.f, 0.f, 0.f};
#pragma unroll
      for (int ks = 0; ks < 20; ks += 2) {
        da = mfma(bw[ks], *(LAS v4u*)(L + L_LN2 + cb * LN2_RS + ks * 64 + r * 16), da);
        db = mfma(bw[ks + 1], *(LAS v4u*)(L + L_LN2 + cb * LN2_RS + ks * 64 + 64 + r * 16), db);
      }
      const f4 d = da + db;
      const float pre = rb(cr == 0 ? d.x : cr == 1 ? d.y : cr == 2 ? d.z : d.w);
      auto sw = __builtin_amdgcn_permlane32_swap(__float_as_uint(pre), __float_as_uint(pre), false, false);
      const float gs = __uint_as_float(sw[0]), us = __uint_as_float(sw[1]);
      const float si = rb(gs * __builtin_amdgcn_rcpf(1.0f + __builtin_amdgcn_exp2f(-gs * LOG2E)));
      if (lane < 32) *(LAS u16*)(L + L_HH + cb * 64 + (8 * t + 4 * r + cr) * 2) = (u16)(pack2(si * us, 0.f) & 0xffffu);
      __syncthreads();
    }
    if (owner) {
      const int pp = lane >> 3, q = lane & 7;
      const v2u xv = *(LAS v2u*)(L + L_LN2 + sb * LN2_RS + (32 * cs + 4 * q) * 2);
      f4 hv;
      {
        v4u g[4];
        const int go = OFF_X2 + (cs * 4 + sb) * X2_BLK + pp * 128 + q * 16;
#pragma nounroll
        for (u32 spin = 0;; ++spin) {
          MEMCLOB();
#pragma unroll
          for (int m = 0; m < 4; ++m) g[m] = ld_poll(wsr, go + m * 1024);
          if (all_ok((bad4(g[0], tag) | bad4(g[1], tag)) | (bad4(g[2], tag) | bad4(g[3], tag))) || spin > SPIN_CAP) break;
        }
        const f4 y = (dec4(g[0]) + dec4(g[1])) + (dec4(g[2]) + dec4(g[3]));
        hv.x = rb(blo(xv.x) + rb(sum8p(y.x))); hv.y = rb(bhi(xv.x) + rb(sum8p(y.y)));
        hv.z = rb(blo(xv.y) + rb(sum8p(y.z))); hv.w = rb(bhi(xv.y) + rb(sum8p(y.w)));
      }
      st_sc1(wsr, OFF_X3 + pp * X3_REP + sb * 2560 + cs * 128 + q * 16, enc4(hv, tag));
      __builtin_amdgcn_s_sleep(40);
      const int go = OFF_X4 + (cs * 4 + sb) * X4_BLK + pp * 128 + q * 16;
      v4u g[7];
      const bool last = pp < NFU - 48;
#pragma nounroll
      for (u32 spin = 0;; ++spin) {
        MEMCLOB();
        g[0] = ld_poll(wsr, go);
        if (all_ok(bad4(g[0], tag)) || spin > SPIN_CAP) break;
      }
#pragma nounroll
      for (u32 spin = 0;; ++spin) {
        MEMCLOB();
#pragma unroll
        for (int m = 1; m < 6; ++m) g[m] = ld_poll_nt(wsr, go + m * 1024);
        g[6] = ld_poll_nt(wsr, go + (last ? 6144 : 1024));
        u32 bad = 0;
#pragma unroll
        for (int m = 1; m < 7; ++m) bad |= bad4(g[m], tag);
        if (all_ok(bad) || spin > SPIN_CAP) break;
      }
      f4 z = ((dec4(g[0]) + dec4(g[1])) + (dec4(g[2]) + dec4(g[3]))) + (dec4(g[4]) + dec4(g[5]));
      if (last) z += dec4(g[6]);
      const float o0 = hv.x + rb(sum8p(z.x)), o1 = hv.y + rb(sum8p(z.y)), o2 = hv.z + rb(sum8p(z.z)), o3 = hv.w + rb(sum8p(z.w));
      if (lane < 8) ((GAS v2u*)p.out)[sb * 160 + 8 * cs + q] = v2u{pack2(o0, o1), pack2(o2, o3)};
      if (task == 0 && lane == 0) __hip_atomic_store((GAS u64*)p.ws, epoch + 1, RLX_AGENT);
    }
    return;
  }

  const int w = wid;
#pragma unroll
  for (int n = W0; n < RD; ++n) { wait_vm<W0 - 1>(); ring[n] = ld_nt(kr, kvo, n * 8192); SB(); }
  u32 selA[4];
#pragma unroll
  for (int k = 0; k < 4; ++k) selA[k] = mksel(c & 7, k, c < 8);
  lds_wait<1>(flags + F_Q, 1);
  const v4u qv = *(LAS v4u*)(L + L_Q + c * 16);
#define REFILL(n) { _Pragma("unroll") for (int m_ = issued((n) - 1); m_ < issued(n); ++m_) { if (m_ < 32) ring[m_ % RD] = ld_nt(kr, kvo, m_ * 8192); else ring[m_ % RD] = ld_nt(vr, kvo, (m_ - 32) * 8192); } SB(); }
  float sc[2];
  float x[16];
#define QK_STEP(n) { const v4u kk = ring[(n) % RD]; x[(n) & 15] = dot2(kk.w, qv.w, dot2(kk.z, qv.z, dot2(kk.y, qv.y, dot2(kk.x, qv.x, 0.f)))); REFILL(n) }
#define QK4(n) QK_STEP(n) QK_STEP((n) + 1) QK_STEP((n) + 2) QK_STEP((n) + 3)
#define TR_STEP(HALF, CTRL, BIT) _Pragma("unroll") for (int hh = 0; hh < (HALF); ++hh) { \
    const float lo_ = x[hh], hi_ = x[hh + (HALF)]; const float t1_ = lo_ + dppf<CTRL>(lo_), t2_ = hi_ + dppf<CTRL>(hi_); x[hh] = (c & (BIT)) ? t2_ : t1_; }
#define TR_ALL() TR_STEP(8, XOR8, 8) TR_STEP(4, XOR7, 4) TR_STEP(2, XOR2, 2) TR_STEP(1, XOR1, 1)
  QK4(0) QK4(4) QK4(8) QK4(12)
  TR_ALL()
  sc[0] = rb(rb(x[0]) * 0.08838834764831845f);
  QK4(16) QK4(20) QK4(24) QK4(28)
  TR_ALL()
  sc[1] = rb(rb(x[0]) * 0.08838834764831845f);
  const float mw = wave_max(fmaxf(sc[0], sc[1]));
  const float pe0 = __builtin_amdgcn_exp2f((sc[0] - mw) * LOG2E), pe1 = __builtin_amdgcn_exp2f((sc[1] - mw) * LOG2E);
  { const float lw = wave_sum(pe0 + pe1); if (lane == 0) { mbuf[w] = mw; lbuf[w] = lw; } }
  lds_arrive(flags + F_KDONE, lane);
  u32 pb[2];
  pb[0] = pack2(pe0, 0.f); pb[1] = pack2(pe1, 0.f);
  f4 acc = {0.f, 0.f, 0.f, 0.f};
#define PV_STEP(i) { const v4u vv = ring[(32 + (i)) % RD]; \
    const u32 xx = (u32)__builtin_amdgcn_update_dpp(0, (int)pb[(i) >> 4], 0x150 + ((i) & 15), 0xf, 0xf, false);     \
    acc = mfma(sparse(xx, selA), vv, acc); REFILL(32 + (i)) \
    if ((i) == WTRIG && lane == 0) __hip_atomic_fetch_add(flags + F_WTRIG, 1u, RLX_WG); \
    if ((i) == RTRIG && lane == 0) __hip_atomic_fetch_add(flags + F_RTRIG, 1u, RLX_WG); }
#define PV4(i) PV_STEP(i) PV_STEP((i) + 1) PV_STEP((i) + 2) PV_STEP((i) + 3)
  PV4(0) PV4(4) PV4(8) PV4(12) PV4(16) PV4(20) PV4(24)
  MEMCLOB();
  const u32 iflag = __hip_atomic_load(flags + F_FAC, RLX_WG);
  MEMCLOB();
  float fw = *(volatile LAS float*)(L + L_FAC + w * 4);
  u32 stag = *(volatile LAS u32*)(L + L_INFO);
  PV4(28)
  if (__builtin_amdgcn_readfirstlane(iflag) == 0u) { lds_wait<0>(flags + F_FAC, 1); fw = *(volatile LAS float*)(L + L_FAC + w * 4); stag = *(volatile LAS u32*)(L + L_INFO); }
  acc *= fw;
  if (lane < 32) *(LAS f4*)(L + L_RED + w * 512 + (8 * c + 4 * r) * 4) = acc;
  asm volatile("s_waitcnt lgkmcnt(0)" ::: "memory");
  u32 old = 0;
  if (lane == 0) old = __hip_atomic_fetch_add(flags + F_PART, 1u, RLX_WG);
  if (__builtin_amdgcn_readfirstlane(old) == 7u) {
    MEMCLOB();
    if (lane < 32) {
      f4 sum = {0.f, 0.f, 0.f, 0.f};
#pragma unroll
      for (int ww = 0; ww < 8; ++ww) sum += *(LAS f4*)(L + L_RED + ww * 512 + lane * 16);
      st_sc1(wsr, OFF_X1 + (h * 8 + bs) * 512 + lane * 16, enc4(sum, stag));
    }
  }
  if (w < 4) {
    lds_wait<8>(flags + F_ROLE, 1);
    const int u = ffn_unit(__builtin_amdgcn_readfirstlane(*(LAS u32*)(L + L_INFO + 8)), grp);
    if (u < NFU) {
      const int q4 = c >> 2, p4 = c & 3, cb = c & 3, cr = c >> 2;
      const __amdgpu_buffer_rsrc_t w2r = mkrsrc(p.w2 + (size_t)32 * u * DM, 32 * DM * 2);
      const int vo2 = r * (DM * 2) + (c ^ (2 * (r + 4 * (w & 1)))) * 16;
#pragma unroll
      for (int i = 0; i < 10; ++i) dma_nt(w2r, L + L_W2 + (10 * w + i) * 1024, vo2, w * (8 * DM * 2) + (i / 5) * 4 * DM * 2 + (i % 5) * 256);
      wait_vm<0>();
      lds_arrive(flags + F_W2, lane);
      lds_wait<1>(flags + F_W2, 4);
      const int sw2 = 2 * (q4 + 4 * (r & 1));
      v4u bd[10];
#pragma unroll
      for (int T = 0; T < 10; ++T) { const int col = 160 * w + 16 * T + 4 * p4; bd[T] = trfrag(L + L_W2 + ((2 * r) * 5 + (col >> 7)) * 1024 + q4 * 256 + ((((col & 127) >> 3) ^ sw2) * 16) + (p4 & 1) * 8, 5 * 1024); }
      asm volatile("" :: "v"(bd[0]), "v"(bd[1]), "v"(bd[2]), "v"(bd[3]), "v"(bd[4]), "v"(bd[5]), "v"(bd[6]), "v"(bd[7]), "v"(bd[8]), "v"(bd[9]));
      __syncthreads();
      __syncthreads();
      const v4u a = *(LAS v4u*)(L + L_HH + cb * 64 + r * 16);
      const int zo = OFF_X4 + cb * X4_BLK + u * 128 + (cr & 1) * 64 + r * 16;
      const f4 zero = {0.f, 0.f, 0.f, 0.f};
      v4u am[4];
#pragma unroll
      for (int k = 0; k < 4; ++k) am[k] = cr == k ? a : v4u{0u, 0u, 0u, 0u};
      f4 z0 = mfma(bd[0], am[0], zero), z1 = mfma(bd[4], am[0], zero), z2 = mfma(bd[8], am[0], zero);
      z0 = mfma(bd[1], am[1], z0); z1 = mfma(bd[5], am[1], z1); z2 = mfma(bd[9], am[1], z2);
      z0 = mfma(bd[2], am[2], z0); z1 = mfma(bd[6], am[2], z1);
      z0 = mfma(bd[3], am[3], z0); z1 = mfma(bd[7], am[3], z1);
      if (cr < 2) st_sc1(wsr, zo + (5 * w + 4) * 4 * X4_BLK, enc4(z2, stag));
      st_sc1(wsr, zo + (5 * w + (cr >> 1)) * 4 * X4_BLK, enc4(z0, stag));
      st_sc1(wsr, zo + (5 * w + 2 + (cr >> 1)) * 4 * X4_BLK, enc4(z1, stag));
    }
  }
}

extern "C" void kernel_launch(void* const* d_in, const int* in_sizes, int n_in, void* d_out, int out_size, void* d_ws, size_t ws_size, hipStream_t stream) {
  Params p;
  p.x = (const u16*)d_in[0]; p.K = (const u16*)d_in[1]; p.V = (const u16*)d_in[2]; p.wq = (const u16*)d_in[3]; p.wo = (const u16*)d_in[4];
  p.w1 = (const u16*)d_in[6]; p.w3 = (const u16*)d_in[7]; p.w2 = (const u16*)d_in[8];
  p.out = (u16*)d_out; p.ws = (unsigned char*)d_ws;
  hipLaunchKernelGGL(fused_block, dim3(256), dim3(NTHR), 0, stream, p);
}
```
